```python
import torch

def _softmax(x, axis=-1):
    return torch.softmax(x, dim=axis)
def _gelu(x):
    return torch.nn.functional.gelu(x, approximate="tanh")
def _silu(x):
    return torch.nn.functional.silu(x)
def _sigmoid(x):
    return torch.sigmoid(x)
def _layernorm(x, axis=-1, eps=1e-5):
    m = x.mean(dim=axis, keepdim=True)
    v = x.var(dim=axis, keepdim=True, unbiased=False)
    return (x - m) / torch.sqrt(v + eps)
def _scatter_add(tgt, idx, src, axis=0):
    return tgt.clone().index_add_(axis, idx, src)
def _segment_sum(src, seg, n):
    out = torch.zeros((n,) + src.shape[1:], dtype=src.dtype)
    return out.index_add_(0, seg, src)
def _pad(x, pw):
    flat = tuple(v for pair in reversed(pw) for v in pair)
    return torch.nn.functional.pad(x, flat)

kernel_name = 'recurrent_scan__mha_block_cached__mixer_token_mlp__prompt'

def setup_inputs():

    _w = (lambda a: (lambda t: t.to(torch.bfloat16) if t.is_floating_point() else t)(torch.as_tensor(a)))
    def _A_setup():
        B, Sq, D = 1, 2048, 640
        H, Sk, Dh = 32, 2048, 128
        return {
            "x": _w((lambda *s: torch.randn(*s, dtype=torch.bfloat16) * 0.1)(B, Sq, D)),
            "k_cache": _w((lambda *s: torch.randn(*s, dtype=torch.bfloat16) * 0.8)(B, H, Sk, Dh)),
            "v_cache": _w((lambda *s: torch.randn(*s, dtype=torch.bfloat16) * 3.2)(B, H, Sk, Dh)),
            "wq": _w((lambda *s: torch.randn(*s, dtype=torch.bfloat16) * 0.8)(D, H * Dh) * (D ** -0.5)),
            "wo": _w((lambda *s: torch.randn(*s, dtype=torch.bfloat16) * 3.2)(H * Dh, D) * ((H * Dh) ** -0.5)),
        }
    _A_in = _A_setup()

    def _B_setup():
        B, S, D, F = 1, 2048, 640, 1728
        return {
            "x": _w((lambda *s: torch.randn(*s, dtype=torch.bfloat16) * 0.1)(B, S, D)),
            "w1": _w((lambda *s: torch.randn(*s, dtype=torch.bfloat16) * 3.2)(S, F) * (S ** -0.5)),
            "b1": _w((lambda *s: torch.randn(*s, dtype=torch.bfloat16) * 3.2)(F) * 0.02),
            "w2": _w((lambda *s: torch.randn(*s, dtype=torch.bfloat16) * 3.2)(F, S) * (F ** -0.5)),
            "b2": _w((lambda *s: torch.randn(*s, dtype=torch.bfloat16) * 3.2)(S) * 0.02),
        }
    _B_in = _B_setup()

    return {
        "A_x": _A_in["x"],
        "A_k_cache": _A_in["k_cache"],
        "A_v_cache": _A_in["v_cache"],
        "A_wq": _A_in["wq"],
        "A_wo": _A_in["wo"],
        "B_x": _B_in["x"],
        "B_w1": _B_in["w1"],
        "B_b1": _B_in["b1"],
        "B_w2": _B_in["w2"],
        "B_b2": _B_in["b2"],
    }

def reference(A_x, A_k_cache, A_v_cache, A_wq, A_wo, B_x, B_w1, B_b1, B_w2, B_b2):
    def _A_ref(x, k_cache, v_cache, wq, wo):
        B, Sq, D = x.shape
        H, Dh = 32, 128
        q = (lambda x, *d: x.permute(*d))((x @ wq).reshape(B, Sq, H, Dh), 0, 2, 1, 3)
        scores = torch.einsum("bhqd,bhkd->bhqk", q, k_cache) * (Dh ** -0.5)
        p = _softmax(scores, axis=-1)
        o = torch.einsum("bhqk,bhkd->bhqd", p, v_cache)
        return (lambda x, *d: x.permute(*d))(o, 0, 2, 1, 3).reshape(B, Sq, H * Dh) @ wo

    def _B_ref(x, w1, b1, w2, b2):
        B, S, D = x.shape
        h = _layernorm(x, axis=-1)
        u = torch.einsum("bsd,sf->bdf", h, w1) + b1
        u = _gelu(u)
        y = torch.einsum("bdf,fs->bds", u, w2) + b2
        return (lambda x, *d: x.permute(*d))(y, 0, 2, 1)

    x = A_x
    xn = _layernorm(x, axis=-1)
    s = _A_ref(xn, A_k_cache, A_v_cache, A_wq, A_wo)
    g = _silu(_B_ref(xn, B_w1, B_b1, B_w2, B_b2))
    return x + s * g
```

```cpp
#include <hip/hip_runtime.h>
#include <hip/hip_bf16.h>
#include <stdint.h>
typedef unsigned short u16;
__device__ __forceinline__ float bf2f(u16 x){ return __uint_as_float(((unsigned)x)<<16); }
__device__ __forceinline__ u16 f2bf(float f){ __hip_bfloat16 b = __float2bfloat16(f); return *(u16*)&b; }
__device__ __forceinline__ float rbf(float f){ return bf2f(f2bf(f)); }
constexpr int S=2048, D=640, H=32, DH=128, F=1728;

__global__ void ln_k(const u16* x, u16* xn, u16* h){
  int row = blockIdx.x*4 + (threadIdx.x>>6), l = threadIdx.x&63;
  float v[10]; float s=0;
  for(int i=0;i<10;i++){ v[i]=bf2f(x[row*D+l+64*i]); s+=v[i]; }
  for(int o=32;o;o>>=1) s+=__shfl_xor(s,o);
  float m=s/D; float q=0; for(int i=0;i<10;i++){ float d=v[i]-m; q+=d*d; }
  for(int o=32;o;o>>=1) q+=__shfl_xor(q,o);
  float r = 1.0f/sqrtf(q/D+1e-5f);
  s=0;
  for(int i=0;i<10;i++){ v[i]=rbf((v[i]-m)*r); xn[row*D+l+64*i]=f2bf(v[i]); s+=v[i]; }
  for(int o=32;o;o>>=1) s+=__shfl_xor(s,o);
  m=s/D; q=0; for(int i=0;i<10;i++){ float d=v[i]-m; q+=d*d; }
  for(int o=32;o;o>>=1) q+=__shfl_xor(q,o);
  r = 1.0f/sqrtf(q/D+1e-5f);
  for(int i=0;i<10;i++){ h[row*D+l+64*i]=f2bf((v[i]-m)*r); }
}
template<int EPI>
__global__ void gemm_k(const u16* A, int sam, int sak, const u16* B, int sbk, int sbn, int K, int N, void* Cv, const u16* bias, const u16* x, const float* sbuf){
  __shared__ float As[16][65], Bs[16][65];
  int bm=blockIdx.y*64, bn=blockIdx.x*64, t=threadIdx.x, tx=t&15, ty=t>>4;
  float acc[4][4]={};
  for(int k0=0;k0<K;k0+=16){
    for(int i=t;i<1024;i+=256){ int kk,mm; if(sak==1){kk=i&15;mm=i>>4;}else{mm=i&63;kk=i>>6;} As[kk][mm]=bf2f(A[(long)(bm+mm)*sam+(long)(k0+kk)*sak]); }
    for(int i=t;i<1024;i+=256){ int kk,nn; if(sbk==1){kk=i&15;nn=i>>4;}else{nn=i&63;kk=i>>6;} Bs[kk][nn]=bf2f(B[(long)(k0+kk)*sbk+(long)(bn+nn)*sbn]); }
    __syncthreads();
    for(int kk=0;kk<16;kk++){ float a[4],b[4]; for(int i=0;i<4;i++){a[i]=As[kk][ty*4+i]; b[i]=Bs[kk][tx*4+i];}
      for(int i=0;i<4;i++)for(int j=0;j<4;j++)acc[i][j]+=a[i]*b[j]; }
    __syncthreads();
  }
  for(int i=0;i<4;i++)for(int j=0;j<4;j++){ int m=bm+ty*4+i,n=bn+tx*4+j; float v=acc[i][j];
    if(EPI==0) ((u16*)Cv)[(long)m*N+n]=f2bf(v);
    if(EPI==1){ v+=bf2f(bias[n]); float g=0.5f*v*(1.f+tanhf(0.7978845608f*(v+0.044715f*v*v*v))); ((u16*)Cv)[(long)m*N+n]=f2bf(g);}
    if(EPI==2) ((float*)Cv)[(long)m*N+n]=v;
    if(EPI==3){ v+=bf2f(bias[m]); float g=v/(1.f+__expf(-v)); ((u16*)Cv)[(long)m*N+n]=f2bf(bf2f(x[(long)m*N+n])+sbuf[(long)m*N+n]*g);}
  }
}
__global__ void attn_k(const u16* q, const u16* k, const u16* v, u16* o){
  __shared__ float sc[16][S]; __shared__ float qs[16][DH];
  int h=blockIdx.y, q0=blockIdx.x*16, t=threadIdx.x;
  for(int i=t;i<16*DH;i+=256) qs[i>>7][i&127]=bf2f(q[(long)(q0+(i>>7))*(H*DH)+h*DH+(i&127)]);
  __syncthreads();
  for(int kk=t;kk<S;kk+=256){ float a[16]={}; const u16* kr=k+((long)h*S+kk)*DH;
    for(int d=0;d<DH;d++){ float kv=bf2f(kr[d]); for(int r=0;r<16;r++) a[r]+=qs[r][d]*kv; }
    for(int r=0;r<16;r++) sc[r][kk]=a[r]*0.08838834764f; }
  __syncthreads();
  { int w=t>>6,l=t&63; for(int r=w*4;r<w*4+4;r++){ float m=-1e30f; for(int i=l;i<S;i+=64) m=fmaxf(m,sc[r][i]); for(int oo=32;oo;oo>>=1) m=fmaxf(m,__shfl_xor(m,oo));
      float s=0; for(int i=l;i<S;i+=64){ float e=__expf(sc[r][i]-m); sc[r][i]=e; s+=e;} for(int oo=32;oo;oo>>=1) s+=__shfl_xor(s,oo);
      float inv=1.f/s; for(int i=l;i<S;i+=64) sc[r][i]*=inv; } }
  __syncthreads();
  { int d=t&127, rh=t>>7; float a[8]={}; for(int kk=0;kk<S;kk++){ float vv=bf2f(v[((long)h*S+kk)*DH+d]); for(int r=0;r<8;r++) a[r]+=sc[rh*8+r][kk]*vv; }
    for(int r=0;r<8;r++) o[(long)(q0+rh*8+r)*(H*DH)+h*DH+d]=f2bf(a[r]); }
}
extern "C" void kernel_launch(void* const* d_in, const int* in_sizes, int n_in, void* d_out, int out_size, void* d_ws, size_t ws_size, hipStream_t stream){
  const u16* x=(const u16*)d_in[0]; const u16* kc=(const u16*)d_in[1]; const u16* vc=(const u16*)d_in[2]; const u16* wq=(const u16*)d_in[3]; const u16* wo=(const u16*)d_in[4];
  const u16* w1=(const u16*)d_in[6]; const u16* b1=(const u16*)d_in[7]; const u16* w2=(const u16*)d_in[8]; const u16* b2=(const u16*)d_in[9];
  char* ws=(char*)d_ws; u16* xn=(u16*)ws; u16* h=(u16*)(ws+(4<<20)); u16* q=(u16*)(ws+(8<<20)); u16* o=(u16*)(ws+(32<<20)); u16* u=(u16*)(ws+(56<<20)); float* sb=(float*)(ws+(64<<20));
  ln_k<<<S/4,256,0,stream>>>(x,xn,h);
  gemm_k<0><<<dim3(H*DH/64,S/64),256,0,stream>>>(xn,D,1,wq,H*DH,1,D,H*DH,q,nullptr,nullptr,nullptr);
  attn_k<<<dim3(S/16,H),256,0,stream>>>(q,kc,vc,o);
  gemm_k<2><<<dim3(D/64,S/64),256,0,stream>>>(o,H*DH,1,wo,D,1,H*DH,D,sb,nullptr,nullptr,nullptr);
  gemm_k<1><<<dim3(F/64,D/64),256,0,stream>>>(h,1,D,w1,F,1,S,F,u,b1,nullptr,nullptr);
  gemm_k<3><<<dim3(D/64,S/64),256,0,stream>>>(w2,1,S,u,1,F,F,D,d_out,b2,x,sb);
}
```

```cpp
#include <hip/hip_runtime.h>

__global__ __launch_bounds__(256, 1) void fused_block_kernel(const void* x, const void* k, const void* v, const void* wq, const void* wo, const void* w1, const void* b1, const void* w2, const void* b2, void* out, void* ws) {
  __shared__ __attribute__((aligned(16))) char smem[163840];
  asm volatile(
"s_load_dwordx16 s[36:51], %0, 0\n"
"s_load_dwordx4 s[52:55], %0, 64\n"
"s_load_dwordx2 s[56:57], %0, 80\n"
"v_lshrrev_b32 v16, 6, v0\n"
"v_and_b32 v13, 63, v0\n"
"v_readfirstlane_b32 s90, v16\n"
"s_and_b32 s58, %1, 7\n"
"s_lshr_b32 s59, %1, 3\n"
"s_and_b32 s59, s59, 3\n"
"s_lshl_b32 s60, s58, 2\n"
"s_add_u32 s33, s60, s59\n"
"s_lshr_b32 s34, %1, 5\n"
"v_lshlrev_b32 v12, 4, v13\n"
"v_bfe_u32 v18, v13, 2, 3\n"
"v_lshrrev_b32 v19, 5, v13\n"
"v_and_b32 v20, 3, v13\n"
"v_and_b32 v22, 31, v13\n"
"v_mul_u32_u24 v23, 1280, v19\n"
"v_lshl_add_u32 v23, v22, 4, v23\n"
"v_mov_b32 v24, 0x7fff0000\n"
"v_cmp_gt_u32 vcc, 16, v22\n"
"s_nop 1\n"
"v_cndmask_b32 v24, v24, v23, vcc\n"
"s_lshr_b32 s61, s90, 1\n"
"s_and_b32 s62, s90, 1\n"
"s_lshl_b32 s58, s61, 13\n"
"s_lshl_b32 s59, s62, 10\n"
"s_add_u32 s29, s58, s59\n"
"s_lshl_b32 s59, s62, 7\n"
"s_lshl_b32 s60, s61, 18\n"
"s_add_u32 s60, s60, s59\n"
"v_lshlrev_b32 v21, 6, v19\n"
"v_lshl_add_u32 v21, v20, 4, v21\n"
"v_lshl_add_u32 v11, v18, 13, v21\n"
"v_add_u32 v11, s60, v11\n"
"s_lshl_b32 s60, %1, 2\n"
"s_add_u32 s60, s60, s90\n"
"s_mul_i32 s60, s60, 2560\n"
"s_waitcnt lgkmcnt(0)\n"
"s_add_u32 s24, s36, s60\n"
"s_addc_u32 s25, s37, 0\n"
"s_mov_b32 s26, 0xa00\n"
"s_mov_b32 s27, 0x20000\n"
"buffer_load_dwordx4 v[208:211], v23, s[24:27], 0 offen\n"
"buffer_load_dwordx4 v[212:215], v23, s[24:27], 0 offen offset:512\n"
"buffer_load_dwordx4 v[216:219], v24, s[24:27], 0 offen offset:1024\n"
"s_add_u32 s92, s56, 0x0\n"
"s_addc_u32 s93, s57, 0\n"
"s_mov_b32 s94, 0x30000\n"
"s_mov_b32 s95, 0x20000\n"
"buffer_load_dwordx2 v[56:57], off, s[92:95], 0 sc1\n"
"s_lshl_b32 s60, s33, 8\n"
"s_sub_u32 s61, 0x500000, s60\n"
"s_add_u32 s16, s42, s60\n"
"s_addc_u32 s17, s43, 0\n"
"s_mov_b32 s18, s61\n"
"s_mov_b32 s19, 0x20000\n"
"s_add_u32 m0, s29, 0x0\n"
"s_mov_b32 s31, 0x0\n"
"buffer_load_dwordx4 v11, s[16:19], s31 offen lds\n"
"s_add_u32 m0, s29, 0x800\n"
"s_mov_b32 s31, 0x10000\n"
"buffer_load_dwordx4 v11, s[16:19], s31 offen lds\n"
"s_add_u32 m0, s29, 0x1000\n"
"s_mov_b32 s31, 0x20000\n"
"buffer_load_dwordx4 v11, s[16:19], s31 offen lds\n"
"s_add_u32 m0, s29, 0x1800\n"
"s_mov_b32 s31, 0x30000\n"
"buffer_load_dwordx4 v11, s[16:19], s31 offen lds\n"
"v_and_b32 v17, 7, v13\n"
"v_lshlrev_b32 v17, 7, v17\n"
"s_getpc_b64 s[80:81]\n"
"L_PC%=:\n"
"s_mov_b32 s82, L_END%=-L_PC%=\n"
"s_mov_b32 s83, 0x20000\n"
"s_lshr_b32 s60, %1, 3\n"
"s_lshl_b32 s60, s60, 2\n"
"s_add_u32 s60, s60, s90\n"
"s_lshl_b32 s60, s60, 10\n"
"buffer_load_dword v15, v17, s[80:83], s60 offen\n"
"s_cmp_eq_u32 %1, 0\n"
"s_cselect_b32 s80, s48, s52\n"
"s_cselect_b32 s81, s49, s53\n"
"s_mov_b32 s82, 4096\n"
"s_cselect_b32 s82, 3456, s82\n"
"s_cmp_lt_u32 %1, 2\n"
"s_cselect_b32 s82, s82, 0\n"
"s_lshl_b32 s60, s90, 10\n"
"buffer_load_dword v15, v17, s[80:83], s60 offen\n"
"s_lshl_b32 s60, s34, 2\n"
"s_add_u32 s60, s60, s90\n"
"s_mul_i32 s60, s60, 147456\n"
"s_add_u32 s60, s60, 524288\n"
"v_min_u32 v17, 35, v13\n"
"v_and_b32 v16, 1, v17\n"
"v_lshrrev_b32 v17, 1, v17\n"
"v_lshlrev_b32 v17, 13, v17\n"
"v_lshl_add_u32 v17, v16, 7, v17\n"
"buffer_load_dword v15, v17, s[16:19], s60 offen\n"
"v_lshrrev_b32 v25, 4, v13\n"
"v_bfe_u32 v23, v13, 2, 2\n"
"s_lshl_b32 s60, s34, 2\n"
"s_add_u32 s60, s60, s90\n"
"s_lshl_b32 s60, s60, 9\n"
"s_add_u32 s60, s60, 4096\n"
"v_and_b32 v22, 7, v13\n"
"v_lshlrev_b32 v22, 6, v22\n"
"v_add_u32 v53, s60, v22\n"
"buffer_load_dword v15, v53, s[92:95], 0 offen sc1\n"
"s_lshl_b32 s60, s90, 1\n"
"v_add_u32 v22, s60, v19\n"
"v_and_b32 v26, 31, v13\n"
"v_and_b32 v24, 7, v13\n"
"v_xor_b32 v24, v22, v24\n"
"v_lshlrev_b32 v24, 4, v24\n"
"v_lshl_add_u32 v24, v26, 7, v24\n"
"v_add_u32 v64, 98304, v24\n"
"v_lshrrev_b32 v27, 3, v13\n"
"v_and_b32 v24, 7, v13\n"
"v_xor_b32 v28, v27, v24\n"
"v_lshlrev_b32 v28, 4, v28\n"
"v_lshl_add_u32 v28, v27, 7, v28\n"
"s_lshl_b32 s60, s90, 10\n"
"s_add_u32 s60, s60, 98304\n"
"v_add_u32 v65, s60, v28\n"
"v_lshlrev_b32 v28, 10, v19\n"
"v_and_b32 v27, 3, v27\n"
"v_lshl_add_u32 v28, v27, 8, v28\n"
"v_lshl_add_u32 v58, v24, 4, v28\n"
"s_lshl_b32 s31, s90, 11\n"
"s_add_u32 s35, s31, 0x2000\n"
"s_add_u32 s82, s31, 0x4000\n"
"v_mul_u32_u24 v22, 1344, v22\n"
"v_lshl_add_u32 v22, v26, 4, v22\n"
"v_add_u32 v59, 110592, v22\n"
"s_lshr_b32 s60, %1, 1\n"
"s_mul_i32 s60, s60, 20480\n"
"s_and_b32 s61, %1, 1\n"
"s_lshl_b32 s61, s61, 7\n"
"s_add_u32 s60, s60, s61\n"
"s_add_u32 s60, s60, 0x40000\n"
"s_add_u32 s24, s56, s60\n"
"s_addc_u32 s25, s57, 0\n"
"s_mov_b32 s26, 0x4f80\n"
"s_mov_b32 s27, 0x20000\n"
"s_lshl_b32 s60, %1, 6\n"
"s_add_u32 s60, s60, 4096\n"
"s_cmp_eq_u32 s90, 0\n"
"s_cselect_b32 s62, s60, 0x7fff0000\n"
"v_mul_u32_u24 v22, 1344, v23\n"
"v_lshl_add_u32 v22, v20, 3, v22\n"
"v_lshl_add_u32 v22, v25, 5, v22\n"
"s_lshl_b32 s60, s90, 7\n"
"s_add_u32 s60, s60, 110592\n"
"v_add_u32 v66, s60, v22\n"
"s_lshl_b32 s60, s90, 2\n"
"v_add_u32 v24, s60, v25\n"
"v_and_b32 v27, 15, v13\n"
"v_lshlrev_b32 v27, 4, v27\n"
"v_add_u32 v26, 0, v24\n"
"v_mul_u32_u24 v28, 52, v26\n"
"v_lshrrev_b32 v28, 8, v28\n"
"v_mul_u32_u24 v29, 5, v28\n"
"v_sub_u32 v29, v26, v29\n"
"v_lshl_add_u32 v29, v29, 10, v27\n"
"v_mul_u32_u24 v28, 327680, v28\n"
"v_add_u32 v60, v28, v29\n"
"v_add_u32 v26, 16, v24\n"
"v_mul_u32_u24 v28, 52, v26\n"
"v_lshrrev_b32 v28, 8, v28\n"
"v_mul_u32_u24 v29, 5, v28\n"
"v_sub_u32 v29, v26, v29\n"
"v_lshl_add_u32 v29, v29, 10, v27\n"
"v_mul_u32_u24 v28, 327680, v28\n"
"v_add_u32 v61, v28, v29\n"
"v_add_u32 v26, 32, v24\n"
"v_mul_u32_u24 v28, 52, v26\n"
"v_lshrrev_b32 v28, 8, v28\n"
"v_mul_u32_u24 v29, 5, v28\n"
"v_sub_u32 v29, v26, v29\n"
"v_lshl_add_u32 v29, v29, 10, v27\n"
"v_mul_u32_u24 v28, 327680, v28\n"
"v_add_u32 v62, v28, v29\n"
"s_waitcnt vmcnt(9) lgkmcnt(0)\n"
"v_lshlrev_b32 v160, 16, v208\n"
"v_and_b32 v161, 0xffff0000, v208\n"
"v_lshlrev_b32 v162, 16, v209\n"
"v_and_b32 v163, 0xffff0000, v209\n"
"v_lshlrev_b32 v164, 16, v210\n"
"v_and_b32 v165, 0xffff0000, v210\n"
"v_lshlrev_b32 v166, 16, v211\n"
"v_and_b32 v167, 0xffff0000, v211\n"
"v_lshlrev_b32 v168, 16, v212\n"
"v_and_b32 v169, 0xffff0000, v212\n"
"v_lshlrev_b32 v170, 16, v213\n"
"v_and_b32 v171, 0xffff0000, v213\n"
"v_lshlrev_b32 v172, 16, v214\n"
"v_and_b32 v173, 0xffff0000, v214\n"
"v_lshlrev_b32 v174, 16, v215\n"
"v_and_b32 v175, 0xffff0000, v215\n"
"v_lshlrev_b32 v176, 16, v216\n"
"v_and_b32 v177, 0xffff0000, v216\n"
"v_lshlrev_b32 v178, 16, v217\n"
"v_and_b32 v179, 0xffff0000, v217\n"
"v_lshlrev_b32 v180, 16, v218\n"
"v_and_b32 v181, 0xffff0000, v218\n"
"v_lshlrev_b32 v182, 16, v219\n"
"v_and_b32 v183, 0xffff0000, v219\n"
"v_pk_add_f32 v[26:27], v[160:161], v[162:163]\n"
"v_pk_add_f32 v[26:27], v[26:27], v[164:165]\n"
"v_pk_add_f32 v[26:27], v[26:27], v[166:167]\n"
"v_pk_add_f32 v[26:27], v[26:27], v[168:169]\n"
"v_pk_add_f32 v[26:27], v[26:27], v[170:171]\n"
"v_pk_add_f32 v[26:27], v[26:27], v[172:173]\n"
"v_pk_add_f32 v[26:27], v[26:27], v[174:175]\n"
"v_pk_add_f32 v[26:27], v[26:27], v[176:177]\n"
"v_pk_add_f32 v[26:27], v[26:27], v[178:179]\n"
"v_pk_add_f32 v[26:27], v[26:27], v[180:181]\n"
"v_pk_add_f32 v[26:27], v[26:27], v[182:183]\n"
"v_add_f32 v26, v26, v27\n"
"s_nop 1\n"
"v_add_f32_dpp v26, v26, v26 row_ror:8 row_mask:0xf bank_mask:0xf\n"
"s_nop 1\n"
"v_add_f32_dpp v26, v26, v26 row_ror:4 row_mask:0xf bank_mask:0xf\n"
"s_nop 1\n"
"v_add_f32_dpp v26, v26, v26 row_ror:2 row_mask:0xf bank_mask:0xf\n"
"s_nop 1\n"
"v_add_f32_dpp v26, v26, v26 row_ror:1 row_mask:0xf bank_mask:0xf\n"
"v_mov_b32 v27, v26\n"
"s_nop 1\n"
"v_permlane16_swap_b32 v26, v27\n"
"v_add_f32 v26, v26, v27\n"
"v_mul_f32 v26, 0xbacccccd, v26\n"
"v_mov_b32 v27, v26\n"
"v_pk_add_f32 v[160:161], v[160:161], v[26:27]\n"
"v_pk_add_f32 v[162:163], v[162:163], v[26:27]\n"
"v_pk_add_f32 v[164:165], v[164:165], v[26:27]\n"
"v_pk_add_f32 v[166:167], v[166:167], v[26:27]\n"
"v_pk_add_f32 v[168:169], v[168:169], v[26:27]\n"
"v_pk_add_f32 v[170:171], v[170:171], v[26:27]\n"
"v_pk_add_f32 v[172:173], v[172:173], v[26:27]\n"
"v_pk_add_f32 v[174:175], v[174:175], v[26:27]\n"
"s_mov_b32 exec_lo, 0xffff\n"
"s_mov_b32 exec_hi, 0xffff\n"
"v_pk_add_f32 v[176:177], v[176:177], v[26:27]\n"
"v_pk_add_f32 v[178:179], v[178:179], v[26:27]\n"
"v_pk_add_f32 v[180:181], v[180:181], v[26:27]\n"
"v_pk_add_f32 v[182:183], v[182:183], v[26:27]\n"
"s_mov_b64 exec, -1\n"
"v_pk_mul_f32 v[28:29], v[160:161], v[160:161]\n"
"v_pk_fma_f32 v[28:29], v[162:163], v[162:163], v[28:29]\n"
"v_pk_fma_f32 v[28:29], v[164:165], v[164:165], v[28:29]\n"
"v_pk_fma_f32 v[28:29], v[166:167], v[166:167], v[28:29]\n"
"v_pk_fma_f32 v[28:29], v[168:169], v[168:169], v[28:29]\n"
"v_pk_fma_f32 v[28:29], v[170:171], v[170:171], v[28:29]\n"
"v_pk_fma_f32 v[28:29], v[172:173], v[172:173], v[28:29]\n"
"v_pk_fma_f32 v[28:29], v[174:175], v[174:175], v[28:29]\n"
"v_pk_fma_f32 v[28:29], v[176:177], v[176:177], v[28:29]\n"
"v_pk_fma_f32 v[28:29], v[178:179], v[178:179], v[28:29]\n"
"v_pk_fma_f32 v[28:29], v[180:181], v[180:181], v[28:29]\n"
"v_pk_fma_f32 v[28:29], v[182:183], v[182:183], v[28:29]\n"
"v_add_f32 v28, v28, v29\n"
"s_nop 1\n"
"v_add_f32_dpp v28, v28, v28 row_ror:8 row_mask:0xf bank_mask:0xf\n"
"s_nop 1\n"
"v_add_f32_dpp v28, v28, v28 row_ror:4 row_mask:0xf bank_mask:0xf\n"
"s_nop 1\n"
"v_add_f32_dpp v28, v28, v28 row_ror:2 row_mask:0xf bank_mask:0xf\n"
"s_nop 1\n"
"v_add_f32_dpp v28, v28, v28 row_ror:1 row_mask:0xf bank_mask:0xf\n"
"v_mov_b32 v29, v28\n"
"s_nop 1\n"
"v_permlane16_swap_b32 v28, v29\n"
"v_add_f32 v28, v28, v29\n"
"v_mov_b32 v29, 0x3727c5ac\n"
"v_fmac_f32 v29, 0x3acccccd, v28\n"
"v_rsq_f32 v30, v29\n"
"s_nop 0\n"
"v_mov_b32 v31, v30\n"
"v_mul_f32 v28, 0x3e0293ee, v30\n"
"v_mov_b32 v29, v28\n"
"v_pk_mul_f32 v[184:185], v[160:161], v[28:29]\n"
"v_pk_mul_f32 v[186:187], v[162:163], v[28:29]\n"
"v_pk_mul_f32 v[188:189], v[164:165], v[28:29]\n"
"v_pk_mul_f32 v[190:191], v[166:167], v[28:29]\n"
"v_pk_mul_f32 v[192:193], v[168:169], v[28:29]\n"
"v_pk_mul_f32 v[194:195], v[170:171], v[28:29]\n"
"v_pk_mul_f32 v[196:197], v[172:173], v[28:29]\n"
"v_pk_mul_f32 v[198:199], v[174:175], v[28:29]\n"
"v_pk_mul_f32 v[200:201], v[176:177], v[28:29]\n"
"v_pk_mul_f32 v[202:203], v[178:179], v[28:29]\n"
"v_pk_mul_f32 v[204:205], v[180:181], v[28:29]\n"
"v_pk_mul_f32 v[206:207], v[182:183], v[28:29]\n"
"v_cvt_pk_bf16_f32 v208, v184, v185\n"
"v_cvt_pk_bf16_f32 v209, v186, v187\n"
"v_cvt_pk_bf16_f32 v210, v188, v189\n"
"v_cvt_pk_bf16_f32 v211, v190, v191\n"
"ds_write_b128 v64, v[208:211] offset:0\n"
"v_cvt_pk_bf16_f32 v212, v192, v193\n"
"v_cvt_pk_bf16_f32 v213, v194, v195\n"
"v_cvt_pk_bf16_f32 v214, v196, v197\n"
"v_cvt_pk_bf16_f32 v215, v198, v199\n"
"ds_write_b128 v64, v[212:215] offset:4096\n"
"v_cvt_pk_bf16_f32 v216, v200, v201\n"
"v_cvt_pk_bf16_f32 v217, v202, v203\n"
"v_cvt_pk_bf16_f32 v218, v204, v205\n"
"v_cvt_pk_bf16_f32 v219, v206, v207\n"
"ds_write_b128 v64, v[216:219] offset:8192\n"
"v_pk_mul_f32 v[160:161], v[160:161], v[30:31]\n"
"v_pk_mul_f32 v[162:163], v[162:163], v[30:31]\n"
"v_pk_mul_f32 v[164:165], v[164:165], v[30:31]\n"
"v_pk_mul_f32 v[166:167], v[166:167], v[30:31]\n"
"v_pk_mul_f32 v[168:169], v[168:169], v[30:31]\n"
"v_pk_mul_f32 v[170:171], v[170:171], v[30:31]\n"
"v_pk_mul_f32 v[172:173], v[172:173], v[30:31]\n"
"v_pk_mul_f32 v[174:175], v[174:175], v[30:31]\n"
"v_pk_mul_f32 v[176:177], v[176:177], v[30:31]\n"
"v_pk_mul_f32 v[178:179], v[178:179], v[30:31]\n"
"v_pk_mul_f32 v[180:181], v[180:181], v[30:31]\n"
"v_pk_mul_f32 v[182:183], v[182:183], v[30:31]\n"
"s_waitcnt lgkmcnt(0)\n"
"s_barrier\n"
"ds_read_b128 v[208:211], v65 offset:0\n"
"ds_read_b128 v[212:215], v65 offset:4096\n"
"ds_read_b128 v[216:219], v65 offset:8192\n"
"v_cvt_pk_bf16_f32 v184, v160, v161\n"
"v_cvt_pk_bf16_f32 v185, v162, v163\n"
"v_cvt_pk_bf16_f32 v186, v164, v165\n"
"v_cvt_pk_bf16_f32 v187, v166, v167\n"
"v_cvt_pk_bf16_f32 v188, v168, v169\n"
"v_cvt_pk_bf16_f32 v189, v170, v171\n"
"v_cvt_pk_bf16_f32 v190, v172, v173\n"
"v_cvt_pk_bf16_f32 v191, v174, v175\n"
"v_cvt_pk_bf16_f32 v192, v176, v177\n"
"v_cvt_pk_bf16_f32 v193, v178, v179\n"
"v_cvt_pk_bf16_f32 v194, v180, v181\n"
"v_cvt_pk_bf16_f32 v195, v182, v183\n"
"s_waitcnt lgkmcnt(0)\n"
"buffer_store_dwordx4 v[208:211], v58, s[24:27], s31 offen sc1\n"
"buffer_store_dwordx4 v[212:215], v58, s[24:27], s35 offen sc1\n"
"buffer_store_dwordx4 v[216:219], v58, s[24:27], s82 offen sc1\n"
"v_lshlrev_b32 v160, 16, v184\n"
"v_and_b32 v161, 0xffff0000, v184\n"
"v_lshlrev_b32 v162, 16, v185\n"
"v_and_b32 v163, 0xffff0000, v185\n"
"v_lshlrev_b32 v164, 16, v186\n"
"v_and_b32 v165, 0xffff0000, v186\n"
"v_lshlrev_b32 v166, 16, v187\n"
"v_and_b32 v167, 0xffff0000, v187\n"
"v_lshlrev_b32 v168, 16, v188\n"
"v_and_b32 v169, 0xffff0000, v188\n"
"v_lshlrev_b32 v170, 16, v189\n"
"v_and_b32 v171, 0xffff0000, v189\n"
"v_lshlrev_b32 v172, 16, v190\n"
"v_and_b32 v173, 0xffff0000, v190\n"
"v_lshlrev_b32 v174, 16, v191\n"
"v_and_b32 v175, 0xffff0000, v191\n"
"v_lshlrev_b32 v176, 16, v192\n"
"v_and_b32 v177, 0xffff0000, v192\n"
"v_lshlrev_b32 v178, 16, v193\n"
"v_and_b32 v179, 0xffff0000, v193\n"
"v_lshlrev_b32 v180, 16, v194\n"
"v_and_b32 v181, 0xffff0000, v194\n"
"v_lshlrev_b32 v182, 16, v195\n"
"v_and_b32 v183, 0xffff0000, v195\n"
"v_pk_add_f32 v[26:27], v[160:161], v[162:163]\n"
"v_pk_add_f32 v[26:27], v[26:27], v[164:165]\n"
"v_pk_add_f32 v[26:27], v[26:27], v[166:167]\n"
"v_pk_add_f32 v[26:27], v[26:27], v[168:169]\n"
"v_pk_add_f32 v[26:27], v[26:27], v[170:171]\n"
"v_pk_add_f32 v[26:27], v[26:27], v[172:173]\n"
"v_pk_add_f32 v[26:27], v[26:27], v[174:175]\n"
"v_pk_add_f32 v[26:27], v[26:27], v[176:177]\n"
"v_pk_add_f32 v[26:27], v[26:27], v[178:179]\n"
"v_pk_add_f32 v[26:27], v[26:27], v[180:181]\n"
"v_pk_add_f32 v[26:27], v[26:27], v[182:183]\n"
"v_add_f32 v26, v26, v27\n"
"s_nop 1\n"
"v_add_f32_dpp v26, v26, v26 row_ror:8 row_mask:0xf bank_mask:0xf\n"
"s_nop 1\n"
"v_add_f32_dpp v26, v26, v26 row_ror:4 row_mask:0xf bank_mask:0xf\n"
"s_nop 1\n"
"v_add_f32_dpp v26, v26, v26 row_ror:2 row_mask:0xf bank_mask:0xf\n"
"s_nop 1\n"
"v_add_f32_dpp v26, v26, v26 row_ror:1 row_mask:0xf bank_mask:0xf\n"
"v_mov_b32 v27, v26\n"
"s_nop 1\n"
"v_permlane16_swap_b32 v26, v27\n"
"v_add_f32 v26, v26, v27\n"
"v_mul_f32 v26, 0xbacccccd, v26\n"
"v_mov_b32 v27, v26\n"
"v_pk_add_f32 v[160:161], v[160:161], v[26:27]\n"
"v_pk_add_f32 v[162:163], v[162:163], v[26:27]\n"
"v_pk_add_f32 v[164:165], v[164:165], v[26:27]\n"
"v_pk_add_f32 v[166:167], v[166:167], v[26:27]\n"
"v_pk_add_f32 v[168:169], v[168:169], v[26:27]\n"
"v_pk_add_f32 v[170:171], v[170:171], v[26:27]\n"
"v_pk_add_f32 v[172:173], v[172:173], v[26:27]\n"
"v_pk_add_f32 v[174:175], v[174:175], v[26:27]\n"
"s_mov_b32 exec_lo, 0xffff\n"
"s_mov_b32 exec_hi, 0xffff\n"
"v_pk_add_f32 v[176:177], v[176:177], v[26:27]\n"
"v_pk_add_f32 v[178:179], v[178:179], v[26:27]\n"
"v_pk_add_f32 v[180:181], v[180:181], v[26:27]\n"
"v_pk_add_f32 v[182:183], v[182:183], v[26:27]\n"
"s_mov_b64 exec, -1\n"
"v_pk_mul_f32 v[28:29], v[160:161], v[160:161]\n"
"v_pk_fma_f32 v[28:29], v[162:163], v[162:163], v[28:29]\n"
"v_pk_fma_f32 v[28:29], v[164:165], v[164:165], v[28:29]\n"
"v_pk_fma_f32 v[28:29], v[166:167], v[166:167], v[28:29]\n"
"v_pk_fma_f32 v[28:29], v[168:169], v[168:169], v[28:29]\n"
"v_pk_fma_f32 v[28:29], v[170:171], v[170:171], v[28:29]\n"
"v_pk_fma_f32 v[28:29], v[172:173], v[172:173], v[28:29]\n"
"v_pk_fma_f32 v[28:29], v[174:175], v[174:175], v[28:29]\n"
"v_pk_fma_f32 v[28:29], v[176:177], v[176:177], v[28:29]\n"
"v_pk_fma_f32 v[28:29], v[178:179], v[178:179], v[28:29]\n"
"v_pk_fma_f32 v[28:29], v[180:181], v[180:181], v[28:29]\n"
"v_pk_fma_f32 v[28:29], v[182:183], v[182:183], v[28:29]\n"
"v_add_f32 v28, v28, v29\n"
"s_nop 1\n"
"v_add_f32_dpp v28, v28, v28 row_ror:8 row_mask:0xf bank_mask:0xf\n"
"s_nop 1\n"
"v_add_f32_dpp v28, v28, v28 row_ror:4 row_mask:0xf bank_mask:0xf\n"
"s_nop 1\n"
"v_add_f32_dpp v28, v28, v28 row_ror:2 row_mask:0xf bank_mask:0xf\n"
"s_nop 1\n"
"v_add_f32_dpp v28, v28, v28 row_ror:1 row_mask:0xf bank_mask:0xf\n"
"v_mov_b32 v29, v28\n"
"s_nop 1\n"
"v_permlane16_swap_b32 v28, v29\n"
"v_add_f32 v28, v28, v29\n"
"v_mov_b32 v29, 0x3727c5ac\n"
"v_fmac_f32 v29, 0x3acccccd, v28\n"
"v_rsq_f32 v30, v29\n"
"s_nop 0\n"
"v_mov_b32 v31, v30\n"
"s_waitcnt vmcnt(11)\n"
"v_add_co_u32 v56, vcc, 1, v56\n"
"s_nop 1\n"
"v_addc_co_u32 v57, vcc, 0, v57, vcc\n"
"v_readfirstlane_b32 s88, v56\n"
"v_readfirstlane_b32 s89, v57\n"
"s_waitcnt vmcnt(0)\n"
"s_barrier\n"
"v_lshrrev_b32 v54, 1, v12\n"
"s_mov_b64 exec, 0xff\n"
"buffer_store_dwordx2 v[56:57], v54, s[92:95], s62 offen sc1\n"
"s_mov_b64 exec, -1\n"
"v_pk_mul_f32 v[160:161], v[160:161], v[30:31]\n"
"v_pk_mul_f32 v[162:163], v[162:163], v[30:31]\n"
"v_pk_mul_f32 v[164:165], v[164:165], v[30:31]\n"
"v_pk_mul_f32 v[166:167], v[166:167], v[30:31]\n"
"v_pk_mul_f32 v[168:169], v[168:169], v[30:31]\n"
"v_pk_mul_f32 v[170:171], v[170:171], v[30:31]\n"
"v_pk_mul_f32 v[172:173], v[172:173], v[30:31]\n"
"v_pk_mul_f32 v[174:175], v[174:175], v[30:31]\n"
"v_pk_mul_f32 v[176:177], v[176:177], v[30:31]\n"
"v_pk_mul_f32 v[178:179], v[178:179], v[30:31]\n"
"v_pk_mul_f32 v[180:181], v[180:181], v[30:31]\n"
"v_pk_mul_f32 v[182:183], v[182:183], v[30:31]\n"
"v_cvt_pk_bf16_f32 v184, v160, v161\n"
"v_cvt_pk_bf16_f32 v185, v162, v163\n"
"v_cvt_pk_bf16_f32 v186, v164, v165\n"
"v_cvt_pk_bf16_f32 v187, v166, v167\n"
"v_cvt_pk_bf16_f32 v188, v168, v169\n"
"v_cvt_pk_bf16_f32 v189, v170, v171\n"
"v_cvt_pk_bf16_f32 v190, v172, v173\n"
"v_cvt_pk_bf16_f32 v191, v174, v175\n"
"v_cvt_pk_bf16_f32 v192, v176, v177\n"
"v_cvt_pk_bf16_f32 v193, v178, v179\n"
"v_cvt_pk_bf16_f32 v194, v180, v181\n"
"v_cvt_pk_bf16_f32 v195, v182, v183\n"
"ds_write_b128 v59, v[184:187]\n"
"ds_write_b128 v59, v[188:191] offset:512\n"
"s_mov_b32 exec_lo, 0xffff\n"
"s_mov_b32 exec_hi, 0xffff\n"
"ds_write_b128 v59, v[192:195] offset:1024\n"
"s_mov_b64 exec, -1\n"
"s_lshr_b32 s60, %1, 2\n"
"s_mul_i32 s60, s60, 5120\n"
"s_and_b32 s61, %1, 3\n"
"s_lshl_b32 s61, s61, 8\n"
"s_add_u32 s60, s60, s61\n"
"s_sub_u32 s61, 2621440, s60\n"
"s_add_u32 s60, s60, 0x2c0000\n"
"s_add_u32 s24, s56, s60\n"
"s_addc_u32 s25, s57, 0\n"
"s_mov_b32 s26, s61\n"
"s_mov_b32 s27, 0x20000\n"
"s_waitcnt lgkmcnt(0)\n"
"s_barrier\n"
"ds_read_b64_tr_b16 v[160:161], v66 offset:0\n"
"ds_read_b64_tr_b16 v[162:163], v66 offset:5376\n"
"ds_read_b64_tr_b16 v[164:165], v66 offset:512\n"
"ds_read_b64_tr_b16 v[166:167], v66 offset:5888\n"
"ds_read_b64_tr_b16 v[168:169], v66 offset:1024\n"
"ds_read_b64_tr_b16 v[170:171], v66 offset:6400\n"
"s_waitcnt lgkmcnt(0)\n"
"buffer_load_dwordx2 v[54:55], v53, s[92:95], 0 offen sc1\n"
"buffer_store_dwordx4 v[160:163], v60, s[24:27], 0 offen sc1\n"
"buffer_store_dwordx4 v[164:167], v61, s[24:27], 0 offen sc1\n"
"buffer_store_dwordx4 v[168:171], v62, s[24:27], 0 offen sc1\n"
"s_lshr_b32 s61, s90, 1\n"
"s_and_b32 s62, s90, 1\n"
"s_lshl_b32 s58, s61, 13\n"
"s_lshl_b32 s59, s62, 7\n"
"s_add_u32 s60, s58, s59\n"
"v_lshl_add_u32 v22, v18, 8, s60\n"
"v_lshl_add_u32 v22, v19, 6, v22\n"
"v_lshrrev_b32 v23, 2, v18\n"
"v_lshlrev_b32 v24, 1, v23\n"
"v_xor_b32 v24, v20, v24\n"
"v_lshl_add_u32 v9, v24, 4, v22\n"
"v_add_u32 v10, 0x1000, v9\n"
"v_sub_u32 v24, 0, v23\n"
"v_and_b32 v24, 3, v24\n"
"v_xor_b32 v24, v20, v24\n"
"v_lshl_add_u32 v5, v24, 4, v22\n"
"v_xor_b32 v24, 2, v24\n"
"v_lshl_add_u32 v6, v24, 4, v22\n"
"v_add_u32 v7, 0x1000, v5\n"
"v_add_u32 v8, 0x1000, v6\n"
"v_lshrrev_b32 v25, 4, v13\n"
"v_bfe_u32 v22, v13, 3, 1\n"
"v_and_b32 v23, 7, v13\n"
"v_lshlrev_b32 v22, 11, v22\n"
"v_lshl_add_u32 v22, v23, 6, v22\n"
"v_bfe_u32 v23, v13, 2, 2\n"
"v_sub_u32 v24, 0, v23\n"
"v_and_b32 v24, 3, v24\n"
"v_xor_b32 v24, v25, v24\n"
"v_lshl_add_u32 v1, v24, 4, v22\n"
"v_and_b32 v24, 1, v25\n"
"v_lshlrev_b32 v22, 11, v19\n"
"v_lshl_add_u32 v22, v24, 8, v22\n"
"v_lshl_add_u32 v22, v23, 6, v22\n"
"v_lshl_add_u32 v22, v20, 3, v22\n"
"v_add_u32 v22, 0xc000, v22\n"
"v_lshl_add_u32 v2, v24, 5, v22\n"
"v_xor_b32 v24, 1, v24\n"
"v_lshl_add_u32 v3, v24, 5, v22\n"
"v_lshlrev_b32 v22, 11, v25\n"
"v_lshl_add_u32 v22, v23, 6, v22\n"
"v_lshl_add_u32 v4, v20, 4, v22\n"
"s_lshl_b32 s60, s33, 19\n"
"s_add_u32 s8, s38, s60\n"
"s_addc_u32 s9, s39, 0\n"
"s_mov_b32 s10, 0x80000\n"
"s_mov_b32 s11, 0x20000\n"
"s_add_u32 s12, s40, s60\n"
"s_addc_u32 s13, s41, 0\n"
"s_mov_b32 s14, 0x80000\n"
"s_mov_b32 s15, 0x20000\n"
"s_lshl_b32 s60, s34, 2\n"
"s_add_u32 s60, s60, s90\n"
"s_mul_i32 s60, s60, 81920\n"
"s_add_u32 s60, s60, 0x40000\n"
"s_add_u32 s20, s56, s60\n"
"s_addc_u32 s21, s57, 0\n"
"s_mov_b32 s22, 0x14000\n"
"s_mov_b32 s23, 0x20000\n"
"s_mov_b32 s30, 0\n"
"s_mov_b32 s28, 0\n"
"v_mov_b32 v32, 0\n"
"v_mov_b32 v33, 0\n"
"v_mov_b32 v34, 0\n"
"v_mov_b32 v35, 0\n"
"s_and_b32 s58, %1, 7\n"
"s_lshr_b32 s59, %1, 3\n"
"s_and_b32 s60, s58, 1\n"
"s_lshl_b32 s60, s60, 2\n"
"s_and_b32 s61, s59, 3\n"
"s_add_u32 s96, s60, s61\n"
"s_lshr_b32 s61, s58, 1\n"
"s_mul_i32 s62, s61, 7\n"
"s_lshr_b32 s97, s59, 2\n"
"s_cmp_lt_u32 s97, 7\n"
"s_cselect_b32 s62, s62, 100\n"
"s_add_u32 s97, s97, s62\n"
"s_lshl_b32 s60, s60, 1\n"
"s_and_b32 s62, s59, 7\n"
"s_add_u32 s60, s60, s62\n"
"s_lshl_b32 s60, s60, 1\n"
"s_lshr_b32 s62, s61, 1\n"
"s_add_u32 s98, s60, s62\n"
"s_lshr_b32 s62, s59, 3\n"
"s_lshl_b32 s62, s62, 1\n"
"s_and_b32 s61, s61, 1\n"
"s_add_u32 s99, s62, s61\n"
"s_mul_i32 s63, s90, 5120\n"
"v_lshrrev_b32 v36, 3, v13\n"
"v_bfe_u32 v37, v13, 1, 2\n"
"v_bfe_u32 v38, v13, 4, 1\n"
"v_xor_b32 v37, v37, v38\n"
"v_mul_u32_u24 v36, 3456, v36\n"
"v_and_b32 v38, 1, v13\n"
"v_lshl_add_u32 v36, v38, 4, v36\n"
"v_lshl_add_u32 v44, v37, 5, v36\n"
"v_xor_b32 v37, 2, v37\n"
"v_lshl_add_u32 v38, v37, 5, v36\n"
"v_add_u32 v45, 26624, v38\n"
"v_add_u32 v46, 53248, v44\n"
"v_add_u32 v47, 79872, v38\n"
"s_lshl_b32 s71, s90, 12\n"
"s_add_u32 s60, s71, 118784\n"
"v_lshrrev_b32 v36, 4, v13\n"
"v_lshlrev_b32 v36, 10, v36\n"
"v_bfe_u32 v37, v13, 2, 2\n"
"v_lshl_add_u32 v36, v37, 7, v36\n"
"v_and_b32 v37, 3, v13\n"
"v_lshl_add_u32 v36, v37, 3, v36\n"
"v_add_u32 v36, s60, v36\n"
"v_bfe_u32 v37, v13, 3, 2\n"
"s_add_u32 s60, s90, 0\n"
"s_and_b32 s60, s60, 3\n"
"v_xor_b32 v38, s60, v37\n"
"v_lshl_add_u32 v48, v38, 5, v36\n"
"s_add_u32 s60, s90, 1\n"
"s_and_b32 s60, s60, 3\n"
"v_xor_b32 v38, s60, v37\n"
"v_lshl_add_u32 v49, v38, 5, v36\n"
"s_add_u32 s60, s90, 2\n"
"s_and_b32 s60, s60, 3\n"
"v_xor_b32 v38, s60, v37\n"
"v_lshl_add_u32 v50, v38, 5, v36\n"
"s_add_u32 s60, s90, 3\n"
"s_and_b32 s60, s60, 3\n"
"v_xor_b32 v38, s60, v37\n"
"v_lshl_add_u32 v51, v38, 5, v36\n"
"v_add_u32 v52, 0x1000, v12\n"
"s_lshl_b32 s58, %1, 2\n"
"s_add_u32 s58, s58, s90\n"
"s_lshl_b32 s68, s58, 9\n"
"s_mov_b32 s69, 0x80000\n"
"s_add_u32 s64, s44, 0x0\n"
"s_addc_u32 s65, s45, 0\n"
"s_mov_b32 s66, 0x500000\n"
"s_mov_b32 s67, 0x20000\n"
"v_and_b32 v63, 3, v13\n"
"v_lshlrev_b32 v63, 7, v63\n"
"s_lshl_b32 s60, s34, 2\n"
"s_add_u32 s60, s60, s90\n"
"s_lshl_b32 s60, s60, 19\n"
"s_lshl_b32 s61, s33, 14\n"
"s_add_u32 s60, s60, s61\n"
"s_add_u32 s60, s60, 0x800000\n"
"s_add_u32 s24, s56, s60\n"
"s_addc_u32 s25, s57, 0\n"
"s_mov_b32 s26, 0x4000\n"
"s_mov_b32 s27, 0x20000\n"
"s_and_b32 s60, %1, 1\n"
"s_lshl_b32 s60, s60, 2\n"
"s_add_u32 s58, s60, s90\n"
"s_lshr_b32 s59, %1, 1\n"
"s_mul_i32 s60, s58, 160\n"
"s_mul_i32 s61, s59, 40960\n"
"s_add_u32 s60, s60, s61\n"
"s_sub_u32 s61, 5242880, s60\n"
"s_add_u32 s72, s44, s60\n"
"s_addc_u32 s73, s45, 0\n"
"s_mov_b32 s74, s61\n"
"s_mov_b32 s75, 0x20000\n"
"v_add_u32 v36, 0, v13\n"
"v_mul_u32_u24 v37, 6554, v36\n"
"v_lshrrev_b32 v37, 16, v37\n"
"v_mul_u32_u24 v38, 10, v37\n"
"v_sub_u32 v38, v36, v38\n"
"v_mul_u32_u24 v37, 1280, v37\n"
"v_lshl_add_u32 v37, v38, 4, v37\n"
"v_subrev_u32 v58, 0, v37\n"
"v_add_u32 v36, 64, v13\n"
"v_mul_u32_u24 v37, 6554, v36\n"
"v_lshrrev_b32 v37, 16, v37\n"
"v_mul_u32_u24 v38, 10, v37\n"
"v_sub_u32 v38, v36, v38\n"
"v_mul_u32_u24 v37, 1280, v37\n"
"v_lshl_add_u32 v37, v38, 4, v37\n"
"v_subrev_u32 v59, 1024, v37\n"
"v_add_u32 v36, 128, v13\n"
"v_mul_u32_u24 v37, 6554, v36\n"
"v_lshrrev_b32 v37, 16, v37\n"
"v_mul_u32_u24 v38, 10, v37\n"
"v_sub_u32 v38, v36, v38\n"
"v_mul_u32_u24 v37, 1280, v37\n"
"v_lshl_add_u32 v37, v38, 4, v37\n"
"v_subrev_u32 v60, 2048, v37\n"
"v_add_u32 v36, 192, v13\n"
"v_mul_u32_u24 v37, 6554, v36\n"
"v_lshrrev_b32 v37, 16, v37\n"
"v_mul_u32_u24 v38, 10, v37\n"
"v_sub_u32 v38, v36, v38\n"
"v_mul_u32_u24 v37, 1280, v37\n"
"v_lshl_add_u32 v37, v38, 4, v37\n"
"v_subrev_u32 v61, 3072, v37\n"
"v_add_u32 v36, 256, v13\n"
"v_mul_u32_u24 v37, 6554, v36\n"
"v_lshrrev_b32 v37, 16, v37\n"
"v_mul_u32_u24 v38, 10, v37\n"
"v_sub_u32 v38, v36, v38\n"
"v_mul_u32_u24 v37, 1280, v37\n"
"v_lshl_add_u32 v37, v38, 4, v37\n"
"v_subrev_u32 v62, 0, v37\n"
"v_lshrrev_b32 v36, 2, v13\n"
"v_mul_u32_u24 v36, 160, v36\n"
"v_lshl_add_u32 v36, v20, 3, v36\n"
"v_add_u32 v36, s63, v36\n"
"v_add_u32 v0, 98304, v36\n"
"s_mul_i32 s60, s58, 655360\n"
"s_mul_i32 s61, s59, 5120\n"
"s_add_u32 s60, s60, s61\n"
"s_add_u32 s60, s60, 0x1900000\n"
"s_add_u32 s84, s56, s60\n"
"s_addc_u32 s85, s57, 0\n"
"s_mov_b32 s86, 0x1400\n"
"s_mov_b32 s87, 0x20000\n"
"s_lshl_b32 s58, s58, 10\n"
"s_lshl_b32 s59, s59, 3\n"
"s_add_u32 s58, s58, s59\n"
"s_add_u32 s70, s58, 0x8000\n"
"s_waitcnt vmcnt(3)\n"
"v_cmp_eq_u64 vcc, s[88:89], v[54:55]\n"
"s_mov_b32 s91, 0\n"
"s_nop 2\n"
"s_cmp_eq_u64 vcc, -1\n"
"s_cbranch_scc1 L_OK1%=\n"
"L_SP1%=:\n"
"s_sleep 2\n"
"buffer_load_dwordx2 v[54:55], v53, s[92:95], 0 offen sc1\n"
"s_waitcnt vmcnt(0)\n"
"v_cmp_eq_u64 vcc, s[88:89], v[54:55]\n"
"s_add_u32 s91, s91, 1\n"
"s_nop 3\n"
"s_cmp_eq_u64 vcc, -1\n"
"s_cbranch_scc1 L_OK1%=\n"
"s_cmp_lt_u32 s91, 0x40000\n"
"s_cbranch_scc1 L_SP1%=\n"
"L_OK1%=:\n"
"s_lshl_b32 s60, s90, 9\n"
"s_add_u32 s60, s60, 20480\n"
"v_lshlrev_b32 v22, 3, v13\n"
"v_add_u32 v53, s60, v22\n"
"s_mov_b32 s35, 0x0\n"
"buffer_load_dwordx4 a[128:131], v12, s[20:23], s35 offen offset:0 sc1\n"
"buffer_load_dwordx4 a[132:135], v12, s[20:23], s35 offen offset:1024 sc1\n"
"s_mov_b32 s35, 0x5000\n"
"buffer_load_dwordx4 a[136:139], v12, s[20:23], s35 offen offset:0 sc1\n"
"buffer_load_dwordx4 a[140:143], v12, s[20:23], s35 offen offset:1024 sc1\n"
"s_mov_b32 s35, 0xa000\n"
"buffer_load_dwordx4 a[144:147], v12, s[20:23], s35 offen offset:0 sc1\n"
"buffer_load_dwordx4 a[148:151], v12, s[20:23], s35 offen offset:1024 sc1\n"
"s_mov_b32 s35, 0xf000\n"
"buffer_load_dwordx4 a[152:155], v12, s[20:23], s35 offen offset:0 sc1\n"
"buffer_load_dwordx4 a[156:159], v12, s[20:23], s35 offen offset:1024 sc1\n"
"s_barrier\n"
"s_lshl_b32 s60, %1, 3\n"
"s_add_u32 s60, s60, 20480\n"
"s_cmp_eq_u32 s90, 0\n"
"s_cselect_b32 s62, s60, 0x7fff0000\n"
"ds_read_b64_tr_b16 v[16:17], v4 offset:0\n"
"ds_read_b64_tr_b16 v[18:19], v4 offset:256\n"
"ds_read_b64_tr_b16 v[20:21], v4 offset:8192\n"
"ds_read_b64_tr_b16 v[22:23], v4 offset:8448\n"
"s_mov_b32 s78, s29\n"
"s_add_u32 s79, s29, 0x4000\n"
"s_add_u32 s80, s29, 0x8000\n"
"s_add_u32 m0, s29, 0x4000\n"
"s_mov_b32 s31, 0x80000\n"
"buffer_load_dwordx4 v11, s[16:19], s31 offen lds\n"
"s_add_u32 m0, s29, 0x4800\n"
"s_mov_b32 s31, 0x90000\n"
"buffer_load_dwordx4 v11, s[16:19], s31 offen lds\n"
"s_add_u32 m0, s29, 0x5000\n"
"s_mov_b32 s31, 0xa0000\n"
"buffer_load_dwordx4 v11, s[16:19], s31 offen lds\n"
"s_add_u32 m0, s29, 0x5800\n"
"s_mov_b32 s31, 0xb0000\n"
"buffer_load_dwordx4 v11, s[16:19], s31 offen lds\n"
"s_waitcnt vmcnt(4) lgkmcnt(0)\n"
"v_mfma_f32_16x16x32_bf16 v[128:131], v[16:19], a[128:131], 0\n"
"s_mov_b32 s35, 0x800\n"
"buffer_load_dwordx4 a[160:163], v12, s[20:23], s35 offen offset:0 sc1\n"
"v_mfma_f32_16x16x32_bf16 v[128:131], v[20:23], a[132:135], v[128:131]\n"
"ds_read_b64_tr_b16 v[24:25], v4 offset:8\n"
"v_accvgpr_write_b32 a0, 0\n"
"v_mfma_f32_16x16x32_bf16 v[132:135], v[20:23], a[140:143], 0\n"
"ds_read_b64_tr_b16 v[26:27], v4 offset:264\n"
"v_mfma_f32_16x16x32_bf16 v[132:135], v[16:19], a[136:139], v[132:135]\n"
"ds_read_b64_tr_b16 v[28:29], v4 offset:8200\n"
"v_accvgpr_write_b32 a1, 0\n"
"v_mfma_f32_16x16x32_bf16 v[136:139], v[16:19], a[144:147], 0\n"
"ds_read_b64_tr_b16 v[30:31], v4 offset:8456\n"
"v_mfma_f32_16x16x32_bf16 v[136:139], v[20:23], a[148:151], v[136:139]\n"
"buffer_load_dwordx4 a[164:167], v12, s[20:23], s35 offen offset:1024 sc1\n"
"v_mfma_f32_16x16x32_bf16 v[140:143], v[20:23], a[156:159], 0\n"
"s_mov_b32 s35, 0x5800\n"
"buffer_load_dwordx4 a[168:171], v12, s[20:23], s35 offen offset:0 sc1\n"
"v_mfma_f32_16x16x32_bf16 v[140:143], v[16:19], a[152:155], v[140:143]\n"
"s_waitcnt lgkmcnt(0)\n"
"v_mfma_f32_16x16x32_bf16 v[144:147], v[24:27], a[128:131], 0\n"
"buffer_load_dwordx4 a[172:175], v12, s[20:23], s35 offen offset:1024 sc1\n"
"v_mfma_f32_16x16x32_bf16 v[144:147], v[28:31], a[132:135], v[144:147]\n"
"ds_read_b64_tr_b16 v[16:17], v4 offset:512\n"
"v_accvgpr_write_b32 a2, 0\n"
"v_mfma_f32_16x16x32_bf16 v[148:151], v[28:31], a[140:143], 0\n"
"ds_read_b64_tr_b16 v[18:19], v4 offset:768\n"
"v_mfma_f32_16x16x32_bf16 v[148:151], v[24:27], a[136:139], v[148:151]\n"
"ds_read_b64_tr_b16 v[20:21], v4 offset:8704\n"
"v_accvgpr_write_b32 a3, 0\n"
"v_mfma_f32_16x16x32_bf16 v[152:155], v[24:27], a[144:147], 0\n"
"ds_read_b64_tr_b16 v[22:23], v4 offset:8960\n"
"v_mfma_f32_16x16x32_bf16 v[152:155], v[28:31], a[148:151], v[152:155]\n"
"s_mov_b32 s35, 0xa800\n"
"buffer_load_dwordx4 a[176:179], v12, s[20:23], s35 offen offset:0 sc1\n"
"v_mfma_f32_16x16x32_bf16 v[156:159], v[28:31], a[156:159], 0\n"
"buffer_load_dwordx4 a[180:183], v12, s[20:23], s35 offen offset:1024 sc1\n"
"v_mfma_f32_16x16x32_bf16 v[156:159], v[24:27], a[152:155], v[156:159]\n"
"s_waitcnt lgkmcnt(0)\n"
"v_mfma_f32_16x16x32_bf16 v[160:163], v[16:19], a[128:131], 0\n"
"s_mov_b32 s35, 0xf800\n"
"buffer_load_dwordx4 a[184:187], v12, s[20:23], s35 offen offset:0 sc1\n"
"v_mfma_f32_16x16x32_bf16 v[160:163], v[20:23], a[132:135], v[160:163]\n"
"ds_read_b64_tr_b16 v[24:25], v4 offset:520\n"
"v_accvgpr_write_b32 a4, 0\n"
"v_mfma_f32_16x16x32_bf16 v[164:167], v[20:23], a[140:143], 0\n"
"ds_read_b64_tr_b16 v[26:27], v4 offset:776\n"
"v_mfma_f32_16x16x32_bf16 v[164:167], v[16:19], a[136:139], v[164:167]\n"
"ds_read_b64_tr_b16 v[28:29], v4 offset:8712\n"
"v_accvgpr_write_b32 a5, 0\n"
"v_mfma_f32_16x16x32_bf16 v[168:171], v[16:19], a[144:147], 0\n"
"ds_read_b64_tr_b16 v[30:31], v4 offset:8968\n"
"v_mfma_f32_16x16x32_bf16 v[168:171], v[20:23], a[148:151], v[168:171]\n"
"buffer_load_dwordx4 a[188:191], v12, s[20:23], s35 offen offset:1024 sc1\n"
"v_mfma_f32_16x16x32_bf16 v[172:175], v[20:23], a[156:159], 0\n"
"v_mfma_f32_16x16x32_bf16 v[172:175], v[16:19], a[152:155], v[172:175]\n"
"s_waitcnt lgkmcnt(0)\n"
"v_mfma_f32_16x16x32_bf16 v[176:179], v[24:27], a[128:131], 0\n"
"v_mfma_f32_16x16x32_bf16 v[176:179], v[28:31], a[132:135], v[176:179]\n"
"ds_read_b64_tr_b16 v[16:17], v4 offset:1024\n"
"v_accvgpr_write_b32 a6, 0\n"
"v_mfma_f32_16x16x32_bf16 v[180:183], v[28:31], a[140:143], 0\n"
"ds_read_b64_tr_b16 v[18:19], v4 offset:1280\n"
"v_mfma_f32_16x16x32_bf16 v[180:183], v[24:27], a[136:139], v[180:183]\n"
"ds_read_b64_tr_b16 v[20:21], v4 offset:9216\n"
"v_accvgpr_write_b32 a7, 0\n"
"v_mfma_f32_16x16x32_bf16 v[184:187], v[24:27], a[144:147], 0\n"
"ds_read_b64_tr_b16 v[22:23], v4 offset:9472\n"
"v_mfma_f32_16x16x32_bf16 v[184:187], v[28:31], a[148:151], v[184:187]\n"
"s_add_u32 m0, s29, 0x8000\n"
"s_mov_b32 s31, 0x100000\n"
"buffer_load_dwordx4 v11, s[16:19], s31 offen lds\n"
"v_mfma_f32_16x16x32_bf16 v[188:191], v[28:31], a[156:159], 0\n"
"s_add_u32 m0, s29, 0x8800\n"
"s_mov_b32 s31, 0x110000\n"
"buffer_load_dwordx4 v11, s[16:19], s31 offen lds\n"
"v_mfma_f32_16x16x32_bf16 v[188:191], v[24:27], a[152:155], v[188:191]\n"
"s_waitcnt lgkmcnt(0)\n"
"v_mfma_f32_16x16x32_bf16 v[192:195], v[16:19], a[128:131], 0\n"
"v_mfma_f32_16x16x32_bf16 v[192:195], v[20:23], a[132:135], v[192:195]\n"
"ds_read_b64_tr_b16 v[24:25], v4 offset:1032\n"
"v_accvgpr_write_b32 a8, 0\n"
"v_mfma_f32_16x16x32_bf16 v[196:199], v[20:23], a[140:143], 0\n"
"ds_read_b64_tr_b16 v[26:27], v4 offset:1288\n"
"v_mfma_f32_16x16x32_bf16 v[196:199], v[16:19], a[136:139], v[196:199]\n"
"ds_read_b64_tr_b16 v[28:29], v4 offset:9224\n"
"v_accvgpr_write_b32 a9, 0\n"
"v_mfma_f32_16x16x32_bf16 v[200:203], v[16:19], a[144:147], 0\n"
"ds_read_b64_tr_b16 v[30:31], v4 offset:9480\n"
"v_mfma_f32_16x16x32_bf16 v[200:203], v[20:23], a[148:151], v[200:203]\n"
"s_add_u32 m0, s29, 0x9000\n"
"s_mov_b32 s31, 0x120000\n"
"buffer_load_dwordx4 v11, s[16:19], s31 offen lds\n"
"v_mfma_f32_16x16x32_bf16 v[204:207], v[20:23], a[156:159], 0\n"
"v_mfma_f32_16x16x32_bf16 v[204:207], v[16:19], a[152:155], v[204:207]\n"
"s_waitcnt lgkmcnt(0)\n"
"v_mfma_f32_16x16x32_bf16 v[208:211], v[24:27], a[128:131], 0\n"
"v_mfma_f32_16x16x32_bf16 v[208:211], v[28:31], a[132:135], v[208:211]\n"
"ds_read_b64_tr_b16 v[16:17], v4 offset:1536\n"
"v_accvgpr_write_b32 a10, 0\n"
"v_mfma_f32_16x16x32_bf16 v[212:215], v[28:31], a[140:143], 0\n"
"ds_read_b64_tr_b16 v[18:19], v4 offset:1792\n"
"v_mfma_f32_16x16x32_bf16 v[212:215], v[24:27], a[136:139], v[212:215]\n"
"ds_read_b64_tr_b16 v[20:21], v4 offset:9728\n"
"v_accvgpr_write_b32 a11, 0\n"
"v_mfma_f32_16x16x32_bf16 v[216:219], v[24:27], a[144:147], 0\n"
"ds_read_b64_tr_b16 v[22:23], v4 offset:9984\n"
"v_mfma_f32_16x16x32_bf16 v[216:219], v[28:31], a[148:151], v[216:219]\n"
"s_add_u32 m0, s29, 0x9800\n"
"s_mov_b32 s31, 0x130000\n"
"buffer_load_dwordx4 v11, s[16:19], s31 offen lds\n"
"v_mfma_f32_16x16x32_bf16 v[220:223], v[28:31], a[156:159], 0\n"
"s_mov_b32 s35, 0x1000\n"
"buffer_load_dwordx4 a[192:195], v12, s[20:23], s35 offen offset:0 sc1\n"
"v_mfma_f32_16x16x32_bf16 v[220:223], v[24:27], a[152:155], v[220:223]\n"
"s_waitcnt lgkmcnt(0)\n"
"v_mfma_f32_16x16x32_bf16 v[224:227], v[16:19], a[128:131], 0\n"
"v_mfma_f32_16x16x32_bf16 v[224:227], v[20:23], a[132:135], v[224:227]\n"
"ds_read_b64_tr_b16 v[24:25], v4 offset:1544\n"
"v_accvgpr_write_b32 a12, 0\n"
"v_mfma_f32_16x16x32_bf16 v[228:231], v[20:23], a[140:143], 0\n"
"ds_read_b64_tr_b16 v[26:27], v4 offset:1800\n"
"v_mfma_f32_16x16x32_bf16 v[228:231], v[16:19], a[136:139], v[228:231]\n"
"ds_read_b64_tr_b16 v[28:29], v4 offset:9736\n"
"v_accvgpr_write_b32 a13, 0\n"
"v_mfma_f32_16x16x32_bf16 v[232:235], v[16:19], a[144:147], 0\n"
"ds_read_b64_tr_b16 v[30:31], v4 offset:9992\n"
"v_mfma_f32_16x16x32_bf16 v[232:235], v[20:23], a[148:151], v[232:235]\n"
"buffer_load_dwordx4 a[196:199], v12, s[20:23], s35 offen offset:1024 sc1\n"
"v_mfma_f32_16x16x32_bf16 v[236:239], v[20:23], a[156:159], 0\n"
"v_mfma_f32_16x16x32_bf16 v[236:239], v[16:19], a[152:155], v[236:239]\n"
"s_waitcnt lgkmcnt(0)\n"
"v_mfma_f32_16x16x32_bf16 v[240:243], v[24:27], a[128:131], 0\n"
"s_waitcnt vmcnt(14)\n"
"s_barrier\n"
"s_mov_b64 exec, 1\n"
"buffer_store_dwordx2 v[56:57], off, s[92:95], s62 sc1\n"
"s_mov_b64 exec, -1\n"
"v_mfma_f32_16x16x32_bf16 v[240:243], v[28:31], a[132:135], v[240:243]\n"
"ds_read_b64_tr_b16 v[16:17], v4 offset:16384\n"
"v_accvgpr_write_b32 a14, 0\n"
"v_mfma_f32_16x16x32_bf16 v[244:247], v[28:31], a[140:143], 0\n"
"ds_read_b64_tr_b16 v[18:19], v4 offset:16640\n"
"v_mfma_f32_16x16x32_bf16 v[244:247], v[24:27], a[136:139], v[244:247]\n"
"ds_read_b64_tr_b16 v[20:21], v4 offset:24576\n"
"v_accvgpr_write_b32 a15, 0\n"
"v_mfma_f32_16x16x32_bf16 v[248:251], v[24:27], a[144:147], 0\n"
"ds_read_b64_tr_b16 v[22:23], v4 offset:24832\n"
"v_mfma_f32_16x16x32_bf16 v[248:251], v[28:31], a[148:151], v[248:251]\n"
"s_mov_b32 s35, 0x6000\n"
"buffer_load_dwordx4 a[200:203], v12, s[20:23], s35 offen offset:0 sc1\n"
"v_mfma_f32_16x16x32_bf16 v[252:255], v[28:31], a[156:159], 0\n"
"buffer_load_dwordx4 a[204:207], v12, s[20:23], s35 offen offset:1024 sc1\n"
"v_mfma_f32_16x16x32_bf16 v[252:255], v[24:27], a[152:155], v[252:255]\n"
"s_waitcnt vmcnt(9) lgkmcnt(0)\n"
"v_mfma_f32_16x16x32_bf16 v[128:131], v[16:19], a[160:163], v[128:131]\n"
"s_mov_b32 s35, 0xb000\n"
"buffer_load_dwordx4 a[208:211], v12, s[20:23], s35 offen offset:0 sc1\n"
"v_mfma_f32_16x16x32_bf16 v[128:131], v[20:23], a[164:167], v[128:131]\n"
"ds_read_b64_tr_b16 v[24:25], v4 offset:16392\n"
"v_accvgpr_write_b32 a16, 0\n"
"v_mfma_f32_16x16x32_bf16 v[132:135], v[20:23], a[172:175], v[132:135]\n"
"ds_read_b64_tr_b16 v[26:27], v4 offset:16648\n"
"v_mfma_f32_16x16x32_bf16 v[132:135], v[16:19], a[168:171], v[132:135]\n"
"ds_read_b64_tr_b16 v[28:29], v4 offset:24584\n"
"v_accvgpr_write_b32 a17, 0\n"
"v_mfma_f32_16x16x32_bf16 v[136:139], v[16:19], a[176:179], v[136:139]\n"
"ds_read_b64_tr_b16 v[30:31], v4 offset:24840\n"
"v_mfma_f32_16x16x32_bf16 v[136:139], v[20:23], a[180:183], v[136:139]\n"
"buffer_load_dwordx4 a[212:215], v12, s[20:23], s35 offen offset:1024 sc1\n"
"v_mfma_f32_16x16x32_bf16 v[140:143], v[20:23], a[188:191], v[140:143]\n"
"s_mov_b32 s35, 0x10000\n"
"buffer_load_dwordx4 a[216:219], v12, s[20:23], s35 offen offset:0 sc1\n"
"v_mfma_f32_16x16x32_bf16 v[140:143], v[16:19], a[184:187], v[140:143]\n"
"s_waitcnt lgkmcnt(0)\n"
"v_mfma_f32_16x16x32_bf16 v[144:147], v[24:27], a[160:163], v[144:147]\n"
"buffer_load_dwordx4 a[220:223], v12, s[20:23], s35 offen offset:1024 sc1\n"
"v_mfma_f32_16x16x32_bf16 v[144:147], v[28:31], a[164:167], v[144:147]\n"
"ds_read_b64_tr_b16 v[16:17], v4 offset:16896\n"
"v_accvgpr_write_b32 a18, 0\n"
"v_mfma_f32_16x16x32_bf16 v[148:151], v[28:31], a[172:175], v[148:151]\n"
"ds_read_b64_tr_b16 v[18:19], v4 offset:17152\n"
"v_mfma_f32_16x16x32_bf16 v[148:151], v[24:27], a[168:171], v[148:151]\n"
"ds_read_b64_tr_b16 v[20:21], v4 offset:25088\n"
"v_accvgpr_write_b32 a19, 0\n"
"v_mfma_f32_16x16x32_bf16 v[152:155], v[24:27], a[176:179], v[152:155]\n"
"ds_read_b64_tr_b16 v[22:23], v4 offset:25344\n"
"v_mfma_f32_16x16x32_bf16 v[152:155], v[28:31], a[180:183], v[152:155]\n"
"s_mov_b32 s35, 0x1800\n"
"buffer_load_dwordx4 a[224:227], v12, s[20:23], s35 offen offset:0 sc1\n"
"v_mfma_f32_16x16x32_bf16 v[156:159], v[28:31], a[188:191], v[156:159]\n"
"buffer_load_dwordx4 a[228:231], v12, s[20:23], s35 offen offset:1024 sc1\n"
"v_mfma_f32_16x16x32_bf16 v[156:159], v[24:27], a[184:187], v[156:159]\n"
"s_waitcnt lgkmcnt(0)\n"
"v_mfma_f32_16x16x32_bf16 v[160:163], v[16:19], a[160:163], v[160:163]\n"
"v_mfma_f32_16x16x32_bf16 v[160:163], v[20:23], a[164:167], v[160:163]\n"
"ds_read_b64_tr_b16 v[24:25], v4 offset:16904\n"
"v_accvgpr_write_b32 a20, 0\n"
"v_mfma_f32_16x16x32_bf16 v[164:167], v[20:23], a[172:175], v[164:167]\n"
"ds_read_b64_tr_b16 v[26:27], v4 offset:17160\n"
"v_mfma_f32_16x16x32_bf16 v[164:167], v[16:19], a[168:171], v[164:167]\n"
"ds_read_b64_tr_b16 v[28:29], v4 offset:25096\n"
"v_accvgpr_write_b32 a21, 0\n"
"v_mfma_f32_16x16x32_bf16 v[168:171], v[16:19], a[176:179], v[168:171]\n"
"ds_read_b64_tr_b16 v[30:31], v4 offset:25352\n"
"v_mfma_f32_16x16x32_bf16 v[168:171], v[20:23], a[180:183], v[168:171]\n"
"s_mov_b32 s35, 0x6800\n"
"buffer_load_dwordx4 a[232:235], v12, s[20:23], s35 offen offset:0 sc1\n"
"v_mfma_f32_16x16x32_bf16 v[172:175], v[20:23], a[188:191], v[172:175]\n"
"v_mfma_f32_16x16x32_bf16 v[172:175], v[16:19], a[184:187], v[172:175]\n"
"s_waitcnt lgkmcnt(0)\n"
"v_mfma_f32_16x16x32_bf16 v[176:179], v[24:27], a[160:163], v[176:179]\n"
"v_mfma_f32_16x16x32_bf16 v[176:179], v[28:31], a[164:167], v[176:179]\n"
"ds_read_b64_tr_b16 v[16:17], v4 offset:17408\n"
"v_accvgpr_write_b32 a22, 0\n"
"v_mfma_f32_16x16x32_bf16 v[180:183], v[28:31], a[172:175], v[180:183]\n"
"ds_read_b64_tr_b16 v[18:19], v4 offset:17664\n"
"v_mfma_f32_16x16x32_bf16 v[180:183], v[24:27], a[168:171], v[180:183]\n"
"ds_read_b64_tr_b16 v[20:21], v4 offset:25600\n"
"v_accvgpr_write_b32 a23, 0\n"
"v_mfma_f32_16x16x32_bf16 v[184:187], v[24:27], a[176:179], v[184:187]\n"
"ds_read_b64_tr_b16 v[22:23], v4 offset:25856\n"
"v_mfma_f32_16x16x32_bf16 v[184:187], v[28:31], a[180:183], v[184:187]\n"
"buffer_load_dwordx4 a[236:239], v12, s[20:23], s35 offen offset:1024 sc1\n"
"v_mfma_f32_16x16x32_bf16 v[188:191], v[28:31], a[188:191], v[188:191]\n"
"s_mov_b32 s35, 0xb800\n"
"buffer_load_dwordx4 a[240:243], v12, s[20:23], s35 offen offset:0 sc1\n"
"v_mfma_f32_16x16x32_bf16 v[188:191], v[24:27], a[184:187], v[188:191]\n"
"s_waitcnt lgkmcnt(0)\n"
"v_mfma_f32_16x16x32_bf16 v[192:195], v[16:19], a[160:163], v[192:195]\n"
"v_mfma_f32_16x16x32_bf16 v[192:195], v[20:23], a[164:167], v[192:195]\n"
"ds_read_b64_tr_b16 v[24:25], v4 offset:17416\n"
"v_accvgpr_write_b32 a24, 0\n"
"v_mfma_f32_16x16x32_bf16 v[196:199], v[20:23], a[172:175], v[196:199]\n"
"ds_read_b64_tr_b16 v[26:27], v4 offset:17672\n"
"v_mfma_f32_16x16x32_bf16 v[196:199], v[16:19], a[168:171], v[196:199]\n"
"ds_read_b64_tr_b16 v[28:29], v4 offset:25608\n"
"v_accvgpr_write_b32 a25, 0\n"
"v_mfma_f32_16x16x32_bf16 v[200:203], v[16:19], a[176:179], v[200:203]\n"
"ds_read_b64_tr_b16 v[30:31], v4 offset:25864\n"
"v_mfma_f32_16x16x32_bf16 v[200:203], v[20:23], a[180:183], v[200:203]\n"
"buffer_load_dwordx4 a[244:247], v12, s[20:23], s35 offen offset:1024 sc1\n"
"v_mfma_f32_16x16x32_bf16 v[204:207], v[20:23], a[188:191], v[204:207]\n"
"v_mfma_f32_16x16x32_bf16 v[204:207], v[16:19], a[184:187], v[204:207]\n"
"s_waitcnt lgkmcnt(0)\n"
"v_mfma_f32_16x16x32_bf16 v[208:211], v[24:27], a[160:163], v[208:211]\n"
"v_mfma_f32_16x16x32_bf16 v[208:211], v[28:31], a[164:167], v[208:211]\n"
"ds_read_b64_tr_b16 v[16:17], v4 offset:17920\n"
"v_accvgpr_write_b32 a26, 0\n"
"v_mfma_f32_16x16x32_bf16 v[212:215], v[28:31], a[172:175], v[212:215]\n"
"ds_read_b64_tr_b16 v[18:19], v4 offset:18176\n"
"v_mfma_f32_16x16x32_bf16 v[212:215], v[24:27], a[168:171], v[212:215]\n"
"ds_read_b64_tr_b16 v[20:21], v4 offset:26112\n"
"v_accvgpr_write_b32 a27, 0\n"
"v_mfma_f32_16x16x32_bf16 v[216:219], v[24:27], a[176:179], v[216:219]\n"
"ds_read_b64_tr_b16 v[22:23], v4 offset:26368\n"
"v_mfma_f32_16x16x32_bf16 v[216:219], v[28:31], a[180:183], v[216:219]\n"
"s_mov_b32 s35, 0x10800\n"
"buffer_load_dwordx4 a[248:251], v12, s[20:23], s35 offen offset:0 sc1\n"
"v_mfma_f32_16x16x32_bf16 v[220:223], v[28:31], a[188:191], v[220:223]\n"
"buffer_load_dwordx4 a[252:255], v12, s[20:23], s35 offen offset:1024 sc1\n"
"v_mfma_f32_16x16x32_bf16 v[220:223], v[24:27], a[184:187], v[220:223]\n"
"s_waitcnt lgkmcnt(0)\n"
"v_mfma_f32_16x16x32_bf16 v[224:227], v[16:19], a[160:163], v[224:227]\n"
"v_mfma_f32_16x16x32_bf16 v[224:227], v[20:23], a[164:167], v[224:227]\n"
"ds_read_b64_tr_b16 v[24:25], v4 offset:17928\n"
"v_accvgpr_write_b32 a28, 0\n"
"v_mfma_f32_16x16x32_bf16 v[228:231], v[20:23], a[172:175], v[228:231]\n"
"ds_read_b64_tr_b16 v[26:27], v4 offset:18184\n"
"v_mfma_f32_16x16x32_bf16 v[228:231], v[16:19], a[168:171], v[228:231]\n"
"ds_read_b64_tr_b16 v[28:29], v4 offset:26120\n"
"v_accvgpr_write_b32 a29, 0\n"
"v_mfma_f32_16x16x32_bf16 v[232:235], v[16:19], a[176:179], v[232:235]\n"
"ds_read_b64_tr_b16 v[30:31], v4 offset:26376\n"
"v_mfma_f32_16x16x32_bf16 v[232:235], v[20:23], a[180:183], v[232:235]\n"
"s_add_u32 m0, s29, 0xc000\n"
"s_mov_b32 s31, 0x180000\n"
"buffer_load_dwordx4 v11, s[16:19], s31 offen lds\n"
"v_mfma_f32_16x16x32_bf16 v[236:239], v[20:23], a[188:191], v[236:239]\n"
"v_mfma_f32_16x16x32_bf16 v[236:239], v[16:19], a[184:187], v[236:239]\n"
"s_waitcnt lgkmcnt(0)\n"
"v_mfma_f32_16x16x32_bf16 v[240:243], v[24:27], a[160:163], v[240:243]\n"
"s_waitcnt vmcnt(18)\n"
"s_barrier\n"
"v_mfma_f32_16x16x32_bf16 v[240:243], v[28:31], a[164:167], v[240:243]\n"
"ds_read_b64_tr_b16 v[16:17], v4 offset:32768\n"
"v_accvgpr_write_b32 a30, 0\n"
"v_mfma_f32_16x16x32_bf16 v[244:247], v[28:31], a[172:175], v[244:247]\n"
"ds_read_b64_tr_b16 v[18:19], v4 offset:33024\n"
"v_mfma_f32_16x16x32_bf16 v[244:247], v[24:27], a[168:171], v[244:247]\n"
"ds_read_b64_tr_b16 v[20:21], v4 offset:40960\n"
"v_accvgpr_write_b32 a31, 0\n"
"v_mfma_f32_16x16x32_bf16 v[248:251], v[24:27], a[176:179], v[248:251]\n"
"ds_read_b64_tr_b16 v[22:23], v4 offset:41216\n"
"v_mfma_f32_16x16x32_bf16 v[248:251], v[28:31], a[180:183], v[248:251]\n"
"s_add_u32 m0, s29, 0xc800\n"
"s_mov_b32 s31, 0x190000\n"
"buffer_load_dwordx4 v11, s[16:19], s31 offen lds\n"
"v_mfma_f32_16x16x32_bf16 v[252:255], v[28:31], a[188:191], v[252:255]\n"
"s_add_u32 m0, s29, 0xd000\n"
"s_mov_b32 s31, 0x1a0000\n"
"buffer_load_dwordx4 v11, s[16:19], s31 offen lds\n"
"v_mfma_f32_16x16x32_bf16 v[252:255], v[24:27], a[184:187], v[252:255]\n"
"s_waitcnt vmcnt(11) lgkmcnt(0)\n"
"v_mfma_f32_16x16x32_bf16 v[128:131], v[16:19], a[192:195], v[128:131]\n"
"s_add_u32 m0, s29, 0xd800\n"
"s_mov_b32 s31, 0x1b0000\n"
"buffer_load_dwordx4 v11, s[16:19], s31 offen lds\n"
"v_mfma_f32_16x16x32_bf16 v[128:131], v[20:23], a[196:199], v[128:131]\n"
"ds_read_b64_tr_b16 v[24:25], v4 offset:32776\n"
"v_accvgpr_write_b32 a32, 0\n"
"v_mfma_f32_16x16x32_bf16 v[132:135], v[20:23], a[204:207], v[132:135]\n"
"ds_read_b64_tr_b16 v[26:27], v4 offset:33032\n"
"v_mfma_f32_16x16x32_bf16 v[132:135], v[16:19], a[200:203], v[132:135]\n"
"ds_read_b64_tr_b16 v[28:29], v4 offset:40968\n"
"v_accvgpr_write_b32 a33, 0\n"
"v_mfma_f32_16x16x32_bf16 v[136:139], v[16:19], a[208:211], v[136:139]\n"
"ds_read_b64_tr_b16 v[30:31], v4 offset:41224\n"
"v_mfma_f32_16x16x32_bf16 v[136:139], v[20:23], a[212:215], v[136:139]\n"
"s_mov_b32 s35, 0x2000\n"
"buffer_load_dwordx4 a[128:131], v12, s[20:23], s35 offen offset:0 sc1\n"
"v_mfma_f32_16x16x32_bf16 v[140:143], v[20:23], a[220:223], v[140:143]\n"
"v_mfma_f32_16x16x32_bf16 v[140:143], v[16:19], a[216:219], v[140:143]\n"
"s_waitcnt lgkmcnt(0)\n"
"v_mfma_f32_16x16x32_bf16 v[144:147], v[24:27], a[192:195], v[144:147]\n"
"v_mfma_f32_16x16x32_bf16 v[144:147], v[28:31], a[196:199], v[144:147]\n"
"ds_read_b64_tr_b16 v[16:17], v4 offset:33280\n"
"v_accvgpr_write_b32 a34, 0\n"
"v_mfma_f32_16x16x32_bf16 v[148:151], v[28:31], a[204:207], v[148:151]\n"
"ds_read_b64_tr_b16 v[18:19], v4 offset:33536\n"
"v_mfma_f32_16x16x32_bf16 v[148:151], v[24:27], a[200:203], v[148:151]\n"
"ds_read_b64_tr_b16 v[20:21], v4 offset:41472\n"
"v_accvgpr_write_b32 a35, 0\n"
"v_mfma_f32_16x16x32_bf16 v[152:155], v[24:27], a[208:211], v[152:155]\n"
"ds_read_b64_tr_b16 v[22:23], v4 offset:41728\n"
"v_mfma_f32_16x16x32_bf16 v[152:155], v[28:31], a[212:215], v[152:155]\n"
"buffer_load_dwordx4 a[132:135], v12, s[20:23], s35 offen offset:1024 sc1\n"
"v_mfma_f32_16x16x32_bf16 v[156:159], v[28:31], a[220:223], v[156:159]\n"
"s_mov_b32 s35, 0x7000\n"
"buffer_load_dwordx4 a[136:139], v12, s[20:23], s35 offen offset:0 sc1\n"
"v_mfma_f32_16x16x32_bf16 v[156:159], v[24:27], a[216:219], v[156:159]\n"
"s_waitcnt lgkmcnt(0)\n"
"v_mfma_f32_16x16x32_bf16 v[160:163], v[16:19], a[192:195], v[160:163]\n"
"v_mfma_f32_16x16x32_bf16 v[160:163], v[20:23], a[196:199], v[160:163]\n"
"ds_read_b64_tr_b16 v[24:25], v4 offset:33288\n"
"v_accvgpr_write_b32 a36, 0\n"
"v_mfma_f32_16x16x32_bf16 v[164:167], v[20:23], a[204:207], v[164:167]\n"
"ds_read_b64_tr_b16 v[26:27], v4 offset:33544\n"
"v_mfma_f32_16x16x32_bf16 v[164:167], v[16:19], a[200:203], v[164:167]\n"
"ds_read_b64_tr_b16 v[28:29], v4 offset:41480\n"
"v_accvgpr_write_b32 a37, 0\n"
"v_mfma_f32_16x16x32_bf16 v[168:171], v[16:19], a[208:211], v[168:171]\n"
"ds_read_b64_tr_b16 v[30:31], v4 offset:41736\n"
"v_mfma_f32_16x16x32_bf16 v[168:171], v[20:23], a[212:215], v[168:171]\n"
"buffer_load_dwordx4 a[140:143], v12, s[20:23], s35 offen offset:1024 sc1\n"
"v_mfma_f32_16x16x32_bf16 v[172:175], v[20:23], a[220:223], v[172:175]\n"
"v_mfma_f32_16x16x32_bf16 v[172:175], v[16:19], a[216:219], v[172:175]\n"
"s_waitcnt lgkmcnt(0)\n"
"v_mfma_f32_16x16x32_bf16 v[176:179], v[24:27], a[192:195], v[176:179]\n"
"v_mfma_f32_16x16x32_bf16 v[176:179], v[28:31], a[196:199], v[176:179]\n"
"ds_read_b64_tr_b16 v[16:17], v4 offset:33792\n"
"v_accvgpr_write_b32 a38, 0\n"
"v_mfma_f32_16x16x32_bf16 v[180:183], v[28:31], a[204:207], v[180:183]\n"
"ds_read_b64_tr_b16 v[18:19], v4 offset:34048\n"
"v_mfma_f32_16x16x32_bf16 v[180:183], v[24:27], a[200:203], v[180:183]\n"
"ds_read_b64_tr_b16 v[20:21], v4 offset:41984\n"
"v_accvgpr_write_b32 a39, 0\n"
"v_mfma_f32_16x16x32_bf16 v[184:187], v[24:27], a[208:211], v[184:187]\n"
"ds_read_b64_tr_b16 v[22:23], v4 offset:42240\n"
"v_mfma_f32_16x16x32_bf16 v[184:187], v[28:31], a[212:215], v[184:187]\n"
"s_mov_b32 s35, 0xc000\n"
"buffer_load_dwordx4 a[144:147], v12, s[20:23], s35 offen offset:0 sc1\n"
"v_mfma_f32_16x16x32_bf16 v[188:191], v[28:31], a[220:223], v[188:191]\n"
"buffer_load_dwordx4 a[148:151], v12, s[20:23], s35 offen offset:1024 sc1\n"
"v_mfma_f32_16x16x32_bf16 v[188:191], v[24:27], a[216:219], v[188:191]\n"
"s_waitcnt lgkmcnt(0)\n"
"v_mfma_f32_16x16x32_bf16 v[192:195], v[16:19], a[192:195], v[192:195]\n"
"v_mfma_f32_16x16x32_bf16 v[192:195], v[20:23], a[196:199], v[192:195]\n"
"ds_read_b64_tr_b16 v[24:25], v4 offset:33800\n"
"v_accvgpr_write_b32 a40, 0\n"
"v_mfma_f32_16x16x32_bf16 v[196:199], v[20:23], a[204:207], v[196:199]\n"
"ds_read_b64_tr_b16 v[26:27], v4 offset:34056\n"
"v_mfma_f32_16x16x32_bf16 v[196:199], v[16:19], a[200:203], v[196:199]\n"
"ds_read_b64_tr_b16 v[28:29], v4 offset:41992\n"
"v_accvgpr_write_b32 a41, 0\n"
"v_mfma_f32_16x16x32_bf16 v[200:203], v[16:19], a[208:211], v[200:203]\n"
"ds_read_b64_tr_b16 v[30:31], v4 offset:42248\n"
"v_mfma_f32_16x16x32_bf16 v[200:203], v[20:23], a[212:215], v[200:203]\n"
"s_mov_b32 s35, 0x11000\n"
"buffer_load_dwordx4 a[152:155], v12, s[20:23], s35 offen offset:0 sc1\n"
"v_mfma_f32_16x16x32_bf16 v[204:207], v[20:23], a[220:223], v[204:207]\n"
"v_mfma_f32_16x16x32_bf16 v[204:207], v[16:19], a[216:219], v[204:207]\n"
"s_waitcnt lgkmcnt(0)\n"
"v_mfma_f32_16x16x32_bf16 v[208:211], v[24:27], a[192:195], v[208:211]\n"
"v_mfma_f32_16x16x32_bf16 v[208:211], v[28:31], a[196:199], v[208:211]\n"
"ds_read_b64_tr_b16 v[16:17], v4 offset:34304\n"
"v_accvgpr_write_b32 a42, 0\n"
"v_mfma_f32_16x16x32_bf16 v[212:215], v[28:31], a[204:207], v[212:215]\n"
"ds_read_b64_tr_b16 v[18:19], v4 offset:34560\n"
"v_mfma_f32_16x16x32_bf16 v[212:215], v[24:27], a[200:203], v[212:215]\n"
"ds_read_b64_tr_b16 v[20:21], v4 offset:42496\n"
"v_accvgpr_write_b32 a43, 0\n"
"v_mfma_f32_16x16x32_bf16 v[216:219], v[24:27], a[208:211], v[216:219]\n"
"ds_read_b64_tr_b16 v[22:23], v4 offset:42752\n"
"v_mfma_f32_16x16x32_bf16 v[216:219], v[28:31], a[212:215], v[216:219]\n"
"buffer_load_dwordx4 a[156:159], v12, s[20:23], s35 offen offset:1024 sc1\n"
"v_mfma_f32_16x16x32_bf16 v[220:223], v[28:31], a[220:223], v[220:223]\n"
"s_add_u32 m0, s29, 0x10000\n"
"s_mov_b32 s31, 0x200000\n"
"buffer_load_dwordx4 v11, s[16:19], s31 offen lds\n"
"v_mfma_f32_16x16x32_bf16 v[220:223], v[24:27], a[216:219], v[220:223]\n"
"s_waitcnt lgkmcnt(0)\n"
"v_mfma_f32_16x16x32_bf16 v[224:227], v[16:19], a[192:195], v[224:227]\n"
"v_mfma_f32_16x16x32_bf16 v[224:227], v[20:23], a[196:199], v[224:227]\n"
"ds_read_b64_tr_b16 v[24:25], v4 offset:34312\n"
"v_accvgpr_write_b32 a44, 0\n"
"v_mfma_f32_16x16x32_bf16 v[228:231], v[20:23], a[204:207], v[228:231]\n"
"ds_read_b64_tr_b16 v[26:27], v4 offset:34568\n"
"v_mfma_f32_16x16x32_bf16 v[228:231], v[16:19], a[200:203], v[228:231]\n"
"ds_read_b64_tr_b16 v[28:29], v4 offset:42504\n"
"v_accvgpr_write_b32 a45, 0\n"
"v_mfma_f32_16x16x32_bf16 v[232:235], v[16:19], a[208:211], v[232:235]\n"
"ds_read_b64_tr_b16 v[30:31], v4 offset:42760\n"
"v_mfma_f32_16x16x32_bf16 v[232:235], v[20:23], a[212:215], v[232:235]\n"
"s_add_u32 m0, s29, 0x10800\n"
"s_mov_b32 s31, 0x210000\n"
"buffer_load_dwordx4 v11, s[16:19], s31 offen lds\n"
"v_mfma_f32_16x16x32_bf16 v[236:239], v[20:23], a[220:223], v[236:239]\n"
"v_mfma_f32_16x16x32_bf16 v[236:239], v[16:19], a[216:219], v[236:239]\n"
"s_waitcnt lgkmcnt(0)\n"
"v_mfma_f32_16x16x32_bf16 v[240:243], v[24:27], a[192:195], v[240:243]\n"
"s_waitcnt vmcnt(10)\n"
"s_barrier\n"
"v_mfma_f32_16x16x32_bf16 v[240:243], v[28:31], a[196:199], v[240:243]\n"
"ds_read_b64_tr_b16 v[16:17], v4 offset:49152\n"
"v_accvgpr_write_b32 a46, 0\n"
"v_mfma_f32_16x16x32_bf16 v[244:247], v[28:31], a[204:207], v[244:247]\n"
"ds_read_b64_tr_b16 v[18:19], v4 offset:49408\n"
"v_mfma_f32_16x16x32_bf16 v[244:247], v[24:27], a[200:203], v[244:247]\n"
"ds_read_b64_tr_b16 v[20:21], v4 offset:57344\n"
"v_accvgpr_write_b32 a47, 0\n"
"v_mfma_f32_16x16x32_bf16 v[248:251], v[24:27], a[208:211], v[248:251]\n"
"ds_read_b64_tr_b16 v[22:23], v4 offset:57600\n"
"v_mfma_f32_16x16x32_bf16 v[248:251], v[28:31], a[212:215], v[248:251]\n"
"s_add_u32 m0, s29, 0x11000\n"
"s_mov_b32 s31, 0x220000\n"
"buffer_load_dwordx4 v11, s[16:19], s31 offen lds\n"
"v_mfma_f32_16x16x32_bf16 v[252:255], v[28:31], a[220:223], v[252:255]\n"
"s_add_u32 m0, s29, 0x11800\n"
"s_mov_b32 s31, 0x230000\n"
"buffer_load_dwordx4 v11, s[16:19], s31 offen lds\n"
"v_mfma_f32_16x16x32_bf16 v[252:255], v[24:27], a[216:219], v[252:255]\n"
"s_waitcnt vmcnt(16) lgkmcnt(0)\n"
"v_mfma_f32_16x16x32_bf16 v[128:131], v[16:19], a[224:227], v[128:131]\n"
"v_mfma_f32_16x16x32_bf16 v[128:131], v[20:23], a[228:231], v[128:131]\n"
"ds_read_b64_tr_b16 v[24:25], v4 offset:49160\n"
"v_accvgpr_write_b32 a48, 0\n"
"v_mfma_f32_16x16x32_bf16 v[132:135], v[20:23], a[236:239], v[132:135]\n"
"ds_read_b64_tr_b16 v[26:27], v4 offset:49416\n"
"v_mfma_f32_16x16x32_bf16 v[132:135], v[16:19], a[232:235], v[132:135]\n"
"ds_read_b64_tr_b16 v[28:29], v4 offset:57352\n"
"v_accvgpr_write_b32 a49, 0\n"
"v_mfma_f32_16x16x32_bf16 v[136:139], v[16:19], a[240:243], v[136:139]\n"
"ds_read_b64_tr_b16 v[30:31], v4 offset:57608\n"
"v_mfma_f32_16x16x32_bf16 v[136:139], v[20:23], a[244:247], v[136:139]\n"
"s_mov_b32 s35, 0x2800\n"
"buffer_load_dwordx4 a[160:163], v12, s[20:23], s35 offen offset:0 sc1\n"
"v_mfma_f32_16x16x32_bf16 v[140:143], v[20:23], a[252:255], v[140:143]\n"
"v_mfma_f32_16x16x32_bf16 v[140:143], v[16:19], a[248:251], v[140:143]\n"
"s_waitcnt lgkmcnt(0)\n"
"v_mfma_f32_16x16x32_bf16 v[144:147], v[24:27], a[224:227], v[144:147]\n"
"v_mfma_f32_16x16x32_bf16 v[144:147], v[28:31], a[228:231], v[144:147]\n"
"ds_read_b64_tr_b16 v[16:17], v4 offset:49664\n"
"v_accvgpr_write_b32 a50, 0\n"
"v_mfma_f32_16x16x32_bf16 v[148:151], v[28:31], a[236:239], v[148:151]\n"
"ds_read_b64_tr_b16 v[18:19], v4 offset:49920\n"
"v_mfma_f32_16x16x32_bf16 v[148:151], v[24:27], a[232:235], v[148:151]\n"
"ds_read_b64_tr_b16 v[20:21], v4 offset:57856\n"
"v_accvgpr_write_b32 a51, 0\n"
"v_mfma_f32_16x16x32_bf16 v[152:155], v[24:27], a[240:243], v[152:155]\n"
"ds_read_b64_tr_b16 v[22:23], v4 offset:58112\n"
"v_mfma_f32_16x16x32_bf16 v[152:155], v[28:31], a[244:247], v[152:155]\n"
"buffer_load_dwordx4 a[164:167], v12, s[20:23], s35 offen offset:1024 sc1\n"
"v_mfma_f32_16x16x32_bf16 v[156:159], v[28:31], a[252:255], v[156:159]\n"
"s_mov_b32 s35, 0x7800\n"
"buffer_load_dwordx4 a[168:171], v12, s[20:23], s35 offen offset:0 sc1\n"
"v_mfma_f32_16x16x32_bf16 v[156:159], v[24:27], a[248:251], v[156:159]\n"
"s_waitcnt lgkmcnt(0)\n"
"v_mfma_f32_16x16x32_bf16 v[160:163], v[16:19], a[224:227], v[160:163]\n"
"v_mfma_f32_16x16x32_bf16 v[160:163], v[20:23], a[228:231], v[160:163]\n"
"ds_read_b64_tr_b16 v[24:25], v4 offset:49672\n"
"v_accvgpr_write_b32 a52, 0\n"
"v_mfma_f32_16x16x32_bf16 v[164:167], v[20:23], a[236:239], v[164:167]\n"
"ds_read_b64_tr_b16 v[26:27], v4 offset:49928\n"
"v_mfma_f32_16x16x32_bf16 v[164:167], v[16:19], a[232:235], v[164:167]\n"
"ds_read_b64_tr_b16 v[28:29], v4 offset:57864\n"
"v_accvgpr_write_b32 a53, 0\n"
"v_mfma_f32_16x16x32_bf16 v[168:171], v[16:19], a[240:243], v[168:171]\n"
"ds_read_b64_tr_b16 v[30:31], v4 offset:58120\n"
"v_mfma_f32_16x16x32_bf16 v[168:171], v[20:23], a[244:247], v[168:171]\n"
"buffer_load_dwordx4 a[172:175], v12, s[20:23], s35 offen offset:1024 sc1\n"
"v_mfma_f32_16x16x32_bf16 v[172:175], v[20:23], a[252:255], v[172:175]\n"
"v_mfma_f32_16x16x32_bf16 v[172:175], v[16:19], a[248:251], v[172:175]\n"
"s_waitcnt lgkmcnt(0)\n"
"v_mfma_f32_16x16x32_bf16 v[176:179], v[24:27], a[224:227], v[176:179]\n"
"v_mfma_f32_16x16x32_bf16 v[176:179], v[28:31], a[228:231], v[176:179]\n"
"ds_read_b64_tr_b16 v[16:17], v4 offset:50176\n"
"v_accvgpr_write_b32 a54, 0\n"
"v_mfma_f32_16x16x32_bf16 v[180:183], v[28:31], a[236:239], v[180:183]\n"
"ds_read_b64_tr_b16 v[18:19], v4 offset:50432\n"
"v_mfma_f32_16x16x32_bf16 v[180:183], v[24:27], a[232:235], v[180:183]\n"
"ds_read_b64_tr_b16 v[20:21], v4 offset:58368\n"
"v_accvgpr_write_b32 a55, 0\n"
"v_mfma_f32_16x16x32_bf16 v[184:187], v[24:27], a[240:243], v[184:187]\n"
"ds_read_b64_tr_b16 v[22:23], v4 offset:58624\n"
"v_mfma_f32_16x16x32_bf16 v[184:187], v[28:31], a[244:247], v[184:187]\n"
"s_mov_b32 s35, 0xc800\n"
"buffer_load_dwordx4 a[176:179], v12, s[20:23], s35 offen offset:0 sc1\n"
"v_mfma_f32_16x16x32_bf16 v[188:191], v[28:31], a[252:255], v[188:191]\n"
"buffer_load_dwordx4 a[180:183], v12, s[20:23], s35 offen offset:1024 sc1\n"
"v_mfma_f32_16x16x32_bf16 v[188:191], v[24:27], a[248:251], v[188:191]\n"
"s_waitcnt lgkmcnt(0)\n"
"v_mfma_f32_16x16x32_bf16 v[192:195], v[16:19], a[224:227], v[192:195]\n"
"v_mfma_f32_16x16x32_bf16 v[192:195], v[20:23], a[228:231], v[192:195]\n"
"ds_read_b64_tr_b16 v[24:25], v4 offset:50184\n"
"v_accvgpr_write_b32 a56, 0\n"
"v_mfma_f32_16x16x32_bf16 v[196:199], v[20:23], a[236:239], v[196:199]\n"
"ds_read_b64_tr_b16 v[26:27], v4 offset:50440\n"
"v_mfma_f32_16x16x32_bf16 v[196:199], v[16:19], a[232:235], v[196:199]\n"
"ds_read_b64_tr_b16 v[28:29], v4 offset:58376\n"
"v_accvgpr_write_b32 a57, 0\n"
"v_mfma_f32_16x16x32_bf16 v[200:203], v[16:19], a[240:243], v[200:203]\n"
"ds_read_b64_tr_b16 v[30:31], v4 offset:58632\n"
"v_mfma_f32_16x16x32_bf16 v[200:203], v[20:23], a[244:247], v[200:203]\n"
"s_mov_b32 s35, 0x11800\n"
"buffer_load_dwordx4 a[184:187], v12, s[20:23], s35 offen offset:0 sc1\n"
"v_mfma_f32_16x16x32_bf16 v[204:207], v[20:23], a[252:255], v[204:207]\n"
"v_mfma_f32_16x16x32_bf16 v[204:207], v[16:19], a[248:251], v[204:207]\n"
"s_waitcnt lgkmcnt(0)\n"
"v_mfma_f32_16x16x32_bf16 v[208:211], v[24:27], a[224:227], v[208:211]\n"
"v_mfma_f32_16x16x32_bf16 v[208:211], v[28:31], a[228:231], v[208:211]\n"
"ds_read_b64_tr_b16 v[16:17], v4 offset:50688\n"
"v_accvgpr_write_b32 a58, 0\n"
"v_mfma_f32_16x16x32_bf16 v[212:215], v[28:31], a[236:239], v[212:215]\n"
"ds_read_b64_tr_b16 v[18:19], v4 offset:50944\n"
"v_mfma_f32_16x16x32_bf16 v[212:215], v[24:27], a[232:235], v[212:215]\n"
"ds_read_b64_tr_b16 v[20:21], v4 offset:58880\n"
"v_accvgpr_write_b32 a59, 0\n"
"v_mfma_f32_16x16x32_bf16 v[216:219], v[24:27], a[240:243], v[216:219]\n"
"ds_read_b64_tr_b16 v[22:23], v4 offset:59136\n"
"v_mfma_f32_16x16x32_bf16 v[216:219], v[28:31], a[244:247], v[216:219]\n"
"s_lshl_b32 s60, %1, 2\n"
"s_add_u32 s60, s60, s90\n"
"s_lshl_b32 s60, s60, 9\n"
"s_add_u32 s60, s60, 20480\n"
"buffer_load_dword v15, v63, s[92:95], s60 offen sc1\n"
"v_mfma_f32_16x16x32_bf16 v[220:223], v[28:31], a[252:255], v[220:223]\n"
"buffer_load_dwordx4 a[188:191], v12, s[20:23], s35 offen offset:1024 sc1\n"
"v_mfma_f32_16x16x32_bf16 v[220:223], v[24:27], a[248:251], v[220:223]\n"
"s_waitcnt lgkmcnt(0)\n"
"v_mfma_f32_16x16x32_bf16 v[224:227], v[16:19], a[224:227], v[224:227]\n"
"v_mfma_f32_16x16x32_bf16 v[224:227], v[20:23], a[228:231], v[224:227]\n"
"ds_read_b64_tr_b16 v[24:25], v4 offset:50696\n"
"v_accvgpr_write_b32 a60, 0\n"
"v_mfma_f32_16x16x32_bf16 v[228:231], v[20:23], a[236:239], v[228:231]\n"
"ds_read_b64_tr_b16 v[26:27], v4 offset:50952\n"
"v_mfma_f32_16x16x32_bf16 v[228:231], v[16:19], a[232:235], v[228:231]\n"
"ds_read_b64_tr_b16 v[28:29], v4 offset:58888\n"
"v_accvgpr_write_b32 a61, 0\n"
"v_mfma_f32_16x16x32_bf16 v[232:235], v[16:19], a[240:243], v[232:235]\n"
"ds_read_b64_tr_b16 v[30:31], v4 offset:59144\n"
"v_mfma_f32_16x16x32_bf16 v[232:235], v[20:23], a[244:247], v[232:235]\n"
"s_add_u32 m0, s29, 0x14000\n"
"s_mov_b32 s31, 0x280000\n"
"buffer_load_dwordx4 v11, s[16:19], s31 offen lds\n"
"v_mfma_f32_16x16x32_bf16 v[236:239], v[20:23], a[252:255], v[236:239]\n"
"v_mfma_f32_16x16x32_bf16 v[236:239], v[16:19], a[248:251], v[236:239]\n"
"s_waitcnt lgkmcnt(0)\n"
"v_mfma_f32_16x16x32_bf16 v[240:243], v[24:27], a[224:227], v[240:243]\n"
"s_waitcnt vmcnt(10)\n"
"s_barrier\n"
"v_add_u32 v4, 0x10000, v4\n"
"v_mfma_f32_16x16x32_bf16 v[240:243], v[28:31], a[228:231], v[240:243]\n"
"ds_read_b64_tr_b16 v[16:17], v4 offset:0\n"
"v_accvgpr_write_b32 a62, 0\n"
"v_mfma_f32_16x16x32_bf16 v[244:247], v[28:31], a[236:239], v[244:247]\n"
"ds_read_b64_tr_b16 v[18:19], v4 offset:256\n"
"v_mfma_f32_16x16x32_bf16 v[244:247], v[24:27], a[232:235], v[244:247]\n"
"ds_read_b64_tr_b16 v[20:21], v4 offset:8192\n"
"v_accvgpr_write_b32 a63, 0\n"
"v_mfma_f32_16x16x32_bf16 v[248:251], v[24:27], a[240:243], v[248:251]\n"
"ds_read_b64_tr_b16 v[22:23], v4 offset:8448\n"
"v_mfma_f32_16x16x32_bf16 v[248:251], v[28:31], a[244:247], v[248:251]\n"
"s_add_u32 m0, s29, 0x14800\n"
"s_mov_b32 s31, 0x290000\n"
"buffer_load_dwordx4 v11, s[16:19], s31 offen lds\n"
"v_mfma_f32_16x16x32_bf16 v[252:255], v[28:31], a[252:255], v[252:255]\n"
"s_add_u32 m0, s29, 0x15000\n"
"s_mov_b32 s31, 0x2a0000\n"
"buffer_load_dwordx4 v11, s[16:19], s31 offen lds\n"
"v_mfma_f32_16x16x32_bf16 v[252:255], v[24:27], a[248:251], v[252:255]\n"
"s_waitcnt vmcnt(16) lgkmcnt(0)\n"
"v_mfma_f32_16x16x32_bf16 v[128:131], v[16:19], a[128:131], v[128:131]\n"
"s_add_u32 m0, s29, 0x15800\n"
"s_mov_b32 s31, 0x2b0000\n"
"buffer_load_dwordx4 v11, s[16:19], s31 offen lds\n"
"v_mfma_f32_16x16x32_bf16 v[128:131], v[20:23], a[132:135], v[128:131]\n"
"ds_read_b64_tr_b16 v[24:25], v4 offset:8\n"
"v_accvgpr_write_b32 a64, 0\n"
"v_mfma_f32_16x16x32_bf16 v[132:135], v[20:23], a[140:143], v[132:135]\n"
"ds_read_b64_tr_b16 v[26:27], v4 offset:264\n"
"v_mfma_f32_16x16x32_bf16 v[132:135], v[16:19], a[136:139], v[132:135]\n"
"ds_read_b64_tr_b16 v[28:29], v4 offset:8200\n"
"v_accvgpr_write_b32 a65, 0\n"
"v_mfma_f32_16x16x32_bf16 v[136:139], v[16:19], a[144:147], v[136:139]\n"
"ds_read_b64_tr_b16 v[30:31], v4 offset:8456\n"
"v_mfma_f32_16x16x32_bf16 v[136:139], v[20:23], a[148:151], v[136:139]\n"
"s_mov_b32 s35, 0x3000\n"
"buffer_load_dwordx4 a[192:195], v12, s[20:23], s35 offen offset:0 sc1\n"
"v_mfma_f32_16x16x32_bf16 v[140:143], v[20:23], a[156:159], v[140:143]\n"
"v_mfma_f32_16x16x32_bf16 v[140:143], v[16:19], a[152:155], v[140:143]\n"
"s_waitcnt lgkmcnt(0)\n"
"v_mfma_f32_16x16x32_bf16 v[144:147], v[24:27], a[128:131], v[144:147]\n"
"v_mfma_f32_16x16x32_bf16 v[144:147], v[28:31], a[132:135], v[144:147]\n"
"ds_read_b64_tr_b16 v[16:17], v4 offset:512\n"
"v_accvgpr_write_b32 a66, 0\n"
"v_mfma_f32_16x16x32_bf16 v[148:151], v[28:31], a[140:143], v[148:151]\n"
"ds_read_b64_tr_b16 v[18:19], v4 offset:768\n"
"v_mfma_f32_16x16x32_bf16 v[148:151], v[24:27], a[136:139], v[148:151]\n"
"ds_read_b64_tr_b16 v[20:21], v4 offset:8704\n"
"v_accvgpr_write_b32 a67, 0\n"
"v_mfma_f32_16x16x32_bf16 v[152:155], v[24:27], a[144:147], v[152:155]\n"
"ds_read_b64_tr_b16 v[22:23], v4 offset:8960\n"
"v_mfma_f32_16x16x32_bf16 v[152:155], v[28:31], a[148:151], v[152:155]\n"
"buffer_load_dwordx4 a[196:199], v12, s[20:23], s35 offen offset:1024 sc1\n"
"v_mfma_f32_16x16x32_bf16 v[156:159], v[28:31], a[156:159], v[156:159]\n"
"s_mov_b32 s35, 0x8000\n"
"buffer_load_dwordx4 a[200:203], v12, s[20:23], s35 offen offset:0 sc1\n"
"v_mfma_f32_16x16x32_bf16 v[156:159], v[24:27], a[152:155], v[156:159]\n"
"s_waitcnt lgkmcnt(0)\n"
"v_mfma_f32_16x16x32_bf16 v[160:163], v[16:19], a[128:131], v[160:163]\n"
"v_mfma_f32_16x16x32_bf16 v[160:163], v[20:23], a[132:135], v[160:163]\n"
"ds_read_b64_tr_b16 v[24:25], v4 offset:520\n"
"v_accvgpr_write_b32 a68, 0\n"
"v_mfma_f32_16x16x32_bf16 v[164:167], v[20:23], a[140:143], v[164:167]\n"
"ds_read_b64_tr_b16 v[26:27], v4 offset:776\n"
"v_mfma_f32_16x16x32_bf16 v[164:167], v[16:19], a[136:139], v[164:167]\n"
"ds_read_b64_tr_b16 v[28:29], v4 offset:8712\n"
"v_accvgpr_write_b32 a69, 0\n"
"v_mfma_f32_16x16x32_bf16 v[168:171], v[16:19], a[144:147], v[168:171]\n"
"ds_read_b64_tr_b16 v[30:31], v4 offset:8968\n"
"v_mfma_f32_16x16x32_bf16 v[168:171], v[20:23], a[148:151], v[168:171]\n"
"buffer_load_dwordx4 a[204:207], v12, s[20:23], s35 offen offset:1024 sc1\n"
"v_mfma_f32_16x16x32_bf16 v[172:175], v[20:23], a[156:159], v[172:175]\n"
"v_mfma_f32_16x16x32_bf16 v[172:175], v[16:19], a[152:155], v[172:175]\n"
"s_waitcnt lgkmcnt(0)\n"
"v_mfma_f32_16x16x32_bf16 v[176:179], v[24:27], a[128:131], v[176:179]\n"
"v_mfma_f32_16x16x32_bf16 v[176:179], v[28:31], a[132:135], v[176:179]\n"
"ds_read_b64_tr_b16 v[16:17], v4 offset:1024\n"
"v_accvgpr_write_b32 a70, 0\n"
"v_mfma_f32_16x16x32_bf16 v[180:183], v[28:31], a[140:143], v[180:183]\n"
"ds_read_b64_tr_b16 v[18:19], v4 offset:1280\n"
"v_mfma_f32_16x16x32_bf16 v[180:183], v[24:27], a[136:139], v[180:183]\n"
"ds_read_b64_tr_b16 v[20:21], v4 offset:9216\n"
"v_accvgpr_write_b32 a71, 0\n"
"v_mfma_f32_16x16x32_bf16 v[184:187], v[24:27], a[144:147], v[184:187]\n"
"ds_read_b64_tr_b16 v[22:23], v4 offset:9472\n"
"v_mfma_f32_16x16x32_bf16 v[184:187], v[28:31], a[148:151], v[184:187]\n"
"s_mov_b32 s35, 0xd000\n"
"buffer_load_dwordx4 a[208:211], v12, s[20:23], s35 offen offset:0 sc1\n"
"v_mfma_f32_16x16x32_bf16 v[188:191], v[28:31], a[156:159], v[188:191]\n"
"buffer_load_dwordx4 a[212:215], v12, s[20:23], s35 offen offset:1024 sc1\n"
"v_mfma_f32_16x16x32_bf16 v[188:191], v[24:27], a[152:155], v[188:191]\n"
"s_waitcnt lgkmcnt(0)\n"
"v_mfma_f32_16x16x32_bf16 v[192:195], v[16:19], a[128:131], v[192:195]\n"
"v_mfma_f32_16x16x32_bf16 v[192:195], v[20:23], a[132:135], v[192:195]\n"
"ds_read_b64_tr_b16 v[24:25], v4 offset:1032\n"
"v_accvgpr_write_b32 a72, 0\n"
"v_mfma_f32_16x16x32_bf16 v[196:199], v[20:23], a[140:143], v[196:199]\n"
"ds_read_b64_tr_b16 v[26:27], v4 offset:1288\n"
"v_mfma_f32_16x16x32_bf16 v[196:199], v[16:19], a[136:139], v[196:199]\n"
"ds_read_b64_tr_b16 v[28:29], v4 offset:9224\n"
"v_accvgpr_write_b32 a73, 0\n"
"v_mfma_f32_16x16x32_bf16 v[200:203], v[16:19], a[144:147], v[200:203]\n"
"ds_read_b64_tr_b16 v[30:31], v4 offset:9480\n"
"v_mfma_f32_16x16x32_bf16 v[200:203], v[20:23], a[148:151], v[200:203]\n"
"s_mov_b32 s35, 0x12000\n"
"buffer_load_dwordx4 a[216:219], v12, s[20:23], s35 offen offset:0 sc1\n"
"v_mfma_f32_16x16x32_bf16 v[204:207], v[20:23], a[156:159], v[204:207]\n"
"v_mfma_f32_16x16x32_bf16 v[204:207], v[16:19], a[152:155], v[204:207]\n"
"s_waitcnt lgkmcnt(0)\n"
"v_mfma_f32_16x16x32_bf16 v[208:211], v[24:27], a[128:131], v[208:211]\n"
"v_mfma_f32_16x16x32_bf16 v[208:211], v[28:31], a[132:135], v[208:211]\n"
"ds_read_b64_tr_b16 v[16:17], v4 offset:1536\n"
"v_accvgpr_write_b32 a74, 0\n"
"v_mfma_f32_16x16x32_bf16 v[212:215], v[28:31], a[140:143], v[212:215]\n"
"ds_read_b64_tr_b16 v[18:19], v4 offset:1792\n"
"v_mfma_f32_16x16x32_bf16 v[212:215], v[24:27], a[136:139], v[212:215]\n"
"ds_read_b64_tr_b16 v[20:21], v4 offset:9728\n"
"v_accvgpr_write_b32 a75, 0\n"
"v_mfma_f32_16x16x32_bf16 v[216:219], v[24:27], a[144:147], v[216:219]\n"
"ds_read_b64_tr_b16 v[22:23], v4 offset:9984\n"
"v_mfma_f32_16x16x32_bf16 v[216:219], v[28:31], a[148:151], v[216:219]\n"
"buffer_load_dword v15, v63, s[64:67], s68 offen\n"
"s_add_u32 s68, s68, s69\n"
"v_mfma_f32_16x16x32_bf16 v[220:223], v[28:31], a[156:159], v[220:223]\n"
"buffer_load_dwordx4 a[220:223], v12, s[20:23], s35 offen offset:1024 sc1\n"
"v_mfma_f32_16x16x32_bf16 v[220:223], v[24:27], a[152:155], v[220:223]\n"
"s_waitcnt lgkmcnt(0)\n"
"v_mfma_f32_16x16x32_bf16 v[224:227], v[16:19], a[128:131], v[224:227]\n"
"v_mfma_f32_16x16x32_bf16 v[224:227], v[20:23], a[132:135], v[224:227]\n"
"ds_read_b64_tr_b16 v[24:25], v4 offset:1544\n"
"v_accvgpr_write_b32 a76, 0\n"
"v_mfma_f32_16x16x32_bf16 v[228:231], v[20:23], a[140:143], v[228:231]\n"
"ds_read_b64_tr_b16 v[26:27], v4 offset:1800\n"
"v_mfma_f32_16x16x32_bf16 v[228:231], v[16:19], a[136:139], v[228:231]\n"
"ds_read_b64_tr_b16 v[28:29], v4 offset:9736\n"
"v_accvgpr_write_b32 a77, 0\n"
"v_mfma_f32_16x16x32_bf16 v[232:235], v[16:19], a[144:147], v[232:235]\n"
"ds_read_b64_tr_b16 v[30:31], v4 offset:9992\n"
"v_mfma_f32_16x16x32_bf16 v[232:235], v[20:23], a[148:151], v[232:235]\n"
"s_add_u32 m0, s29, 0x18000\n"
"s_mov_b32 s31, 0x300000\n"
"buffer_load_dwordx4 v11, s[16:19], s31 offen lds\n"
"v_mfma_f32_16x16x32_bf16 v[236:239], v[20:23], a[156:159], v[236:239]\n"
"v_mfma_f32_16x16x32_bf16 v[236:239], v[16:19], a[152:155], v[236:239]\n"
"s_waitcnt lgkmcnt(0)\n"
"v_mfma_f32_16x16x32_bf16 v[240:243], v[24:27], a[128:131], v[240:243]\n"
"s_waitcnt vmcnt(10)\n"
"s_barrier\n"
"v_mfma_f32_16x16x32_bf16 v[240:243], v[28:31], a[132:135], v[240:243]\n"
"ds_read_b64_tr_b16 v[16:17], v4 offset:16384\n"
"v_accvgpr_write_b32 a78, 0\n"
"v_mfma_f32_16x16x32_bf16 v[244:247], v[28:31], a[140:143], v[244:247]\n"
"ds_read_b64_tr_b16 v[18:19], v4 offset:16640\n"
"v_mfma_f32_16x16x32_bf16 v[244:247], v[24:27], a[136:139], v[244:247]\n"
"ds_read_b64_tr_b16 v[20:21], v4 offset:24576\n"
"v_accvgpr_write_b32 a79, 0\n"
"v_mfma_f32_16x16x32_bf16 v[248:251], v[24:27], a[144:147], v[248:251]\n"
"ds_read_b64_tr_b16 v[22:23], v4 offset:24832\n"
"v_mfma_f32_16x16x32_bf16 v[248:251], v[28:31], a[148:151], v[248:251]\n"
"s_add_u32 m0, s29, 0x18800\n"
"s_mov_b32 s31, 0x310000\n"
"buffer_load_dwordx4 v11, s[16:19], s31 offen lds\n"
"v_mfma_f32_16x16x32_bf16 v[252:255], v[28:31], a[156:159], v[252:255]\n"
"s_add_u32 m0, s29, 0x19000\n"
"s_mov_b32 s31, 0x320000\n"
"buffer_load_dwordx4 v11, s[16:19], s31 offen lds\n"
"v_mfma_f32_16x16x32_bf16 v[252:255], v[24:27], a[152:155], v[252:255]\n"
"s_waitcnt vmcnt(16) lgkmcnt(0)\n"
"v_mfma_f32_16x16x32_bf16 v[128:131], v[16:19], a[160:163], v[128:131]\n"
"s_add_u32 m0, s29, 0x19800\n"
"s_mov_b32 s31, 0x330000\n"
"buffer_load_dwordx4 v11, s[16:19], s31 offen lds\n"
"v_mfma_f32_16x16x32_bf16 v[128:131], v[20:23], a[164:167], v[128:131]\n"
"ds_read_b64_tr_b16 v[24:25], v4 offset:16392\n"
"v_accvgpr_write_b32 a80, 0\n"
"v_mfma_f32_16x16x32_bf16 v[132:135], v[20:23], a[172:175], v[132:135]\n"
"ds_read_b64_tr_b16 v[26:27], v4 offset:16648\n"
"v_mfma_f32_16x16x32_bf16 v[132:135], v[16:19], a[168:171], v[132:135]\n"
"ds_read_b64_tr_b16 v[28:29], v4 offset:24584\n"
"v_accvgpr_write_b32 a81, 0\n"
"v_mfma_f32_16x16x32_bf16 v[136:139], v[16:19], a[176:179], v[136:139]\n"
"ds_read_b64_tr_b16 v[30:31], v4 offset:24840\n"
"v_mfma_f32_16x16x32_bf16 v[136:139], v[20:23], a[180:183], v[136:139]\n"
"s_mov_b32 s35, 0x3800\n"
"buffer_load_dwordx4 a[224:227], v12, s[20:23], s35 offen offset:0 sc1\n"
"v_mfma_f32_16x16x32_bf16 v[140:143], v[20:23], a[188:191], v[140:143]\n"
"v_mfma_f32_16x16x32_bf16 v[140:143], v[16:19], a[184:187], v[140:143]\n"
"s_waitcnt lgkmcnt(0)\n"
"v_mfma_f32_16x16x32_bf16 v[144:147], v[24:27], a[160:163], v[144:147]\n"
"v_mfma_f32_16x16x32_bf16 v[144:147], v[28:31], a[164:167], v[144:147]\n"
"ds_read_b64_tr_b16 v[16:17], v4 offset:16896\n"
"v_accvgpr_write_b32 a82, 0\n"
"v_mfma_f32_16x16x32_bf16 v[148:151], v[28:31], a[172:175], v[148:151]\n"
"ds_read_b64_tr_b16 v[18:19], v4 offset:17152\n"
"v_mfma_f32_16x16x32_bf16 v[148:151], v[24:27], a[168:171], v[148:151]\n"
"ds_read_b64_tr_b16 v[20:21], v4 offset:25088\n"
"v_accvgpr_write_b32 a83, 0\n"
"v_mfma_f32_16x16x32_bf16 v[152:155], v[24:27], a[176:179], v[152:155]\n"
"ds_read_b64_tr_b16 v[22:23], v4 offset:25344\n"
"v_mfma_f32_16x16x32_bf16 v[152:155], v[28:31], a[180:183], v[152:155]\n"
"buffer_load_dwordx4 a[228:231], v12, s[20:23], s35 offen offset:1024 sc1\n"
"v_mfma_f32_16x16x32_bf16 v[156:159], v[28:31], a[188:191], v[156:159]\n"
"s_mov_b32 s35, 0x8800\n"
"buffer_load_dwordx4 a[232:235], v12, s[20:23], s35 offen offset:0 sc1\n"
"v_mfma_f32_16x16x32_bf16 v[156:159], v[24:27], a[184:187], v[156:159]\n"
"s_waitcnt lgkmcnt(0)\n"
"v_mfma_f32_16x16x32_bf16 v[160:163], v[16:19], a[160:163], v[160:163]\n"
"v_mfma_f32_16x16x32_bf16 v[160:163], v[20:23], a[164:167], v[160:163]\n"
"ds_read_b64_tr_b16 v[24:25], v4 offset:16904\n"
"v_accvgpr_write_b32 a84, 0\n"
"v_mfma_f32_16x16x32_bf16 v[164:167], v[20:23], a[172:175], v[164:167]\n"
"ds_read_b64_tr_b16 v[26:27], v4 offset:17160\n"
"v_mfma_f32_16x16x32_bf16 v[164:167], v[16:19], a[168:171], v[164:167]\n"
"ds_read_b64_tr_b16 v[28:29], v4 offset:25096\n"
"v_accvgpr_write_b32 a85, 0\n"
"v_mfma_f32_16x16x32_bf16 v[168:171], v[16:19], a[176:179], v[168:171]\n"
"ds_read_b64_tr_b16 v[30:31], v4 offset:25352\n"
"v_mfma_f32_16x16x32_bf16 v[168:171], v[20:23], a[180:183], v[168:171]\n"
"buffer_load_dwordx4 a[236:239], v12, s[20:23], s35 offen offset:1024 sc1\n"
"v_mfma_f32_16x16x32_bf16 v[172:175], v[20:23], a[188:191], v[172:175]\n"
"v_mfma_f32_16x16x32_bf16 v[172:175], v[16:19], a[184:187], v[172:175]\n"
"s_waitcnt lgkmcnt(0)\n"
"v_mfma_f32_16x16x32_bf16 v[176:179], v[24:27], a[160:163], v[176:179]\n"
"v_mfma_f32_16x16x32_bf16 v[176:179], v[28:31], a[164:167], v[176:179]\n"
"ds_read_b64_tr_b16 v[16:17], v4 offset:17408\n"
"v_accvgpr_write_b32 a86, 0\n"
"v_mfma_f32_16x16x32_bf16 v[180:183], v[28:31], a[172:175], v[180:183]\n"
"ds_read_b64_tr_b16 v[18:19], v4 offset:17664\n"
"v_mfma_f32_16x16x32_bf16 v[180:183], v[24:27], a[168:171], v[180:183]\n"
"ds_read_b64_tr_b16 v[20:21], v4 offset:25600\n"
"v_accvgpr_write_b32 a87, 0\n"
"v_mfma_f32_16x16x32_bf16 v[184:187], v[24:27], a[176:179], v[184:187]\n"
"ds_read_b64_tr_b16 v[22:23], v4 offset:25856\n"
"v_mfma_f32_16x16x32_bf16 v[184:187], v[28:31], a[180:183], v[184:187]\n"
"s_mov_b32 s35, 0xd800\n"
"buffer_load_dwordx4 a[240:243], v12, s[20:23], s35 offen offset:0 sc1\n"
"v_mfma_f32_16x16x32_bf16 v[188:191], v[28:31], a[188:191], v[188:191]\n"
"buffer_load_dwordx4 a[244:247], v12, s[20:23], s35 offen offset:1024 sc1\n"
"v_mfma_f32_16x16x32_bf16 v[188:191], v[24:27], a[184:187], v[188:191]\n"
"s_waitcnt lgkmcnt(0)\n"
"v_mfma_f32_16x16x32_bf16 v[192:195], v[16:19], a[160:163], v[192:195]\n"
"v_mfma_f32_16x16x32_bf16 v[192:195], v[20:23], a[164:167], v[192:195]\n"
"ds_read_b64_tr_b16 v[24:25], v4 offset:17416\n"
"v_accvgpr_write_b32 a88, 0\n"
"v_mfma_f32_16x16x32_bf16 v[196:199], v[20:23], a[172:175], v[196:199]\n"
"ds_read_b64_tr_b16 v[26:27], v4 offset:17672\n"
"v_mfma_f32_16x16x32_bf16 v[196:199], v[16:19], a[168:171], v[196:199]\n"
"ds_read_b64_tr_b16 v[28:29], v4 offset:25608\n"
"v_accvgpr_write_b32 a89, 0\n"
"v_mfma_f32_16x16x32_bf16 v[200:203], v[16:19], a[176:179], v[200:203]\n"
"ds_read_b64_tr_b16 v[30:31], v4 offset:25864\n"
"v_mfma_f32_16x16x32_bf16 v[200:203], v[20:23], a[180:183], v[200:203]\n"
"s_mov_b32 s35, 0x12800\n"
"buffer_load_dwordx4 a[248:251], v12, s[20:23], s35 offen offset:0 sc1\n"
"v_mfma_f32_16x16x32_bf16 v[204:207], v[20:23], a[188:191], v[204:207]\n"
"v_mfma_f32_16x16x32_bf16 v[204:207], v[16:19], a[184:187], v[204:207]\n"
"s_waitcnt lgkmcnt(0)\n"
"v_mfma_f32_16x16x32_bf16 v[208:211], v[24:27], a[160:163], v[208:211]\n"
"v_mfma_f32_16x16x32_bf16 v[208:211], v[28:31], a[164:167], v[208:211]\n"
"ds_read_b64_tr_b16 v[16:17], v4 offset:17920\n"
"v_accvgpr_write_b32 a90, 0\n"
"v_mfma_f32_16x16x32_bf16 v[212:215], v[28:31], a[172:175], v[212:215]\n"
"ds_read_b64_tr_b16 v[18:19], v4 offset:18176\n"
"v_mfma_f32_16x16x32_bf16 v[212:215], v[24:27], a[168:171], v[212:215]\n"
"ds_read_b64_tr_b16 v[20:21], v4 offset:26112\n"
"v_accvgpr_write_b32 a91, 0\n"
"v_mfma_f32_16x16x32_bf16 v[216:219], v[24:27], a[176:179], v[216:219]\n"
"ds_read_b64_tr_b16 v[22:23], v4 offset:26368\n"
"v_mfma_f32_16x16x32_bf16 v[216:219], v[28:31], a[180:183], v[216:219]\n"
"buffer_load_dwordx4 a[252:255], v12, s[20:23], s35 offen offset:1024 sc1\n"
"v_mfma_f32_16x16x32_bf16 v[220:223], v[28:31], a[188:191], v[220:223]\n"
"s_add_u32 m0, s29, 0x1c000\n"
"s_mov_b32 s31, 0x380000\n"
"buffer_load_dwordx4 v11, s[16:19], s31 offen lds\n"
"v_mfma_f32_16x16x32_bf16 v[220:223], v[24:27], a[184:187], v[220:223]\n"
"s_waitcnt lgkmcnt(0)\n"
"v_mfma_f32_16x16x32_bf16 v[224:227], v[16:19], a[160:163], v[224:227]\n"
"v_mfma_f32_16x16x32_bf16 v[224:227], v[20:23], a[164:167], v[224:227]\n"
"ds_read_b64_tr_b16 v[24:25], v4 offset:17928\n"
"v_accvgpr_write_b32 a92, 0\n"
"v_mfma_f32_16x16x32_bf16 v[228:231], v[20:23], a[172:175], v[228:231]\n"
"ds_read_b64_tr_b16 v[26:27], v4 offset:18184\n"
"v_mfma_f32_16x16x32_bf16 v[228:231], v[16:19], a[168:171], v[228:231]\n"
"ds_read_b64_tr_b16 v[28:29], v4 offset:26120\n"
"v_accvgpr_write_b32 a93, 0\n"
"v_mfma_f32_16x16x32_bf16 v[232:235], v[16:19], a[176:179], v[232:235]\n"
"ds_read_b64_tr_b16 v[30:31], v4 offset:26376\n"
"v_mfma_f32_16x16x32_bf16 v[232:235], v[20:23], a[180:183], v[232:235]\n"
"s_add_u32 m0, s29, 0x1c800\n"
"s_mov_b32 s31, 0x390000\n"
"buffer_load_dwordx4 v11, s[16:19], s31 offen lds\n"
"v_mfma_f32_16x16x32_bf16 v[236:239], v[20:23], a[188:191], v[236:239]\n"
"v_mfma_f32_16x16x32_bf16 v[236:239], v[16:19], a[184:187], v[236:239]\n"
"s_waitcnt lgkmcnt(0)\n"
"v_mfma_f32_16x16x32_bf16 v[240:243], v[24:27], a[160:163], v[240:243]\n"
"s_waitcnt vmcnt(10)\n"
"s_barrier\n"
"v_mfma_f32_16x16x32_bf16 v[240:243], v[28:31], a[164:167], v[240:243]\n"
"ds_read_b64_tr_b16 v[16:17], v4 offset:32768\n"
"v_accvgpr_write_b32 a94, 0\n"
"v_mfma_f32_16x16x32_bf16 v[244:247], v[28:31], a[172:175], v[244:247]\n"
"ds_read_b64_tr_b16 v[18:19], v4 offset:33024\n"
"v_mfma_f32_16x16x32_bf16 v[244:247], v[24:27], a[168:171], v[244:247]\n"
"ds_read_b64_tr_b16 v[20:21], v4 offset:40960\n"
"v_accvgpr_write_b32 a95, 0\n"
"v_mfma_f32_16x16x32_bf16 v[248:251], v[24:27], a[176:179], v[248:251]\n"
"ds_read_b64_tr_b16 v[22:23], v4 offset:41216\n"
"v_mfma_f32_16x16x32_bf16 v[248:251], v[28:31], a[180:183], v[248:251]\n"
"s_add_u32 m0, s29, 0x1d000\n"
"s_mov_b32 s31, 0x3a0000\n"
"buffer_load_dwordx4 v11, s[16:19], s31 offen lds\n"
"v_mfma_f32_16x16x32_bf16 v[252:255], v[28:31], a[188:191], v[252:255]\n"
"s_add_u32 m0, s29, 0x1d800\n"
"s_mov_b32 s31, 0x3b0000\n"
"buffer_load_dwordx4 v11, s[16:19], s31 offen lds\n"
"v_mfma_f32_16x16x32_bf16 v[252:255], v[24:27], a[184:187], v[252:255]\n"
"s_waitcnt vmcnt(16) lgkmcnt(0)\n"
"v_mfma_f32_16x16x32_bf16 v[128:131], v[16:19], a[192:195], v[128:131]\n"
"v_mfma_f32_16x16x32_bf16 v[128:131], v[20:23], a[196:199], v[128:131]\n"
"ds_read_b64_tr_b16 v[24:25], v4 offset:32776\n"
"v_accvgpr_write_b32 a96, 0\n"
"v_mfma_f32_16x16x32_bf16 v[132:135], v[20:23], a[204:207], v[132:135]\n"
"ds_read_b64_tr_b16 v[26:27], v4 offset:33032\n"
"v_mfma_f32_16x16x32_bf16 v[132:135], v[16:19], a[200:203], v[132:135]\n"
"ds_read_b64_tr_b16 v[28:29], v4 offset:40968\n"
"v_accvgpr_write_b32 a97, 0\n"
"v_mfma_f32_16x16x32_bf16 v[136:139], v[16:19], a[208:211], v[136:139]\n"
"ds_read_b64_tr_b16 v[30:31], v4 offset:41224\n"
"v_mfma_f32_16x16x32_bf16 v[136:139], v[20:23], a[212:215], v[136:139]\n"
"s_mov_b32 s35, 0x4000\n"
"buffer_load_dwordx4 a[128:131], v12, s[20:23], s35 offen offset:0 sc1\n"
"v_mfma_f32_16x16x32_bf16 v[140:143], v[20:23], a[220:223], v[140:143]\n"
"v_mfma_f32_16x16x32_bf16 v[140:143], v[16:19], a[216:219], v[140:143]\n"
"s_waitcnt lgkmcnt(0)\n"
"v_mfma_f32_16x16x32_bf16 v[144:147], v[24:27], a[192:195], v[144:147]\n"
"v_mfma_f32_16x16x32_bf16 v[144:147], v[28:31], a[196:199], v[144:147]\n"
"ds_read_b64_tr_b16 v[16:17], v4 offset:33280\n"
"v_accvgpr_write_b32 a98, 0\n"
"v_mfma_f32_16x16x32_bf16 v[148:151], v[28:31], a[204:207], v[148:151]\n"
"ds_read_b64_tr_b16 v[18:19], v4 offset:33536\n"
"v_mfma_f32_16x16x32_bf16 v[148:151], v[24:27], a[200:203], v[148:151]\n"
"ds_read_b64_tr_b16 v[20:21], v4 offset:41472\n"
"v_accvgpr_write_b32 a99, 0\n"
"v_mfma_f32_16x16x32_bf16 v[152:155], v[24:27], a[208:211], v[152:155]\n"
"ds_read_b64_tr_b16 v[22:23], v4 offset:41728\n"
"v_mfma_f32_16x16x32_bf16 v[152:155], v[28:31], a[212:215], v[152:155]\n"
"buffer_load_dwordx4 a[132:135], v12, s[20:23], s35 offen offset:1024 sc1\n"
"v_mfma_f32_16x16x32_bf16 v[156:159], v[28:31], a[220:223], v[156:159]\n"
"s_mov_b32 s35, 0x9000\n"
"buffer_load_dwordx4 a[136:139], v12, s[20:23], s35 offen offset:0 sc1\n"
"v_mfma_f32_16x16x32_bf16 v[156:159], v[24:27], a[216:219], v[156:159]\n"
"s_waitcnt lgkmcnt(0)\n"
"v_mfma_f32_16x16x32_bf16 v[160:163], v[16:19], a[192:195], v[160:163]\n"
"v_mfma_f32_16x16x32_bf16 v[160:163], v[20:23], a[196:199], v[160:163]\n"
"ds_read_b64_tr_b16 v[24:25], v4 offset:33288\n"
"v_accvgpr_write_b32 a100, 0\n"
"v_mfma_f32_16x16x32_bf16 v[164:167], v[20:23], a[204:207], v[164:167]\n"
"ds_read_b64_tr_b16 v[26:27], v4 offset:33544\n"
"v_mfma_f32_16x16x32_bf16 v[164:167], v[16:19], a[200:203], v[164:167]\n"
"ds_read_b64_tr_b16 v[28:29], v4 offset:41480\n"
"v_accvgpr_write_b32 a101, 0\n"
"v_mfma_f32_16x16x32_bf16 v[168:171], v[16:19], a[208:211], v[168:171]\n"
"ds_read_b64_tr_b16 v[30:31], v4 offset:41736\n"
"v_mfma_f32_16x16x32_bf16 v[168:171], v[20:23], a[212:215], v[168:171]\n"
"buffer_load_dwordx4 a[140:143], v12, s[20:23], s35 offen offset:1024 sc1\n"
"v_mfma_f32_16x16x32_bf16 v[172:175], v[20:23], a[220:223], v[172:175]\n"
"v_mfma_f32_16x16x32_bf16 v[172:175], v[16:19], a[216:219], v[172:175]\n"
"s_waitcnt lgkmcnt(0)\n"
"v_mfma_f32_16x16x32_bf16 v[176:179], v[24:27], a[192:195], v[176:179]\n"
"v_mfma_f32_16x16x32_bf16 v[176:179], v[28:31], a[196:199], v[176:179]\n"
"ds_read_b64_tr_b16 v[16:17], v4 offset:33792\n"
"v_accvgpr_write_b32 a102, 0\n"
"v_mfma_f32_16x16x32_bf16 v[180:183], v[28:31], a[204:207], v[180:183]\n"
"ds_read_b64_tr_b16 v[18:19], v4 offset:34048\n"
"v_mfma_f32_16x16x32_bf16 v[180:183], v[24:27], a[200:203], v[180:183]\n"
"ds_read_b64_tr_b16 v[20:21], v4 offset:41984\n"
"v_accvgpr_write_b32 a103, 0\n"
"v_mfma_f32_16x16x32_bf16 v[184:187], v[24:27], a[208:211], v[184:187]\n"
"ds_read_b64_tr_b16 v[22:23], v4 offset:42240\n"
"v_mfma_f32_16x16x32_bf16 v[184:187], v[28:31], a[212:215], v[184:187]\n"
"s_mov_b32 s35, 0xe000\n"
"buffer_load_dwordx4 a[144:147], v12, s[20:23], s35 offen offset:0 sc1\n"
"v_mfma_f32_16x16x32_bf16 v[188:191], v[28:31], a[220:223], v[188:191]\n"
"buffer_load_dwordx4 a[148:151], v12, s[20:23], s35 offen offset:1024 sc1\n"
"v_mfma_f32_16x16x32_bf16 v[188:191], v[24:27], a[216:219], v[188:191]\n"
"s_waitcnt lgkmcnt(0)\n"
"v_mfma_f32_16x16x32_bf16 v[192:195], v[16:19], a[192:195], v[192:195]\n"
"v_mfma_f32_16x16x32_bf16 v[192:195], v[20:23], a[196:199], v[192:195]\n"
"ds_read_b64_tr_b16 v[24:25], v4 offset:33800\n"
"v_accvgpr_write_b32 a104, 0\n"
"v_mfma_f32_16x16x32_bf16 v[196:199], v[20:23], a[204:207], v[196:199]\n"
"ds_read_b64_tr_b16 v[26:27], v4 offset:34056\n"
"v_mfma_f32_16x16x32_bf16 v[196:199], v[16:19], a[200:203], v[196:199]\n"
"ds_read_b64_tr_b16 v[28:29], v4 offset:41992\n"
"v_accvgpr_write_b32 a105, 0\n"
"v_mfma_f32_16x16x32_bf16 v[200:203], v[16:19], a[208:211], v[200:203]\n"
"ds_read_b64_tr_b16 v[30:31], v4 offset:42248\n"
"v_mfma_f32_16x16x32_bf16 v[200:203], v[20:23], a[212:215], v[200:203]\n"
"s_mov_b32 s35, 0x13000\n"
"buffer_load_dwordx4 a[152:155], v12, s[20:23], s35 offen offset:0 sc1\n"
"v_mfma_f32_16x16x32_bf16 v[204:207], v[20:23], a[220:223], v[204:207]\n"
"v_mfma_f32_16x16x32_bf16 v[204:207], v[16:19], a[216:219], v[204:207]\n"
"s_waitcnt lgkmcnt(0)\n"
"v_mfma_f32_16x16x32_bf16 v[208:211], v[24:27], a[192:195], v[208:211]\n"
"v_mfma_f32_16x16x32_bf16 v[208:211], v[28:31], a[196:199], v[208:211]\n"
"ds_read_b64_tr_b16 v[16:17], v4 offset:34304\n"
"v_accvgpr_write_b32 a106, 0\n"
"v_mfma_f32_16x16x32_bf16 v[212:215], v[28:31], a[204:207], v[212:215]\n"
"ds_read_b64_tr_b16 v[18:19], v4 offset:34560\n"
"v_mfma_f32_16x16x32_bf16 v[212:215], v[24:27], a[200:203], v[212:215]\n"
"ds_read_b64_tr_b16 v[20:21], v4 offset:42496\n"
"v_accvgpr_write_b32 a107, 0\n"
"v_mfma_f32_16x16x32_bf16 v[216:219], v[24:27], a[208:211], v[216:219]\n"
"ds_read_b64_tr_b16 v[22:23], v4 offset:42752\n"
"v_mfma_f32_16x16x32_bf16 v[216:219], v[28:31], a[212:215], v[216:219]\n"
"buffer_load_dword v15, v63, s[64:67], s68 offen\n"
"s_add_u32 s68, s68, s69\n"
"v_mfma_f32_16x16x32_bf16 v[220:223], v[28:31], a[220:223], v[220:223]\n"
"buffer_load_dwordx4 a[156:159], v12, s[20:23], s35 offen offset:1024 sc1\n"
"v_mfma_f32_16x16x32_bf16 v[220:223], v[24:27], a[216:219], v[220:223]\n"
"s_waitcnt lgkmcnt(0)\n"
"v_mfma_f32_16x16x32_bf16 v[224:227], v[16:19], a[192:195], v[224:227]\n"
"v_mfma_f32_16x16x32_bf16 v[224:227], v[20:23], a[196:199], v[224:227]\n"
"ds_read_b64_tr_b16 v[24:25], v4 offset:34312\n"
"v_accvgpr_write_b32 a108, 0\n"
"v_mfma_f32_16x16x32_bf16 v[228:231], v[20:23], a[204:207], v[228:231]\n"
"ds_read_b64_tr_b16 v[26:27], v4 offset:34568\n"
"v_mfma_f32_16x16x32_bf16 v[228:231], v[16:19], a[200:203], v[228:231]\n"
"ds_read_b64_tr_b16 v[28:29], v4 offset:42504\n"
"v_accvgpr_write_b32 a109, 0\n"
"v_mfma_f32_16x16x32_bf16 v[232:235], v[16:19], a[208:211], v[232:235]\n"
"ds_read_b64_tr_b16 v[30:31], v4 offset:42760\n"
"v_mfma_f32_16x16x32_bf16 v[232:235], v[20:23], a[212:215], v[232:235]\n"
"s_add_u32 m0, s29, 0x20000\n"
"s_mov_b32 s31, 0x400000\n"
"buffer_load_dwordx4 v11, s[16:19], s31 offen lds\n"
"v_mfma_f32_16x16x32_bf16 v[236:239], v[20:23], a[220:223], v[236:239]\n"
"v_mfma_f32_16x16x32_bf16 v[236:239], v[16:19], a[216:219], v[236:239]\n"
"s_waitcnt lgkmcnt(0)\n"
"v_mfma_f32_16x16x32_bf16 v[240:243], v[24:27], a[192:195], v[240:243]\n"
"s_waitcnt vmcnt(10)\n"
"s_barrier\n"
"v_mfma_f32_16x16x32_bf16 v[240:243], v[28:31], a[196:199], v[240:243]\n"
"ds_read_b64_tr_b16 v[16:17], v4 offset:49152\n"
"v_accvgpr_write_b32 a110, 0\n"
"v_mfma_f32_16x16x32_bf16 v[244:247], v[28:31], a[204:207], v[244:247]\n"
"ds_read_b64_tr_b16 v[18:19], v4 offset:49408\n"
"v_mfma_f32_16x16x32_bf16 v[244:247], v[24:27], a[200:203], v[244:247]\n"
"ds_read_b64_tr_b16 v[20:21], v4 offset:57344\n"
"v_accvgpr_write_b32 a111, 0\n"
"v_mfma_f32_16x16x32_bf16 v[248:251], v[24:27], a[208:211], v[248:251]\n"
"ds_read_b64_tr_b16 v[22:23], v4 offset:57600\n"
"v_mfma_f32_16x16x32_bf16 v[248:251], v[28:31], a[212:215], v[248:251]\n"
"s_add_u32 m0, s29, 0x20800\n"
"s_mov_b32 s31, 0x410000\n"
"buffer_load_dwordx4 v11, s[16:19], s31 offen lds\n"
"v_mfma_f32_16x16x32_bf16 v[252:255], v[28:31], a[220:223], v[252:255]\n"
"s_add_u32 m0, s29, 0x21000\n"
"s_mov_b32 s31, 0x420000\n"
"buffer_load_dwordx4 v11, s[16:19], s31 offen lds\n"
"v_mfma_f32_16x16x32_bf16 v[252:255], v[24:27], a[216:219], v[252:255]\n"
"s_waitcnt vmcnt(16) lgkmcnt(0)\n"
"v_mfma_f32_16x16x32_bf16 v[128:131], v[16:19], a[224:227], v[128:131]\n"
"s_add_u32 m0, s29, 0x21800\n"
"s_mov_b32 s31, 0x430000\n"
"buffer_load_dwordx4 v11, s[16:19], s31 offen lds\n"
"v_mfma_f32_16x16x32_bf16 v[128:131], v[20:23], a[228:231], v[128:131]\n"
"ds_read_b64_tr_b16 v[24:25], v4 offset:49160\n"
"v_accvgpr_write_b32 a112, 0\n"
"v_mfma_f32_16x16x32_bf16 v[132:135], v[20:23], a[236:239], v[132:135]\n"
"ds_read_b64_tr_b16 v[26:27], v4 offset:49416\n"
"v_mfma_f32_16x16x32_bf16 v[132:135], v[16:19], a[232:235], v[132:135]\n"
"ds_read_b64_tr_b16 v[28:29], v4 offset:57352\n"
"v_accvgpr_write_b32 a113, 0\n"
"v_mfma_f32_16x16x32_bf16 v[136:139], v[16:19], a[240:243], v[136:139]\n"
"ds_read_b64_tr_b16 v[30:31], v4 offset:57608\n"
"v_mfma_f32_16x16x32_bf16 v[136:139], v[20:23], a[244:247], v[136:139]\n"
"s_mov_b32 s35, 0x4800\n"
"buffer_load_dwordx4 a[160:163], v12, s[20:23], s35 offen offset:0 sc1\n"
"v_mfma_f32_16x16x32_bf16 v[140:143], v[20:23], a[252:255], v[140:143]\n"
"v_mfma_f32_16x16x32_bf16 v[140:143], v[16:19], a[248:251], v[140:143]\n"
"s_waitcnt lgkmcnt(0)\n"
"v_mfma_f32_16x16x32_bf16 v[144:147], v[24:27], a[224:227], v[144:147]\n"
"v_mfma_f32_16x16x32_bf16 v[144:147], v[28:31], a[228:231], v[144:147]\n"
"ds_read_b64_tr_b16 v[16:17], v4 offset:49664\n"
"v_accvgpr_write_b32 a114, 0\n"
"v_mfma_f32_16x16x32_bf16 v[148:151], v[28:31], a[236:239], v[148:151]\n"
"ds_read_b64_tr_b16 v[18:19], v4 offset:49920\n"
"v_mfma_f32_16x16x32_bf16 v[148:151], v[24:27], a[232:235], v[148:151]\n"
"ds_read_b64_tr_b16 v[20:21], v4 offset:57856\n"
"v_accvgpr_write_b32 a115, 0\n"
"v_mfma_f32_16x16x32_bf16 v[152:155], v[24:27], a[240:243], v[152:155]\n"
"ds_read_b64_tr_b16 v[22:23], v4 offset:58112\n"
"v_mfma_f32_16x16x32_bf16 v[152:155], v[28:31], a[244:247], v[152:155]\n"
"buffer_load_dwordx4 a[164:167], v12, s[20:23], s35 offen offset:1024 sc1\n"
"v_mfma_f32_16x16x32_bf16 v[156:159], v[28:31], a[252:255], v[156:159]\n"
"s_mov_b32 s35, 0x9800\n"
"buffer_load_dwordx4 a[168:171], v12, s[20:23], s35 offen offset:0 sc1\n"
"v_mfma_f32_16x16x32_bf16 v[156:159], v[24:27], a[248:251], v[156:159]\n"
"s_waitcnt lgkmcnt(0)\n"
"v_mfma_f32_16x16x32_bf16 v[160:163], v[16:19], a[224:227], v[160:163]\n"
"v_mfma_f32_16x16x32_bf16 v[160:163], v[20:23], a[228:231], v[160:163]\n"
"ds_read_b64_tr_b16 v[24:25], v4 offset:49672\n"
"v_accvgpr_write_b32 a116, 0\n"
"v_mfma_f32_16x16x32_bf16 v[164:167], v[20:23], a[236:239], v[164:167]\n"
"ds_read_b64_tr_b16 v[26:27], v4 offset:49928\n"
"v_mfma_f32_16x16x32_bf16 v[164:167], v[16:19], a[232:235], v[164:167]\n"
"ds_read_b64_tr_b16 v[28:29], v4 offset:57864\n"
"v_accvgpr_write_b32 a117, 0\n"
"v_mfma_f32_16x16x32_bf16 v[168:171], v[16:19], a[240:243], v[168:171]\n"
"ds_read_b64_tr_b16 v[30:31], v4 offset:58120\n"
"v_mfma_f32_16x16x32_bf16 v[168:171], v[20:23], a[244:247], v[168:171]\n"
"buffer_load_dwordx4 a[172:175], v12, s[20:23], s35 offen offset:1024 sc1\n"
"v_mfma_f32_16x16x32_bf16 v[172:175], v[20:23], a[252:255], v[172:175]\n"
"v_mfma_f32_16x16x32_bf16 v[172:175], v[16:19], a[248:251], v[172:175]\n"
"s_waitcnt lgkmcnt(0)\n"
"v_mfma_f32_16x16x32_bf16 v[176:179], v[24:27], a[224:227], v[176:179]\n"
"v_mfma_f32_16x16x32_bf16 v[176:179], v[28:31], a[228:231], v[176:179]\n"
"ds_read_b64_tr_b16 v[16:17], v4 offset:50176\n"
"v_accvgpr_write_b32 a118, 0\n"
"v_mfma_f32_16x16x32_bf16 v[180:183], v[28:31], a[236:239], v[180:183]\n"
"ds_read_b64_tr_b16 v[18:19], v4 offset:50432\n"
"v_mfma_f32_16x16x32_bf16 v[180:183], v[24:27], a[232:235], v[180:183]\n"
"ds_read_b64_tr_b16 v[20:21], v4 offset:58368\n"
"v_accvgpr_write_b32 a119, 0\n"
"v_mfma_f32_16x16x32_bf16 v[184:187], v[24:27], a[240:243], v[184:187]\n"
"ds_read_b64_tr_b16 v[22:23], v4 offset:58624\n"
"v_mfma_f32_16x16x32_bf16 v[184:187], v[28:31], a[244:247], v[184:187]\n"
"s_mov_b32 s35, 0xe800\n"
"buffer_load_dwordx4 a[176:179], v12, s[20:23], s35 offen offset:0 sc1\n"
"v_mfma_f32_16x16x32_bf16 v[188:191], v[28:31], a[252:255], v[188:191]\n"
"buffer_load_dwordx4 a[180:183], v12, s[20:23], s35 offen offset:1024 sc1\n"
"v_mfma_f32_16x16x32_bf16 v[188:191], v[24:27], a[248:251], v[188:191]\n"
"s_waitcnt lgkmcnt(0)\n"
"v_mfma_f32_16x16x32_bf16 v[192:195], v[16:19], a[224:227], v[192:195]\n"
"v_mfma_f32_16x16x32_bf16 v[192:195], v[20:23], a[228:231], v[192:195]\n"
"ds_read_b64_tr_b16 v[24:25], v4 offset:50184\n"
"v_accvgpr_write_b32 a120, 0\n"
"v_mfma_f32_16x16x32_bf16 v[196:199], v[20:23], a[236:239], v[196:199]\n"
"ds_read_b64_tr_b16 v[26:27], v4 offset:50440\n"
"v_mfma_f32_16x16x32_bf16 v[196:199], v[16:19], a[232:235], v[196:199]\n"
"ds_read_b64_tr_b16 v[28:29], v4 offset:58376\n"
"v_accvgpr_write_b32 a121, 0\n"
"v_mfma_f32_16x16x32_bf16 v[200:203], v[16:19], a[240:243], v[200:203]\n"
"ds_read_b64_tr_b16 v[30:31], v4 offset:58632\n"
"v_mfma_f32_16x16x32_bf16 v[200:203], v[20:23], a[244:247], v[200:203]\n"
"s_mov_b32 s35, 0x13800\n"
"buffer_load_dwordx4 a[184:187], v12, s[20:23], s35 offen offset:0 sc1\n"
"v_mfma_f32_16x16x32_bf16 v[204:207], v[20:23], a[252:255], v[204:207]\n"
"v_mfma_f32_16x16x32_bf16 v[204:207], v[16:19], a[248:251], v[204:207]\n"
"s_waitcnt lgkmcnt(0)\n"
"v_mfma_f32_16x16x32_bf16 v[208:211], v[24:27], a[224:227], v[208:211]\n"
"v_mfma_f32_16x16x32_bf16 v[208:211], v[28:31], a[228:231], v[208:211]\n"
"ds_read_b64_tr_b16 v[16:17], v4 offset:50688\n"
"v_accvgpr_write_b32 a122, 0\n"
"v_mfma_f32_16x16x32_bf16 v[212:215], v[28:31], a[236:239], v[212:215]\n"
"ds_read_b64_tr_b16 v[18:19], v4 offset:50944\n"
"v_mfma_f32_16x16x32_bf16 v[212:215], v[24:27], a[232:235], v[212:215]\n"
"ds_read_b64_tr_b16 v[20:21], v4 offset:58880\n"
"v_accvgpr_write_b32 a123, 0\n"
"v_mfma_f32_16x16x32_bf16 v[216:219], v[24:27], a[240:243], v[216:219]\n"
"ds_read_b64_tr_b16 v[22:23], v4 offset:59136\n"
"v_mfma_f32_16x16x32_bf16 v[216:219], v[28:31], a[244:247], v[216:219]\n"
"buffer_load_dwordx4 a[188:191], v12, s[20:23], s35 offen offset:1024 sc1\n"
"v_mfma_f32_16x16x32_bf16 v[220:223], v[28:31], a[252:255], v[220:223]\n"
"s_add_u32 m0, s29, 0x24000\n"
"s_mov_b32 s31, 0x480000\n"
"buffer_load_dwordx4 v11, s[16:19], s31 offen lds\n"
"v_mfma_f32_16x16x32_bf16 v[220:223], v[24:27], a[248:251], v[220:223]\n"
"s_waitcnt lgkmcnt(0)\n"
"v_mfma_f32_16x16x32_bf16 v[224:227], v[16:19], a[224:227], v[224:227]\n"
"v_mfma_f32_16x16x32_bf16 v[224:227], v[20:23], a[228:231], v[224:227]\n"
"ds_read_b64_tr_b16 v[24:25], v4 offset:50696\n"
"v_accvgpr_write_b32 a124, 0\n"
"v_mfma_f32_16x16x32_bf16 v[228:231], v[20:23], a[236:239], v[228:231]\n"
"ds_read_b64_tr_b16 v[26:27], v4 offset:50952\n"
"v_mfma_f32_16x16x32_bf16 v[228:231], v[16:19], a[232:235], v[228:231]\n"
"ds_read_b64_tr_b16 v[28:29], v4 offset:58888\n"
"v_accvgpr_write_b32 a125, 0\n"
"v_mfma_f32_16x16x32_bf16 v[232:235], v[16:19], a[240:243], v[232:235]\n"
"ds_read_b64_tr_b16 v[30:31], v4 offset:59144\n"
"v_mfma_f32_16x16x32_bf16 v[232:235], v[20:23], a[244:247], v[232:235]\n"
"s_add_u32 m0, s29, 0x24800\n"
"s_mov_b32 s31, 0x490000\n"
"buffer_load_dwordx4 v11, s[16:19], s31 offen lds\n"
"v_mfma_f32_16x16x32_bf16 v[236:239], v[20:23], a[252:255], v[236:239]\n"
"v_mfma_f32_16x16x32_bf16 v[236:239], v[16:19], a[248:251], v[236:239]\n"
"s_waitcnt lgkmcnt(0)\n"
"v_mfma_f32_16x16x32_bf16 v[240:243], v[24:27], a[224:227], v[240:243]\n"
"s_waitcnt vmcnt(10)\n"
"s_barrier\n"
"v_add_u32 v4, 0x10000, v4\n"
"v_mfma_f32_16x16x32_bf16 v[240:243], v[28:31], a[228:231], v[240:243]\n"
"ds_read_b64_tr_b16 v[16:17], v4 offset:0\n"
"v_accvgpr_write_b32 a126, 0\n"
"v_mfma_f32_16x16x32_bf16 v[244:247], v[28:31], a[236:239], v[244:247]\n"
"ds_read_b64_tr_b16 v[18:19], v4 offset:256\n"
"v_mfma_f32_16x16x32_bf16 v[244:247], v[24:27], a[232:235], v[244:247]\n"
"ds_read_b64_tr_b16 v[20:21], v4 offset:8192\n"
"v_accvgpr_write_b32 a127, 0\n"
"v_mfma_f32_16x16x32_bf16 v[248:251], v[24:27], a[240:243], v[248:251]\n"
"ds_read_b64_tr_b16 v[22:23], v4 offset:8448\n"
"v_mfma_f32_16x16x32_bf16 v[248:251], v[28:31], a[244:247], v[248:251]\n"
"s_add_u32 m0, s29, 0x25000\n"
"s_mov_b32 s31, 0x4a0000\n"
"buffer_load_dwordx4 v11, s[16:19], s31 offen lds\n"
"v_mfma_f32_16x16x32_bf16 v[252:255], v[28:31], a[252:255], v[252:255]\n"
"s_add_u32 m0, s29, 0x25800\n"
"s_mov_b32 s31, 0x4b0000\n"
"buffer_load_dwordx4 v11, s[16:19], s31 offen lds\n"
"v_mfma_f32_16x16x32_bf16 v[252:255], v[24:27], a[248:251], v[252:255]\n"
"s_waitcnt vmcnt(16) lgkmcnt(0)\n"
"v_mfma_f32_16x16x32_bf16 v[128:131], v[16:19], a[128:131], v[128:131]\n"
"s_add_u32 m0, s29, 0x0\n"
"s_mov_b32 s31, 0x0\n"
"buffer_load_dwordx4 v5, s[8:11], s31 offen lds\n"
"v_mfma_f32_16x16x32_bf16 v[128:131], v[20:23], a[132:135], v[128:131]\n"
"ds_read_b64_tr_b16 v[24:25], v4 offset:8\n"
"v_mfma_f32_16x16x32_bf16 v[132:135], v[20:23], a[140:143], v[132:135]\n"
"ds_read_b64_tr_b16 v[26:27], v4 offset:264\n"
"v_mfma_f32_16x16x32_bf16 v[132:135], v[16:19], a[136:139], v[132:135]\n"
"ds_read_b64_tr_b16 v[28:29], v4 offset:8200\n"
"v_mfma_f32_16x16x32_bf16 v[136:139], v[16:19], a[144:147], v[136:139]\n"
"ds_read_b64_tr_b16 v[30:31], v4 offset:8456\n"
"v_mfma_f32_16x16x32_bf16 v[136:139], v[20:23], a[148:151], v[136:139]\n"
"v_mfma_f32_16x16x32_bf16 v[140:143], v[20:23], a[156:159], v[140:143]\n"
"s_add_u32 m0, s29, 0x0\n"
"s_mov_b32 s31, 0x0\n"
"buffer_load_dwordx4 v6, s[8:11], s31 offen offset:2048 lds\n"
"v_mfma_f32_16x16x32_bf16 v[140:143], v[16:19], a[152:155], v[140:143]\n"
"s_waitcnt lgkmcnt(0)\n"
"v_mfma_f32_16x16x32_bf16 v[144:147], v[24:27], a[128:131], v[144:147]\n"
"s_add_u32 m0, s29, 0x1000\n"
"s_mov_b32 s31, 0x0\n"
"buffer_load_dwordx4 v7, s[8:11], s31 offen lds\n"
"v_mfma_f32_16x16x32_bf16 v[144:147], v[28:31], a[132:135], v[144:147]\n"
"ds_read_b64_tr_b16 v[16:17], v4 offset:512\n"
"v_mfma_f32_16x16x32_bf16 v[148:151], v[28:31], a[140:143], v[148:151]\n"
"ds_read_b64_tr_b16 v[18:19], v4 offset:768\n"
"v_mfma_f32_16x16x32_bf16 v[148:151], v[24:27], a[136:139], v[148:151]\n"
"ds_read_b64_tr_b16 v[20:21], v4 offset:8704\n"
"v_mfma_f32_16x16x32_bf16 v[152:155], v[24:27], a[144:147], v[152:155]\n"
"ds_read_b64_tr_b16 v[22:23], v4 offset:8960\n"
"v_mfma_f32_16x16x32_bf16 v[152:155], v[28:31], a[148:151], v[152:155]\n"
"v_mfma_f32_16x16x32_bf16 v[156:159], v[28:31], a[156:159], v[156:159]\n"
"s_add_u32 m0, s29, 0x1000\n"
"s_mov_b32 s31, 0x0\n"
"buffer_load_dwordx4 v8, s[8:11], s31 offen offset:2048 lds\n"
"v_mfma_f32_16x16x32_bf16 v[156:159], v[24:27], a[152:155], v[156:159]\n"
"s_waitcnt lgkmcnt(0)\n"
"v_mfma_f32_16x16x32_bf16 v[160:163], v[16:19], a[128:131], v[160:163]\n"
"s_add_u32 m0, s29, 0xc000\n"
"s_mov_b32 s31, 0x0\n"
"buffer_load_dwordx4 v9, s[12:15], s31 offen lds\n"
"v_mfma_f32_16x16x32_bf16 v[160:163], v[20:23], a[132:135], v[160:163]\n"
"ds_read_b64_tr_b16 v[24:25], v4 offset:520\n"
"v_mfma_f32_16x16x32_bf16 v[164:167], v[20:23], a[140:143], v[164:167]\n"
"ds_read_b64_tr_b16 v[26:27], v4 offset:776\n"
"v_mfma_f32_16x16x32_bf16 v[164:167], v[16:19], a[136:139], v[164:167]\n"
"ds_read_b64_tr_b16 v[28:29], v4 offset:8712\n"
"v_mfma_f32_16x16x32_bf16 v[168:171], v[16:19], a[144:147], v[168:171]\n"
"ds_read_b64_tr_b16 v[30:31], v4 offset:8968\n"
"v_mfma_f32_16x16x32_bf16 v[168:171], v[20:23], a[148:151], v[168:171]\n"
"v_mfma_f32_16x16x32_bf16 v[172:175], v[20:23], a[156:159], v[172:175]\n"
"s_add_u32 m0, s29, 0xc000\n"
"s_mov_b32 s31, 0x0\n"
"buffer_load_dwordx4 v9, s[12:15], s31 offen offset:2048 lds\n"
"v_mfma_f32_16x16x32_bf16 v[172:175], v[16:19], a[152:155], v[172:175]\n"
"s_waitcnt lgkmcnt(0)\n"
"v_mfma_f32_16x16x32_bf16 v[176:179], v[24:27], a[128:131], v[176:179]\n"
"s_add_u32 m0, s29, 0xd000\n"
"s_mov_b32 s31, 0x0\n"
"buffer_load_dwordx4 v10, s[12:15], s31 offen lds\n"
"v_mfma_f32_16x16x32_bf16 v[176:179], v[28:31], a[132:135], v[176:179]\n"
"ds_read_b64_tr_b16 v[16:17], v4 offset:1024\n"
"v_mfma_f32_16x16x32_bf16 v[180:183], v[28:31], a[140:143], v[180:183]\n"
"ds_read_b64_tr_b16 v[18:19], v4 offset:1280\n"
"v_mfma_f32_16x16x32_bf16 v[180:183], v[24:27], a[136:139], v[180:183]\n"
"ds_read_b64_tr_b16 v[20:21], v4 offset:9216\n"
"v_mfma_f32_16x16x32_bf16 v[184:187], v[24:27], a[144:147], v[184:187]\n"
"ds_read_b64_tr_b16 v[22:23], v4 offset:9472\n"
"v_mfma_f32_16x16x32_bf16 v[184:187], v[28:31], a[148:151], v[184:187]\n"
"v_mfma_f32_16x16x32_bf16 v[188:191], v[28:31], a[156:159], v[188:191]\n"
"s_add_u32 m0, s29, 0xd000\n"
"s_mov_b32 s31, 0x0\n"
"buffer_load_dwordx4 v10, s[12:15], s31 offen offset:2048 lds\n"
"v_mfma_f32_16x16x32_bf16 v[188:191], v[24:27], a[152:155], v[188:191]\n"
"s_waitcnt lgkmcnt(0)\n"
"v_mfma_f32_16x16x32_bf16 v[192:195], v[16:19], a[128:131], v[192:195]\n"
"s_add_u32 m0, s29, 0x4000\n"
"s_mov_b32 s31, 0x4000\n"
"buffer_load_dwordx4 v5, s[8:11], s31 offen lds\n"
"v_mfma_f32_16x16x32_bf16 v[192:195], v[20:23], a[132:135], v[192:195]\n"
"ds_read_b64_tr_b16 v[24:25], v4 offset:1032\n"
"v_mfma_f32_16x16x32_bf16 v[196:199], v[20:23], a[140:143], v[196:199]\n"
"ds_read_b64_tr_b16 v[26:27], v4 offset:1288\n"
"v_mfma_f32_16x16x32_bf16 v[196:199], v[16:19], a[136:139], v[196:199]\n"
"ds_read_b64_tr_b16 v[28:29], v4 offset:9224\n"
"v_mfma_f32_16x16x32_bf16 v[200:203], v[16:19], a[144:147], v[200:203]\n"
"ds_read_b64_tr_b16 v[30:31], v4 offset:9480\n"
"v_mfma_f32_16x16x32_bf16 v[200:203], v[20:23], a[148:151], v[200:203]\n"
"v_mfma_f32_16x16x32_bf16 v[204:207], v[20:23], a[156:159], v[204:207]\n"
"s_add_u32 m0, s29, 0x4000\n"
"s_mov_b32 s31, 0x4000\n"
"buffer_load_dwordx4 v6, s[8:11], s31 offen offset:2048 lds\n"
"v_mfma_f32_16x16x32_bf16 v[204:207], v[16:19], a[152:155], v[204:207]\n"
"s_waitcnt lgkmcnt(0)\n"
"v_mfma_f32_16x16x32_bf16 v[208:211], v[24:27], a[128:131], v[208:211]\n"
"s_add_u32 m0, s29, 0x5000\n"
"s_mov_b32 s31, 0x4000\n"
"buffer_load_dwordx4 v7, s[8:11], s31 offen lds\n"
"v_mfma_f32_16x16x32_bf16 v[208:211], v[28:31], a[132:135], v[208:211]\n"
"ds_read_b64_tr_b16 v[16:17], v4 offset:1536\n"
"v_mfma_f32_16x16x32_bf16 v[212:215], v[28:31], a[140:143], v[212:215]\n"
"ds_read_b64_tr_b16 v[18:19], v4 offset:1792\n"
"v_mfma_f32_16x16x32_bf16 v[212:215], v[24:27], a[136:139], v[212:215]\n"
"ds_read_b64_tr_b16 v[20:21], v4 offset:9728\n"
"v_mfma_f32_16x16x32_bf16 v[216:219], v[24:27], a[144:147], v[216:219]\n"
"ds_read_b64_tr_b16 v[22:23], v4 offset:9984\n"
"v_mfma_f32_16x16x32_bf16 v[216:219], v[28:31], a[148:151], v[216:219]\n"
"v_mfma_f32_16x16x32_bf16 v[220:223], v[28:31], a[156:159], v[220:223]\n"
"s_add_u32 m0, s29, 0x5000\n"
"s_mov_b32 s31, 0x4000\n"
"buffer_load_dwordx4 v8, s[8:11], s31 offen offset:2048 lds\n"
"v_mfma_f32_16x16x32_bf16 v[220:223], v[24:27], a[152:155], v[220:223]\n"
"s_waitcnt lgkmcnt(0)\n"
"v_mfma_f32_16x16x32_bf16 v[224:227], v[16:19], a[128:131], v[224:227]\n"
"v_mfma_f32_16x16x32_bf16 v[224:227], v[20:23], a[132:135], v[224:227]\n"
"ds_read_b64_tr_b16 v[24:25], v4 offset:1544\n"
"v_mfma_f32_16x16x32_bf16 v[228:231], v[20:23], a[140:143], v[228:231]\n"
"ds_read_b64_tr_b16 v[26:27], v4 offset:1800\n"
"v_mfma_f32_16x16x32_bf16 v[228:231], v[16:19], a[136:139], v[228:231]\n"
"ds_read_b64_tr_b16 v[28:29], v4 offset:9736\n"
"v_mfma_f32_16x16x32_bf16 v[232:235], v[16:19], a[144:147], v[232:235]\n"
"ds_read_b64_tr_b16 v[30:31], v4 offset:9992\n"
"v_mfma_f32_16x16x32_bf16 v[232:235], v[20:23], a[148:151], v[232:235]\n"
"v_mfma_f32_16x16x32_bf16 v[236:239], v[20:23], a[156:159], v[236:239]\n"
"v_mfma_f32_16x16x32_bf16 v[236:239], v[16:19], a[152:155], v[236:239]\n"
"s_waitcnt lgkmcnt(0)\n"
"v_mfma_f32_16x16x32_bf16 v[240:243], v[24:27], a[128:131], v[240:243]\n"
"s_waitcnt vmcnt(12)\n"
"s_barrier\n"
"v_mfma_f32_16x16x32_bf16 v[240:243], v[28:31], a[132:135], v[240:243]\n"
"ds_read_b64_tr_b16 v[16:17], v4 offset:16384\n"
"v_mfma_f32_16x16x32_bf16 v[244:247], v[28:31], a[140:143], v[244:247]\n"
"ds_read_b64_tr_b16 v[18:19], v4 offset:16640\n"
"v_mfma_f32_16x16x32_bf16 v[244:247], v[24:27], a[136:139], v[244:247]\n"
"ds_read_b64_tr_b16 v[20:21], v4 offset:24576\n"
"v_mfma_f32_16x16x32_bf16 v[248:251], v[24:27], a[144:147], v[248:251]\n"
"ds_read_b64_tr_b16 v[22:23], v4 offset:24832\n"
"v_mfma_f32_16x16x32_bf16 v[248:251], v[28:31], a[148:151], v[248:251]\n"
"buffer_load_dwordx2 v[54:55], v53, s[92:95], 0 offen sc1\n"
"v_mfma_f32_16x16x32_bf16 v[252:255], v[28:31], a[156:159], v[252:255]\n"
"v_mfma_f32_16x16x32_bf16 v[252:255], v[24:27], a[152:155], v[252:255]\n"
"s_waitcnt vmcnt(17) lgkmcnt(0)\n"
"v_mfma_f32_16x16x32_bf16 v[128:131], v[16:19], a[160:163], v[128:131]\n"
"s_lshl_b32 s60, s97, 7\n"
"v_mfma_f32_16x16x32_bf16 v[128:131], v[20:23], a[164:167], v[128:131]\n"
"ds_read_b64_tr_b16 v[24:25], v4 offset:16392\n"
"v_mfma_f32_16x16x32_bf16 v[132:135], v[20:23], a[172:175], v[132:135]\n"
"ds_read_b64_tr_b16 v[26:27], v4 offset:16648\n"
"v_mfma_f32_16x16x32_bf16 v[132:135], v[16:19], a[168:171], v[132:135]\n"
"ds_read_b64_tr_b16 v[28:29], v4 offset:24584\n"
"v_mfma_f32_16x16x32_bf16 v[136:139], v[16:19], a[176:179], v[136:139]\n"
"ds_read_b64_tr_b16 v[30:31], v4 offset:24840\n"
"v_mfma_f32_16x16x32_bf16 v[136:139], v[20:23], a[180:183], v[136:139]\n"
"s_mul_i32 s61, s90, 1769472\n"
"s_add_u32 s60, s60, s61\n"
"s_cmp_lt_u32 s97, 27\n"
"s_cselect_b32 s61, 1766144, 0\n"
"s_cselect_b32 s62, 81920, 0\n"
"v_mfma_f32_16x16x32_bf16 v[140:143], v[20:23], a[188:191], v[140:143]\n"
"s_add_u32 s16, s46, s60\n"
"s_addc_u32 s17, s47, 0\n"
"s_mov_b32 s18, s61\n"
"s_mov_b32 s19, 0x20000\n"
"v_mfma_f32_16x16x32_bf16 v[140:143], v[16:19], a[184:187], v[140:143]\n"
"s_waitcnt lgkmcnt(0)\n"
"v_mfma_f32_16x16x32_bf16 v[144:147], v[24:27], a[160:163], v[144:147]\n"
"s_mul_i32 s60, s96, 327680\n"
"s_mul_i32 s61, s90, 81920\n"
"s_add_u32 s60, s60, s61\n"
"v_mfma_f32_16x16x32_bf16 v[144:147], v[28:31], a[164:167], v[144:147]\n"
"ds_read_b64_tr_b16 v[16:17], v4 offset:16896\n"
"v_mfma_f32_16x16x32_bf16 v[148:151], v[28:31], a[172:175], v[148:151]\n"
"ds_read_b64_tr_b16 v[18:19], v4 offset:17152\n"
"v_mfma_f32_16x16x32_bf16 v[148:151], v[24:27], a[168:171], v[148:151]\n"
"ds_read_b64_tr_b16 v[20:21], v4 offset:25088\n"
"v_mfma_f32_16x16x32_bf16 v[152:155], v[24:27], a[176:179], v[152:155]\n"
"ds_read_b64_tr_b16 v[22:23], v4 offset:25344\n"
"v_mfma_f32_16x16x32_bf16 v[152:155], v[28:31], a[180:183], v[152:155]\n"
"s_add_u32 s60, s60, 0x2c0000\n"
"s_add_u32 s20, s56, s60\n"
"s_addc_u32 s21, s57, 0\n"
"s_mov_b32 s22, s62\n"
"v_mfma_f32_16x16x32_bf16 v[156:159], v[28:31], a[188:191], v[156:159]\n"
"s_mov_b32 s23, 0x20000\n"
"s_mov_b32 s82, 0\n"
"s_mov_b32 s83, 0\n"
"v_mfma_f32_16x16x32_bf16 v[156:159], v[24:27], a[184:187], v[156:159]\n"
"s_waitcnt lgkmcnt(0)\n"
"v_mfma_f32_16x16x32_bf16 v[160:163], v[16:19], a[160:163], v[160:163]\n"
"s_add_u32 m0, s71, 0x1d000\n"
"s_nop 0\n"
"buffer_load_dwordx4 v44, s[16:19], s82 offen lds\n"
"v_cvt_pk_bf16_f32 v64, v128, v129\n"
"v_mfma_f32_16x16x32_bf16 v[160:163], v[20:23], a[164:167], v[160:163]\n"
"ds_read_b64_tr_b16 v[24:25], v4 offset:16904\n"
"v_cvt_pk_bf16_f32 v65, v130, v131\n"
"v_mfma_f32_16x16x32_bf16 v[164:167], v[20:23], a[172:175], v[164:167]\n"
"ds_read_b64_tr_b16 v[26:27], v4 offset:17160\n"
"v_cvt_pk_bf16_f32 v66, v144, v145\n"
"v_mfma_f32_16x16x32_bf16 v[164:167], v[16:19], a[168:171], v[164:167]\n"
"ds_read_b64_tr_b16 v[28:29], v4 offset:25096\n"
"v_cvt_pk_bf16_f32 v67, v146, v147\n"
"v_mfma_f32_16x16x32_bf16 v[168:171], v[16:19], a[176:179], v[168:171]\n"
"ds_read_b64_tr_b16 v[30:31], v4 offset:25352\n"
"v_cvt_pk_bf16_f32 v80, v132, v133\n"
"v_mfma_f32_16x16x32_bf16 v[168:171], v[20:23], a[180:183], v[168:171]\n"
"buffer_load_dwordx4 v45, s[16:19], s82 offen offset:1024 lds\n"
"v_cvt_pk_bf16_f32 v81, v134, v135\n"
"v_mfma_f32_16x16x32_bf16 v[172:175], v[20:23], a[188:191], v[172:175]\n"
"buffer_load_dwordx4 v46, s[16:19], s82 offen offset:2048 lds\n"
"v_cvt_pk_bf16_f32 v82, v148, v149\n"
"v_mfma_f32_16x16x32_bf16 v[172:175], v[16:19], a[184:187], v[172:175]\n"
"v_cvt_pk_bf16_f32 v83, v150, v151\n"
"s_waitcnt lgkmcnt(0)\n"
"v_mfma_f32_16x16x32_bf16 v[176:179], v[24:27], a[160:163], v[176:179]\n"
"buffer_load_dwordx4 v47, s[16:19], s82 offen offset:3072 lds\n"
"v_cvt_pk_bf16_f32 v96, v136, v137\n"
"v_mfma_f32_16x16x32_bf16 v[176:179], v[28:31], a[164:167], v[176:179]\n"
"ds_read_b64_tr_b16 v[16:17], v4 offset:17408\n"
"v_cvt_pk_bf16_f32 v97, v138, v139\n"
"v_mfma_f32_16x16x32_bf16 v[180:183], v[28:31], a[172:175], v[180:183]\n"
"ds_read_b64_tr_b16 v[18:19], v4 offset:17664\n"
"v_cvt_pk_bf16_f32 v98, v152, v153\n"
"v_mfma_f32_16x16x32_bf16 v[180:183], v[24:27], a[168:171], v[180:183]\n"
"ds_read_b64_tr_b16 v[20:21], v4 offset:25600\n"
"v_cvt_pk_bf16_f32 v99, v154, v155\n"
"v_mfma_f32_16x16x32_bf16 v[184:187], v[24:27], a[176:179], v[184:187]\n"
"ds_read_b64_tr_b16 v[22:23], v4 offset:25856\n"
"v_cvt_pk_bf16_f32 v112, v140, v141\n"
"v_mfma_f32_16x16x32_bf16 v[184:187], v[28:31], a[180:183], v[184:187]\n"
"v_cvt_pk_bf16_f32 v113, v142, v143\n"
"v_mfma_f32_16x16x32_bf16 v[188:191], v[28:31], a[188:191], v[188:191]\n"
"v_cvt_pk_bf16_f32 v114, v156, v157\n"
"v_mfma_f32_16x16x32_bf16 v[188:191], v[24:27], a[184:187], v[188:191]\n"
"v_cvt_pk_bf16_f32 v115, v158, v159\n"
"s_waitcnt lgkmcnt(0)\n"
"v_mfma_f32_16x16x32_bf16 v[192:195], v[16:19], a[160:163], v[192:195]\n"
"v_cvt_pk_bf16_f32 v68, v160, v161\n"
"v_mfma_f32_16x16x32_bf16 v[192:195], v[20:23], a[164:167], v[192:195]\n"
"ds_read_b64_tr_b16 v[24:25], v4 offset:17416\n"
"v_cvt_pk_bf16_f32 v69, v162, v163\n"
"v_mfma_f32_16x16x32_bf16 v[196:199], v[20:23], a[172:175], v[196:199]\n"
"ds_read_b64_tr_b16 v[26:27], v4 offset:17672\n"
"v_cvt_pk_bf16_f32 v70, v176, v177\n"
"v_mfma_f32_16x16x32_bf16 v[196:199], v[16:19], a[168:171], v[196:199]\n"
"ds_read_b64_tr_b16 v[28:29], v4 offset:25608\n"
"v_cvt_pk_bf16_f32 v71, v178, v179\n"
"v_mfma_f32_16x16x32_bf16 v[200:203], v[16:19], a[176:179], v[200:203]\n"
"ds_read_b64_tr_b16 v[30:31], v4 offset:25864\n"
"v_cvt_pk_bf16_f32 v84, v164, v165\n"
"v_mfma_f32_16x16x32_bf16 v[200:203], v[20:23], a[180:183], v[200:203]\n"
"v_cvt_pk_bf16_f32 v85, v166, v167\n"
"v_mfma_f32_16x16x32_bf16 v[204:207], v[20:23], a[188:191], v[204:207]\n"
"v_cvt_pk_bf16_f32 v86, v180, v181\n"
"v_mfma_f32_16x16x32_bf16 v[204:207], v[16:19], a[184:187], v[204:207]\n"
"v_cvt_pk_bf16_f32 v87, v182, v183\n"
"s_waitcnt lgkmcnt(0)\n"
"v_mfma_f32_16x16x32_bf16 v[208:211], v[24:27], a[160:163], v[208:211]\n"
"v_cvt_pk_bf16_f32 v100, v168, v169\n"
"v_mfma_f32_16x16x32_bf16 v[208:211], v[28:31], a[164:167], v[208:211]\n"
"ds_read_b64_tr_b16 v[16:17], v4 offset:17920\n"
"v_cvt_pk_bf16_f32 v101, v170, v171\n"
"v_mfma_f32_16x16x32_bf16 v[212:215], v[28:31], a[172:175], v[212:215]\n"
"ds_read_b64_tr_b16 v[18:19], v4 offset:18176\n"
"v_cvt_pk_bf16_f32 v102, v184, v185\n"
"v_mfma_f32_16x16x32_bf16 v[212:215], v[24:27], a[168:171], v[212:215]\n"
"ds_read_b64_tr_b16 v[20:21], v4 offset:26112\n"
"v_cvt_pk_bf16_f32 v103, v186, v187\n"
"v_mfma_f32_16x16x32_bf16 v[216:219], v[24:27], a[176:179], v[216:219]\n"
"ds_read_b64_tr_b16 v[22:23], v4 offset:26368\n"
"v_cvt_pk_bf16_f32 v116, v172, v173\n"
"v_mfma_f32_16x16x32_bf16 v[216:219], v[28:31], a[180:183], v[216:219]\n"
"v_cvt_pk_bf16_f32 v117, v174, v175\n"
"v_mfma_f32_16x16x32_bf16 v[220:223], v[28:31], a[188:191], v[220:223]\n"
"v_cvt_pk_bf16_f32 v118, v188, v189\n"
"v_mfma_f32_16x16x32_bf16 v[220:223], v[24:27], a[184:187], v[220:223]\n"
"v_cvt_pk_bf16_f32 v119, v190, v191\n"
"s_waitcnt lgkmcnt(0)\n"
"v_mfma_f32_16x16x32_bf16 v[224:227], v[16:19], a[160:163], v[224:227]\n"
"v_cvt_pk_bf16_f32 v72, v192, v193\n"
"v_mfma_f32_16x16x32_bf16 v[224:227], v[20:23], a[164:167], v[224:227]\n"
"ds_read_b64_tr_b16 v[24:25], v4 offset:17928\n"
"v_cvt_pk_bf16_f32 v73, v194, v195\n"
"v_mfma_f32_16x16x32_bf16 v[228:231], v[20:23], a[172:175], v[228:231]\n"
"ds_read_b64_tr_b16 v[26:27], v4 offset:18184\n"
"v_cvt_pk_bf16_f32 v74, v208, v209\n"
"v_mfma_f32_16x16x32_bf16 v[228:231], v[16:19], a[168:171], v[228:231]\n"
"ds_read_b64_tr_b16 v[28:29], v4 offset:26120\n"
"v_cvt_pk_bf16_f32 v75, v210, v211\n"
"v_mfma_f32_16x16x32_bf16 v[232:235], v[16:19], a[176:179], v[232:235]\n"
"ds_read_b64_tr_b16 v[30:31], v4 offset:26376\n"
"v_cvt_pk_bf16_f32 v88, v196, v197\n"
"v_mfma_f32_16x16x32_bf16 v[232:235], v[20:23], a[180:183], v[232:235]\n"
"v_cvt_pk_bf16_f32 v89, v198, v199\n"
"v_mfma_f32_16x16x32_bf16 v[236:239], v[20:23], a[188:191], v[236:239]\n"
"v_cvt_pk_bf16_f32 v90, v212, v213\n"
"v_mfma_f32_16x16x32_bf16 v[236:239], v[16:19], a[184:187], v[236:239]\n"
"v_cvt_pk_bf16_f32 v91, v214, v215\n"
"s_waitcnt lgkmcnt(0)\n"
"v_mfma_f32_16x16x32_bf16 v[240:243], v[24:27], a[160:163], v[240:243]\n"
"s_waitcnt vmcnt(13)\n"
"s_barrier\n"
"v_cvt_pk_bf16_f32 v104, v200, v201\n"
"v_mfma_f32_16x16x32_bf16 v[240:243], v[28:31], a[164:167], v[240:243]\n"
"ds_read_b128 a[192:195], v1 offset:0\n"
"v_cvt_pk_bf16_f32 v105, v202, v203\n"
"v_mfma_f32_16x16x32_bf16 v[244:247], v[28:31], a[172:175], v[244:247]\n"
"ds_read_b128 a[196:199], v1 offset:512\n"
"v_cvt_pk_bf16_f32 v106, v216, v217\n"
"v_mfma_f32_16x16x32_bf16 v[244:247], v[24:27], a[168:171], v[244:247]\n"
"ds_read_b128 a[200:203], v1 offset:1024\n"
"v_cvt_pk_bf16_f32 v107, v218, v219\n"
"v_mfma_f32_16x16x32_bf16 v[248:251], v[24:27], a[176:179], v[248:251]\n"
"ds_read_b128 a[204:207], v1 offset:1536\n"
"v_cvt_pk_bf16_f32 v120, v204, v205\n"
"v_mfma_f32_16x16x32_bf16 v[248:251], v[28:31], a[180:183], v[248:251]\n"
"v_cvt_pk_bf16_f32 v121, v206, v207\n"
"v_mfma_f32_16x16x32_bf16 v[252:255], v[28:31], a[188:191], v[252:255]\n"
"v_cvt_pk_bf16_f32 v122, v220, v221\n"
"v_mfma_f32_16x16x32_bf16 v[252:255], v[24:27], a[184:187], v[252:255]\n"
"v_cvt_pk_bf16_f32 v123, v222, v223\n"
"v_add_u32 v5, 0x8000, v5\n"
"v_add_u32 v6, 0x8000, v6\n"
"v_add_u32 v7, 0x8000, v7\n"
"v_add_u32 v8, 0x8000, v8\n"
"v_add_u32 v9, 0x4000, v9\n"
"v_add_u32 v10, 0x4000, v10\n"
"v_cvt_pk_bf16_f32 v76, v224, v225\n"
"v_cvt_pk_bf16_f32 v77, v226, v227\n"
"v_cvt_pk_bf16_f32 v78, v240, v241\n"
"v_cvt_pk_bf16_f32 v79, v242, v243\n"
"s_waitcnt vmcnt(4) lgkmcnt(0)\n"
"v_cmp_eq_u64 vcc, s[88:89], v[54:55]\n"
"s_mov_b32 s91, 0\n"
"s_nop 2\n"
"s_cmp_eq_u64 vcc, -1\n"
"s_cbranch_scc1 L_OK2%=\n"
"L_SP2%=:\n"
"s_sleep 2\n"
"buffer_load_dwordx2 v[54:55], v53, s[92:95], 0 offen sc1\n"
"s_waitcnt vmcnt(0)\n"
"v_cmp_eq_u64 vcc, s[88:89], v[54:55]\n"
"s_add_u32 s91, s91, 1\n"
"s_nop 3\n"
"s_cmp_eq_u64 vcc, -1\n"
"s_cbranch_scc1 L_OK2%=\n"
"s_cmp_lt_u32 s91, 0x40000\n"
"s_cbranch_scc1 L_SP2%=\n"
"L_OK2%=:\n"
"v_mfma_f32_16x16x32_bf16 v[144:147], a[192:195], v[64:67], 0\n"
"v_cvt_pk_bf16_f32 v92, v228, v229\n"
"v_cvt_pk_bf16_f32 v93, v230, v231\n"
"v_mfma_f32_16x16x32_bf16 v[144:147], a[196:199], v[68:71], v[144:147]\n"
"v_cvt_pk_bf16_f32 v94, v244, v245\n"
"v_cvt_pk_bf16_f32 v95, v246, v247\n"
"v_mfma_f32_16x16x32_bf16 v[144:147], a[200:203], v[72:75], v[144:147]\n"
"v_cvt_pk_bf16_f32 v108, v232, v233\n"
"v_cvt_pk_bf16_f32 v109, v234, v235\n"
"v_mfma_f32_16x16x32_bf16 v[144:147], a[204:207], v[76:79], v[144:147]\n"
"v_cvt_pk_bf16_f32 v110, v248, v249\n"
"v_cvt_pk_bf16_f32 v111, v250, v251\n"
"v_mfma_f32_16x16x32_bf16 v[148:151], a[192:195], v[80:83], 0\n"
"v_cvt_pk_bf16_f32 v124, v236, v237\n"
"v_cvt_pk_bf16_f32 v125, v238, v239\n"
"v_mfma_f32_16x16x32_bf16 v[148:151], a[196:199], v[84:87], v[148:151]\n"
"v_cvt_pk_bf16_f32 v126, v252, v253\n"
"v_cvt_pk_bf16_f32 v127, v254, v255\n"
"v_mfma_f32_16x16x32_bf16 v[148:151], a[200:203], v[88:91], v[148:151]\n"
"v_subrev_u32 v4, s29, v1\n"
"v_subrev_u32 v11, s29, v2\n"
"v_mfma_f32_16x16x32_bf16 v[148:151], a[204:207], v[92:95], v[148:151]\n"
"buffer_load_dwordx4 v[224:227], v12, s[20:23], s83 offen sc1\n"
"v_mfma_f32_16x16x32_bf16 v[152:155], a[192:195], v[96:99], 0\n"
"buffer_load_dwordx4 v[228:231], v12, s[20:23], s83 offen offset:1024 sc1\n"
"v_mfma_f32_16x16x32_bf16 v[152:155], a[196:199], v[100:103], v[152:155]\n"
"buffer_load_dwordx4 v[232:235], v12, s[20:23], s83 offen offset:2048 sc1\n"
"v_mfma_f32_16x16x32_bf16 v[152:155], a[200:203], v[104:107], v[152:155]\n"
"buffer_load_dwordx4 v[236:239], v12, s[20:23], s83 offen offset:3072 sc1\n"
"v_mfma_f32_16x16x32_bf16 v[152:155], a[204:207], v[108:111], v[152:155]\n"
"buffer_load_dwordx4 v[240:243], v52, s[20:23], s83 offen sc1\n"
"v_mfma_f32_16x16x32_bf16 v[156:159], a[192:195], v[112:115], 0\n"
"v_mfma_f32_16x16x32_bf16 v[156:159], a[196:199], v[116:119], v[156:159]\n"
"v_mfma_f32_16x16x32_bf16 v[156:159], a[200:203], v[120:123], v[156:159]\n"
"v_mfma_f32_16x16x32_bf16 v[156:159], a[204:207], v[124:127], v[156:159]\n"
"s_lshl_b32 s60, s97, 7\n"
"s_lshl_b32 s61, s90, 5\n"
"s_add_u32 s60, s60, s61\n"
"s_add_u32 s58, s48, s60\n"
"s_addc_u32 s59, s49, 0\n"
"s_cmp_lt_u32 s97, 27\n"
"s_cselect_b32 s58, s58, s48\n"
"s_cselect_b32 s59, s59, s49\n"
"v_lshrrev_b32 v22, 4, v13\n"
"v_lshlrev_b32 v22, 3, v22\n"
"global_load_dwordx2 v[22:23], v22, s[58:59]\n"
"v_max3_f32 v36, v144, v145, v146\n"
"v_max_f32 v36, v36, v147\n"
"v_max3_f32 v37, v148, v149, v150\n"
"v_max_f32 v37, v37, v151\n"
"v_max3_f32 v38, v152, v153, v154\n"
"v_max_f32 v38, v38, v155\n"
"v_max3_f32 v39, v156, v157, v158\n"
"v_max_f32 v39, v39, v159\n"
"v_mov_b32 v40, v36\n"
"v_mov_b32 v41, v37\n"
"v_mov_b32 v42, v38\n"
"v_mov_b32 v43, v39\n"
"v_permlane32_swap_b32 v36, v40\n"
"v_permlane32_swap_b32 v37, v41\n"
"v_permlane32_swap_b32 v38, v42\n"
"v_permlane32_swap_b32 v39, v43\n"
"v_max_f32 v36, v36, v40\n"
"v_max_f32 v37, v37, v41\n"
"v_max_f32 v38, v38, v42\n"
"v_max_f32 v39, v39, v43\n"
"v_mov_b32 v40, v36\n"
"v_mov_b32 v41, v37\n"
"v_mov_b32 v42, v38\n"
"v_mov_b32 v43, v39\n"
"v_permlane16_swap_b32 v36, v40\n"
"v_permlane16_swap_b32 v37, v41\n"
"v_permlane16_swap_b32 v38, v42\n"
"v_permlane16_swap_b32 v39, v43\n"
"v_max_f32 v36, v36, v40\n"
"v_max_f32 v37, v37, v41\n"
"v_max_f32 v38, v38, v42\n"
"v_max_f32 v39, v39, v43\n"
"v_sub_f32 v36, 0x41000000, v36\n"
"v_sub_f32 v37, 0x41000000, v37\n"
"v_sub_f32 v38, 0x41000000, v38\n"
"v_sub_f32 v39, 0x41000000, v39\n"
"v_mov_b32 v128, v36\n"
"v_mov_b32 v129, v36\n"
"v_mov_b32 v130, v36\n"
"v_mov_b32 v131, v36\n"
"v_mov_b32 v132, v37\n"
"v_mov_b32 v133, v37\n"
"v_mov_b32 v134, v37\n"
"v_mov_b32 v135, v37\n"
"v_mov_b32 v136, v38\n"
"v_mov_b32 v137, v38\n"
"v_mov_b32 v138, v38\n"
"v_mov_b32 v139, v38\n"
"v_mov_b32 v140, v39\n"
"v_mov_b32 v141, v39\n"
"v_mov_b32 v142, v39\n"
"v_mov_b32 v143, v39\n"
"v_pk_add_f32 v[144:145], v[144:145], v[128:129]\n"
"v_pk_add_f32 v[146:147], v[146:147], v[128:129]\n"
"v_pk_add_f32 v[148:149], v[148:149], v[132:133]\n"
"v_pk_add_f32 v[150:151], v[150:151], v[132:133]\n"
"v_pk_add_f32 v[152:153], v[152:153], v[136:137]\n"
"v_pk_add_f32 v[154:155], v[154:155], v[136:137]\n"
"v_pk_add_f32 v[156:157], v[156:157], v[140:141]\n"
"v_pk_add_f32 v[158:159], v[158:159], v[140:141]\n"
"L_JOIN%=:\n"
"s_add_u32 m0, s79, 0xc000\n"
"s_nop 0\n"
"buffer_load_dwordx4 v9, s[12:15], s28 offen lds\n"
"v_exp_f32 v144, v144\n"
"s_nop 0\n"
"v_add_f32 v32, v32, v144\n"
"v_exp_f32 v145, v145\n"
"s_nop 0\n"
"v_add_f32 v32, v32, v145\n"
"v_cvt_pk_bf16_f32 v160, v144, v145\n"
"v_exp_f32 v146, v146\n"
"buffer_load_dwordx4 v9, s[12:15], s28 offen offset:2048 lds\n"
"v_add_f32 v32, v32, v146\n"
"v_exp_f32 v147, v147\n"
"ds_read_b128 a[208:211], v1 offset:4096\n"
"v_add_f32 v32, v32, v147\n"
"v_cvt_pk_bf16_f32 v161, v146, v147\n"
"v_exp_f32 v148, v148\n"
"ds_read_b128 a[212:215], v1 offset:4608\n"
"v_add_f32 v33, v33, v148\n"
"v_exp_f32 v149, v149\n"
"s_nop 0\n"
"v_add_f32 v33, v33, v149\n"
"v_cvt_pk_bf16_f32 v168, v148, v149\n"
"v_exp_f32 v150, v150\n"
"s_nop 0\n"
"v_add_f32 v33, v33, v150\n"
"v_exp_f32 v151, v151\n"
"ds_read_b128 a[216:219], v1 offset:5120\n"
"v_add_f32 v33, v33, v151\n"
"v_mfma_f32_16x16x32_bf16 a[252:255], a[0:3], a[0:3], 0\n"
"v_cvt_pk_bf16_f32 v169, v150, v151\n"
"v_exp_f32 v152, v152\n"
"ds_read_b128 a[220:223], v1 offset:5632\n"
"v_add_f32 v34, v34, v152\n"
"v_exp_f32 v153, v153\n"
"s_nop 0\n"
"v_add_f32 v34, v34, v153\n"
"v_mfma_f32_16x16x32_bf16 a[248:251], a[0:3], a[0:3], 0\n"
"v_cvt_pk_bf16_f32 v176, v152, v153\n"
"s_add_u32 m0, s79, 0xd000\n"
"v_exp_f32 v154, v154\n"
"s_nop 0\n"
"v_add_f32 v34, v34, v154\n"
"s_waitcnt lgkmcnt(0)\n"
"v_exp_f32 v155, v155\n"
"s_nop 0\n"
"v_add_f32 v34, v34, v155\n"
"v_mfma_f32_16x16x32_bf16 a[244:247], a[0:3], a[0:3], 0\n"
"v_cvt_pk_bf16_f32 v177, v154, v155\n"
"v_mfma_f32_16x16x32_bf16 v[144:147], a[208:211], v[64:67], v[128:131]\n"
"v_exp_f32 v156, v156\n"
"v_mfma_f32_16x16x32_bf16 v[144:147], a[212:215], v[68:71], v[144:147]\n"
"v_add_f32 v35, v35, v156\n"
"v_mfma_f32_16x16x32_bf16 v[144:147], a[216:219], v[72:75], v[144:147]\n"
"v_exp_f32 v157, v157\n"
"v_mfma_f32_16x16x32_bf16 v[144:147], a[220:223], v[76:79], v[144:147]\n"
"v_add_f32 v35, v35, v157\n"
"v_cvt_pk_bf16_f32 v184, v156, v157\n"
"v_exp_f32 v158, v158\n"
"buffer_load_dwordx4 v10, s[12:15], s28 offen lds\n"
"v_add_f32 v35, v35, v158\n"
"v_exp_f32 v159, v159\n"
"s_nop 0\n"
"v_add_f32 v35, v35, v159\n"
"v_mfma_f32_16x16x32_bf16 a[240:243], a[0:3], a[0:3], 0\n"
"v_cvt_pk_bf16_f32 v185, v158, v159\n"
"v_mfma_f32_16x16x32_bf16 v[148:151], a[208:211], v[80:83], v[132:135]\n"
"s_add_u32 s82, s82, 110592\n"
"v_exp_f32 v144, v144\n"
"v_mfma_f32_16x16x32_bf16 v[148:151], a[212:215], v[84:87], v[148:151]\n"
"v_add_f32 v32, v32, v144\n"
"v_mfma_f32_16x16x32_bf16 v[148:151], a[216:219], v[88:91], v[148:151]\n"
"v_exp_f32 v145, v145\n"
"v_mfma_f32_16x16x32_bf16 v[148:151], a[220:223], v[92:95], v[148:151]\n"
"v_add_f32 v32, v32, v145\n"
"v_cvt_pk_bf16_f32 v162, v144, v145\n"
"v_exp_f32 v146, v146\n"
"buffer_load_dwordx4 v10, s[12:15], s28 offen offset:2048 lds\n"
"v_add_f32 v32, v32, v146\n"
"v_exp_f32 v147, v147\n"
"ds_read_b128 a[192:195], v1 offset:8192\n"
"v_add_f32 v32, v32, v147\n"
"v_mfma_f32_16x16x32_bf16 a[188:191], a[0:3], a[0:3], 0\n"
"v_cvt_pk_bf16_f32 v163, v146, v147\n"
"v_mfma_f32_16x16x32_bf16 v[152:155], a[208:211], v[96:99], v[136:139]\n"
"v_exp_f32 v148, v148\n"
"v_mfma_f32_16x16x32_bf16 v[152:155], a[212:215], v[100:103], v[152:155]\n"
"ds_read_b128 a[196:199], v1 offset:8704\n"
"v_add_f32 v33, v33, v148\n"
"v_mfma_f32_16x16x32_bf16 v[152:155], a[216:219], v[104:107], v[152:155]\n"
"v_exp_f32 v149, v149\n"
"v_mfma_f32_16x16x32_bf16 v[152:155], a[220:223], v[108:111], v[152:155]\n"
"v_add_f32 v33, v33, v149\n"
"v_mfma_f32_16x16x32_bf16 a[184:187], a[0:3], a[0:3], 0\n"
"v_cvt_pk_bf16_f32 v170, v148, v149\n"
"v_exp_f32 v150, v150\n"
"s_nop 0\n"
"v_add_f32 v33, v33, v150\n"
"v_exp_f32 v151, v151\n"
"ds_read_b128 a[200:203], v1 offset:9216\n"
"v_add_f32 v33, v33, v151\n"
"v_mfma_f32_16x16x32_bf16 a[180:183], a[0:3], a[0:3], 0\n"
"v_cvt_pk_bf16_f32 v171, v150, v151\n"
"v_mfma_f32_16x16x32_bf16 v[156:159], a[208:211], v[112:115], v[140:143]\n"
"v_exp_f32 v152, v152\n"
"v_mfma_f32_16x16x32_bf16 v[156:159], a[212:215], v[116:119], v[156:159]\n"
"ds_read_b128 a[204:207], v1 offset:9728\n"
"v_add_f32 v34, v34, v152\n"
"v_mfma_f32_16x16x32_bf16 v[156:159], a[216:219], v[120:123], v[156:159]\n"
"v_exp_f32 v153, v153\n"
"v_mfma_f32_16x16x32_bf16 v[156:159], a[220:223], v[124:127], v[156:159]\n"
"v_add_f32 v34, v34, v153\n"
"v_mfma_f32_16x16x32_bf16 a[176:179], a[0:3], a[0:3], 0\n"
"v_cvt_pk_bf16_f32 v178, v152, v153\n"
"s_add_u32 m0, s80, 0x0\n"
"v_exp_f32 v154, v154\n"
"s_nop 0\n"
"v_add_f32 v34, v34, v154\n"
"s_waitcnt lgkmcnt(0)\n"
"v_exp_f32 v155, v155\n"
"s_nop 0\n"
"v_add_f32 v34, v34, v155\n"
"v_mfma_f32_16x16x32_bf16 a[172:175], a[0:3], a[0:3], 0\n"
"v_cvt_pk_bf16_f32 v179, v154, v155\n"
"v_mfma_f32_16x16x32_bf16 v[144:147], a[192:195], v[64:67], v[128:131]\n"
"v_exp_f32 v156, v156\n"
"v_mfma_f32_16x16x32_bf16 v[144:147], a[196:199], v[68:71], v[144:147]\n"
"v_add_f32 v35, v35, v156\n"
"v_mfma_f32_16x16x32_bf16 v[144:147], a[200:203], v[72:75], v[144:147]\n"
"v_exp_f32 v157, v157\n"
"v_mfma_f32_16x16x32_bf16 v[144:147], a[204:207], v[76:79], v[144:147]\n"
"v_add_f32 v35, v35, v157\n"
"v_cvt_pk_bf16_f32 v186, v156, v157\n"
"v_exp_f32 v158, v158\n"
"buffer_load_dwordx4 v5, s[8:11], s28 offen lds\n"
"v_add_f32 v35, v35, v158\n"
"v_exp_f32 v159, v159\n"
"s_nop 0\n"
"v_add_f32 v35, v35, v159\n"
"v_mfma_f32_16x16x32_bf16 a[168:171], a[0:3], a[0:3], 0\n"
"v_cvt_pk_bf16_f32 v187, v158, v159\n"
"v_mfma_f32_16x16x32_bf16 v[148:151], a[192:195], v[80:83], v[132:135]\n"
"v_exp_f32 v144, v144\n"
"v_mfma_f32_16x16x32_bf16 v[148:151], a[196:199], v[84:87], v[148:151]\n"
"v_add_f32 v32, v32, v144\n"
"v_mfma_f32_16x16x32_bf16 v[148:151], a[200:203], v[88:91], v[148:151]\n"
"v_exp_f32 v145, v145\n"
"v_mfma_f32_16x16x32_bf16 v[148:151], a[204:207], v[92:95], v[148:151]\n"
"v_add_f32 v32, v32, v145\n"
"v_cvt_pk_bf16_f32 v164, v144, v145\n"
"v_exp_f32 v146, v146\n"
"buffer_load_dwordx4 v6, s[8:11], s28 offen offset:2048 lds\n"
"v_add_f32 v32, v32, v146\n"
"v_exp_f32 v147, v147\n"
"ds_read_b128 a[208:211], v1 offset:12288\n"
"v_add_f32 v32, v32, v147\n"
"v_mfma_f32_16x16x32_bf16 a[164:167], a[0:3], a[0:3], 0\n"
"v_cvt_pk_bf16_f32 v165, v146, v147\n"
"v_mfma_f32_16x16x32_bf16 v[152:155], a[192:195], v[96:99], v[136:139]\n"
"s_waitcnt vmcnt(6)\n"
"v_lshlrev_b32 v24, 16, v22\n"
"v_and_b32 v25, 0xffff0000, v22\n"
"v_lshlrev_b32 v26, 16, v23\n"
"v_and_b32 v27, 0xffff0000, v23\n"
"v_accvgpr_write_b32 a236, v24\n"
"v_accvgpr_write_b32 a237, v25\n"
"v_accvgpr_write_b32 a238, v26\n"
"v_accvgpr_write_b32 a239, v27\n"
"v_exp_f32 v148, v148\n"
"v_mfma_f32_16x16x32_bf16 v[152:155], a[196:199], v[100:103], v[152:155]\n"
"ds_read_b128 a[212:215], v1 offset:12800\n"
"v_add_f32 v33, v33, v148\n"
"v_mfma_f32_16x16x32_bf16 v[152:155], a[200:203], v[104:107], v[152:155]\n"
"v_exp_f32 v149, v149\n"
"v_mfma_f32_16x16x32_bf16 v[152:155], a[204:207], v[108:111], v[152:155]\n"
"v_add_f32 v33, v33, v149\n"
"v_mfma_f32_16x16x32_bf16 a[160:163], a[0:3], a[0:3], 0\n"
"v_cvt_pk_bf16_f32 v172, v148, v149\n"
"v_exp_f32 v150, v150\n"
"s_nop 0\n"
"v_add_f32 v33, v33, v150\n"
"v_exp_f32 v151, v151\n"
"ds_read_b128 a[216:219], v1 offset:13312\n"
"v_add_f32 v33, v33, v151\n"
"v_mfma_f32_16x16x32_bf16 a[156:159], a[0:3], a[0:3], 0\n"
"v_cvt_pk_bf16_f32 v173, v150, v151\n"
"v_mfma_f32_16x16x32_bf16 v[156:159], a[192:195], v[112:115], v[140:143]\n"
"v_exp_f32 v152, v152\n"
"v_mfma_f32_16x16x32_bf16 v[156:159], a[196:199], v[116:119], v[156:159]\n"
"ds_read_b128 a[220:223], v1 offset:13824\n"
"v_add_f32 v34, v34, v152\n"
"v_mfma_f32_16x16x32_bf16 v[156:159], a[200:203], v[120:123], v[156:159]\n"
"v_exp_f32 v153, v153\n"
"v_mfma_f32_16x16x32_bf16 v[156:159], a[204:207], v[124:127], v[156:159]\n"
"v_add_f32 v34, v34, v153\n"
"v_mfma_f32_16x16x32_bf16 a[152:155], a[0:3], a[0:3], 0\n"
"v_cvt_pk_bf16_f32 v180, v152, v153\n"
"v_add_u32 v1, s79, v4\n"
"s_add_u32 m0, s80, 0x1000\n"
"v_exp_f32 v154, v154\n"
"s_nop 0\n"
"v_add_f32 v34, v34, v154\n"
"s_waitcnt lgkmcnt(0)\n"
"v_exp_f32 v155, v155\n"
"s_nop 0\n"
"v_add_f32 v34, v34, v155\n"
"v_mfma_f32_16x16x32_bf16 a[148:151], a[0:3], a[0:3], 0\n"
"v_cvt_pk_bf16_f32 v181, v154, v155\n"
"v_mfma_f32_16x16x32_bf16 v[144:147], a[208:211], v[64:67], v[128:131]\n"
"v_exp_f32 v156, v156\n"
"v_mfma_f32_16x16x32_bf16 v[144:147], a[212:215], v[68:71], v[144:147]\n"
"v_add_f32 v35, v35, v156\n"
"v_add_u32 v2, s78, v11\n"
"v_mfma_f32_16x16x32_bf16 v[144:147], a[216:219], v[72:75], v[144:147]\n"
"v_exp_f32 v157, v157\n"
"v_mfma_f32_16x16x32_bf16 v[144:147], a[220:223], v[76:79], v[144:147]\n"
"v_add_f32 v35, v35, v157\n"
"v_cvt_pk_bf16_f32 v188, v156, v157\n"
"v_exp_f32 v158, v158\n"
"buffer_load_dwordx4 v7, s[8:11], s28 offen lds\n"
"v_add_f32 v35, v35, v158\n"
"s_add_u32 m0, s71, 0x21000\n"
"v_exp_f32 v159, v159\n"
"s_nop 0\n"
"v_add_f32 v35, v35, v159\n"
"v_mfma_f32_16x16x32_bf16 a[144:147], a[0:3], a[0:3], a[236:239]\n"
"buffer_load_dwordx4 v44, s[16:19], s82 offen lds\n"
"v_cvt_pk_bf16_f32 v189, v158, v159\n"
"v_mfma_f32_16x16x32_bf16 v[148:151], a[208:211], v[80:83], v[132:135]\n"
"s_add_u32 m0, s80, 0x1000\n"
"v_exp_f32 v144, v144\n"
"v_mfma_f32_16x16x32_bf16 v[148:151], a[212:215], v[84:87], v[148:151]\n"
"v_add_f32 v32, v32, v144\n"
"v_mfma_f32_16x16x32_bf16 v[148:151], a[216:219], v[88:91], v[148:151]\n"
"s_mov_b32 s81, s78\n"
"v_exp_f32 v145, v145\n"
"v_mfma_f32_16x16x32_bf16 v[148:151], a[220:223], v[92:95], v[148:151]\n"
"v_add_f32 v32, v32, v145\n"
"v_cvt_pk_bf16_f32 v166, v144, v145\n"
"s_mov_b32 s78, s79\n"
"v_exp_f32 v146, v146\n"
"buffer_load_dwordx4 v8, s[8:11], s28 offen offset:2048 lds\n"
"v_add_f32 v32, v32, v146\n"
"v_xor_b32 v3, 32, v2\n"
"s_waitcnt vmcnt(20) lgkmcnt(0)\n"
"s_barrier\n"
"s_add_u32 m0, s71, 0x21000\n"
"v_exp_f32 v147, v147\n"
"ds_read_b128 a[192:195], v1 offset:0\n"
"v_add_f32 v32, v32, v147\n"
"v_mfma_f32_16x16x32_bf16 a[140:143], a[0:3], a[0:3], a[236:239]\n"
"buffer_load_dwordx4 v45, s[16:19], s82 offen offset:1024 lds\n"
"v_cvt_pk_bf16_f32 v167, v146, v147\n"
"v_mfma_f32_16x16x32_bf16 v[152:155], a[208:211], v[96:99], v[136:139]\n"
"v_exp_f32 v148, v148\n"
"v_mfma_f32_16x16x32_bf16 v[152:155], a[212:215], v[100:103], v[152:155]\n"
"ds_read_b128 a[196:199], v1 offset:512\n"
"v_add_f32 v33, v33, v148\n"
"v_mfma_f32_16x16x32_bf16 v[152:155], a[216:219], v[104:107], v[152:155]\n"
"v_exp_f32 v149, v149\n"
"v_mfma_f32_16x16x32_bf16 v[152:155], a[220:223], v[108:111], v[152:155]\n"
"ds_read_b64_tr_b16 a[224:225], v2 offset:0\n"
"v_add_f32 v33, v33, v149\n"
"v_mfma_f32_16x16x32_bf16 a[136:139], a[0:3], a[0:3], a[236:239]\n"
"buffer_load_dwordx4 v46, s[16:19], s82 offen offset:2048 lds\n"
"v_cvt_pk_bf16_f32 v174, v148, v149\n"
"s_waitcnt vmcnt(17)\n"
"v_exp_f32 v150, v150\n"
"ds_read_b64_tr_b16 a[226:227], v2 offset:4096\n"
"v_add_f32 v33, v33, v150\n"
"s_mov_b32 s79, s80\n"
"v_exp_f32 v151, v151\n"
"ds_read_b128 a[200:203], v1 offset:1024\n"
"v_add_f32 v33, v33, v151\n"
"v_mfma_f32_16x16x32_bf16 a[132:135], a[0:3], a[0:3], a[236:239]\n"
"ds_read_b64_tr_b16 v[16:17], v48 offset:0\n"
"v_cvt_pk_bf16_f32 v175, v150, v151\n"
"v_mfma_f32_16x16x32_bf16 v[156:159], a[208:211], v[112:115], v[140:143]\n"
"s_add_u32 s28, s28, 0x4000\n"
"v_exp_f32 v152, v152\n"
"v_mfma_f32_16x16x32_bf16 v[156:159], a[212:215], v[116:119], v[156:159]\n"
"ds_read_b128 a[204:207], v1 offset:1536\n"
"v_add_f32 v34, v34, v152\n"
"v_mfma_f32_16x16x32_bf16 v[156:159], a[216:219], v[120:123], v[156:159]\n"
"s_mov_b32 s80, s81\n"
"v_exp_f32 v153, v153\n"
"v_mfma_f32_16x16x32_bf16 v[156:159], a[220:223], v[124:127], v[156:159]\n"
"ds_read_b64_tr_b16 a[228:229], v2 offset:8192\n"
"v_add_f32 v34, v34, v153\n"
"v_mfma_f32_16x16x32_bf16 a[128:131], a[0:3], a[0:3], a[236:239]\n"
"ds_read_b64_tr_b16 v[18:19], v48 offset:512\n"
"v_cvt_pk_bf16_f32 v182, v152, v153\n"
"s_add_u32 m0, s79, 0xc000\n"
"v_exp_f32 v154, v154\n"
"ds_read_b64_tr_b16 a[230:231], v2 offset:12288\n"
"v_add_f32 v34, v34, v154\n"
"s_waitcnt lgkmcnt(3)\n"
"v_exp_f32 v155, v155\n"
"L_LOOPA0%=:\n"
"ds_read_b64_tr_b16 a[232:233], v3 offset:0\n"
"v_add_f32 v34, v34, v155\n"
"v_cvt_pk_bf16_f32 v183, v154, v155\n"
"v_mfma_f32_16x16x32_bf16 v[144:147], a[192:195], v[64:67], v[128:131]\n"
"v_exp_f32 v156, v156\n"
"v_mfma_f32_16x16x32_bf16 v[144:147], a[196:199], v[68:71], v[144:147]\n"
"ds_read_b64_tr_b16 a[234:235], v3 offset:4096\n"
"v_add_f32 v35, v35, v156\n"
"v_mfma_f32_16x16x32_bf16 v[144:147], a[200:203], v[72:75], v[144:147]\n"
"s_waitcnt lgkmcnt(2)\n"
"v_exp_f32 v157, v157\n"
"v_mfma_f32_16x16x32_bf16 v[144:147], a[204:207], v[76:79], v[144:147]\n"
"v_add_f32 v35, v35, v157\n"
"v_cvt_pk_bf16_f32 v190, v156, v157\n"
"v_mfma_f32_16x16x32_bf16 a[0:3], v[160:163], a[224:227], a[0:3]\n"
"v_exp_f32 v158, v158\n"
"v_mfma_f32_16x16x32_bf16 a[0:3], v[164:167], a[228:231], a[0:3]\n"
"buffer_load_dwordx4 v9, s[12:15], s28 offen lds\n"
"v_add_f32 v35, v35, v158\n"
"v_mfma_f32_16x16x32_bf16 a[4:7], v[172:175], a[228:231], a[4:7]\n"
"v_exp_f32 v159, v159\n"
"v_mfma_f32_16x16x32_bf16 a[4:7], v[168:171], a[224:227], a[4:7]\n"
"ds_read_b64_tr_b16 a[236:237], v3 offset:8192\n"
"v_add_f32 v35, v35, v159\n"
"v_cvt_pk_bf16_f32 v191, v158, v159\n"
"v_mfma_f32_16x16x32_bf16 v[148:151], a[192:195], v[80:83], v[132:135]\n"
"v_exp_f32 v144, v144\n"
"v_mfma_f32_16x16x32_bf16 v[148:151], a[196:199], v[84:87], v[148:151]\n"
"ds_read_b64_tr_b16 a[238:239], v3 offset:12288\n"
"v_add_f32 v32, v32, v144\n"
"v_mfma_f32_16x16x32_bf16 v[148:151], a[200:203], v[88:91], v[148:151]\n"
"v_exp_f32 v145, v145\n"
"v_mfma_f32_16x16x32_bf16 v[148:151], a[204:207], v[92:95], v[148:151]\n"
"v_add_f32 v32, v32, v145\n"
"v_cvt_pk_bf16_f32 v192, v144, v145\n"
"v_mfma_f32_16x16x32_bf16 a[8:11], v[176:179], a[224:227], a[8:11]\n"
"v_exp_f32 v146, v146\n"
"v_mfma_f32_16x16x32_bf16 a[8:11], v[180:183], a[228:231], a[8:11]\n"
"buffer_load_dwordx4 v9, s[12:15], s28 offen offset:2048 lds\n"
"v_add_f32 v32, v32, v146\n"
"v_mfma_f32_16x16x32_bf16 a[12:15], v[188:191], a[228:231], a[12:15]\n"
"v_exp_f32 v147, v147\n"
"v_mfma_f32_16x16x32_bf16 a[12:15], v[184:187], a[224:227], a[12:15]\n"
"ds_read_b128 a[208:211], v1 offset:4096\n"
"v_add_f32 v32, v32, v147\n"
"v_cvt_pk_bf16_f32 v193, v146, v147\n"
"v_mfma_f32_16x16x32_bf16 v[152:155], a[192:195], v[96:99], v[136:139]\n"
"buffer_load_dword v15, v63, s[64:67], s68 offen\n"
"v_exp_f32 v148, v148\n"
"v_mfma_f32_16x16x32_bf16 v[152:155], a[196:199], v[100:103], v[152:155]\n"
"ds_read_b128 a[212:215], v1 offset:4608\n"
"v_add_f32 v33, v33, v148\n"
"v_mfma_f32_16x16x32_bf16 v[152:155], a[200:203], v[104:107], v[152:155]\n"
"s_waitcnt lgkmcnt(2)\n"
"s_add_u32 s68, s68, s69\n"
"v_exp_f32 v149, v149\n"
"v_mfma_f32_16x16x32_bf16 v[152:155], a[204:207], v[108:111], v[152:155]\n"
"ds_read_b64_tr_b16 a[224:225], v2 offset:512\n"
"v_add_f32 v33, v33, v149\n"
"v_cvt_pk_bf16_f32 v200, v148, v149\n"
"v_mfma_f32_16x16x32_bf16 a[16:19], v[160:163], a[232:235], a[16:19]\n"
"s_add_u32 m0, s71, 0x21000\n"
"v_exp_f32 v150, v150\n"
"v_mfma_f32_16x16x32_bf16 a[16:19], v[164:167], a[236:239], a[16:19]\n"
"ds_read_b64_tr_b16 a[226:227], v2 offset:4608\n"
"v_add_f32 v33, v33, v150\n"
"v_mfma_f32_16x16x32_bf16 a[20:23], v[172:175], a[236:239], a[20:23]\n"
"s_waitcnt vmcnt(12) lgkmcnt(9)\n"
"v_exp_f32 v151, v151\n"
"v_mfma_f32_16x16x32_bf16 a[20:23], v[168:171], a[232:235], a[20:23]\n"
"ds_read_b128 a[216:219], v1 offset:5120\n"
"v_add_f32 v33, v33, v151\n"
"v_mfma_f32_16x16x32_bf16 a[128:131], v[16:19], v[224:227], a[128:131]\n"
"buffer_load_dwordx4 v47, s[16:19], s82 offen offset:3072 lds\n"
"v_cvt_pk_bf16_f32 v201, v150, v151\n"
"v_mfma_f32_16x16x32_bf16 v[156:159], a[192:195], v[112:115], v[140:143]\n"
"v_exp_f32 v152, v152\n"
"v_mfma_f32_16x16x32_bf16 v[156:159], a[196:199], v[116:119], v[156:159]\n"
"ds_read_b128 a[220:223], v1 offset:5632\n"
"v_add_f32 v34, v34, v152\n"
"v_mfma_f32_16x16x32_bf16 v[156:159], a[200:203], v[120:123], v[156:159]\n"
"v_exp_f32 v153, v153\n"
"v_mfma_f32_16x16x32_bf16 v[156:159], a[204:207], v[124:127], v[156:159]\n"
"ds_read_b64_tr_b16 a[228:229], v2 offset:8704\n"
"v_add_f32 v34, v34, v153\n"
"v_mfma_f32_16x16x32_bf16 a[132:135], v[16:19], v[228:231], a[132:135]\n"
"ds_read_b64_tr_b16 v[20:21], v49 offset:0\n"
"v_cvt_pk_bf16_f32 v208, v152, v153\n"
"v_mfma_f32_16x16x32_bf16 a[24:27], v[176:179], a[232:235], a[24:27]\n"
"s_add_u32 m0, s79, 0xd000\n"
"v_exp_f32 v154, v154\n"
"v_mfma_f32_16x16x32_bf16 a[24:27], v[180:183], a[236:239], a[24:27]\n"
"ds_read_b64_tr_b16 a[230:231], v2 offset:12800\n"
"v_add_f32 v34, v34, v154\n"
"v_mfma_f32_16x16x32_bf16 a[28:31], v[188:191], a[236:239], a[28:31]\n"
"s_waitcnt lgkmcnt(3)\n"
"v_exp_f32 v155, v155\n"
"v_mfma_f32_16x16x32_bf16 a[28:31], v[184:187], a[232:235], a[28:31]\n"
"ds_read_b64_tr_b16 a[232:233], v3 offset:512\n"
"v_add_f32 v34, v34, v155\n"
"v_mfma_f32_16x16x32_bf16 a[136:139], v[16:19], v[232:235], a[136:139]\n"
"ds_read_b64_tr_b16 v[22:23], v49 offset:512\n"
"v_cvt_pk_bf16_f32 v209, v154, v155\n"
"v_mfma_f32_16x16x32_bf16 v[144:147], a[208:211], v[64:67], v[128:131]\n"
"s_add_u32 s83, s83, 5120\n"
"v_exp_f32 v156, v156\n"
"v_mfma_f32_16x16x32_bf16 v[144:147], a[212:215], v[68:71], v[144:147]\n"
"ds_read_b64_tr_b16 a[234:235], v3 offset:4608\n"
"v_add_f32 v35, v35, v156\n"
"v_mfma_f32_16x16x32_bf16 v[144:147], a[216:219], v[72:75], v[144:147]\n"
"s_waitcnt lgkmcnt(3)\n"
"v_exp_f32 v157, v157\n"
"v_mfma_f32_16x16x32_bf16 v[144:147], a[220:223], v[76:79], v[144:147]\n"
"v_add_f32 v35, v35, v157\n"
"v_cvt_pk_bf16_f32 v216, v156, v157\n"
"v_mfma_f32_16x16x32_bf16 a[32:35], v[160:163], a[224:227], a[32:35]\n"
"v_exp_f32 v158, v158\n"
"v_mfma_f32_16x16x32_bf16 a[32:35], v[164:167], a[228:231], a[32:35]\n"
"buffer_load_dwordx4 v10, s[12:15], s28 offen lds\n"
"v_add_f32 v35, v35, v158\n"
"v_mfma_f32_16x16x32_bf16 a[36:39], v[172:175], a[228:231], a[36:39]\n"
"s_waitcnt vmcnt(11)\n"
"v_exp_f32 v159, v159\n"
"v_mfma_f32_16x16x32_bf16 a[36:39], v[168:171], a[224:227], a[36:39]\n"
"ds_read_b64_tr_b16 a[236:237], v3 offset:8704\n"
"v_add_f32 v35, v35, v159\n"
"v_mfma_f32_16x16x32_bf16 a[140:143], v[16:19], v[236:239], a[140:143]\n"
"buffer_load_dwordx4 v[244:247], v12, s[20:23], s83 offen sc1\n"
"v_cvt_pk_bf16_f32 v217, v158, v159\n"
"v_mfma_f32_16x16x32_bf16 v[148:151], a[208:211], v[80:83], v[132:135]\n"
"s_add_u32 s82, s82, 110592\n"
"v_exp_f32 v144, v144\n"
"v_mfma_f32_16x16x32_bf16 v[148:151], a[212:215], v[84:87], v[148:151]\n"
"ds_read_b64_tr_b16 a[238:239], v3 offset:12800\n"
"v_add_f32 v32, v32, v144\n"
"v_mfma_f32_16x16x32_bf16 v[148:151], a[216:219], v[88:91], v[148:151]\n"
"v_exp_f32 v145, v145\n"
"v_mfma_f32_16x16x32_bf16 v[148:151], a[220:223], v[92:95], v[148:151]\n"
"v_add_f32 v32, v32, v145\n"
"v_cvt_pk_bf16_f32 v194, v144, v145\n"
"v_mfma_f32_16x16x32_bf16 a[40:43], v[176:179], a[224:227], a[40:43]\n"
"v_exp_f32 v146, v146\n"
"v_mfma_f32_16x16x32_bf16 a[40:43], v[180:183], a[228:231], a[40:43]\n"
"buffer_load_dwordx4 v10, s[12:15], s28 offen offset:2048 lds\n"
"v_add_f32 v32, v32, v146\n"
"v_mfma_f32_16x16x32_bf16 a[44:47], v[188:191], a[228:231], a[44:47]\n"
"v_exp_f32 v147, v147\n"
"v_mfma_f32_16x16x32_bf16 a[44:47], v[184:187], a[224:227], a[44:47]\n"
"ds_read_b128 a[192:195], v1 offset:8192\n"
"v_add_f32 v32, v32, v147\n"
"v_mfma_f32_16x16x32_bf16 a[144:147], v[16:19], v[240:243], a[144:147]\n"
"buffer_load_dwordx4 v[248:251], v12, s[20:23], s83 offen offset:1024 sc1\n"
"v_cvt_pk_bf16_f32 v195, v146, v147\n"
"v_mfma_f32_16x16x32_bf16 v[152:155], a[208:211], v[96:99], v[136:139]\n"
"s_waitcnt lgkmcnt(4)\n"
"v_exp_f32 v148, v148\n"
"v_mfma_f32_16x16x32_bf16 v[152:155], a[212:215], v[100:103], v[152:155]\n"
"ds_read_b128 a[196:199], v1 offset:8704\n"
"v_add_f32 v33, v33, v148\n"
"v_mfma_f32_16x16x32_bf16 v[152:155], a[216:219], v[104:107], v[152:155]\n"
"s_waitcnt lgkmcnt(2)\n"
"v_exp_f32 v149, v149\n"
"v_mfma_f32_16x16x32_bf16 v[152:155], a[220:223], v[108:111], v[152:155]\n"
"ds_read_b64_tr_b16 a[224:225], v2 offset:1024\n"
"v_add_f32 v33, v33, v149\n"
"v_mfma_f32_16x16x32_bf16 a[148:151], v[20:23], v[224:227], a[148:151]\n"
"buffer_load_dwordx4 v[252:255], v12, s[20:23], s83 offen offset:2048 sc1\n"
"v_cvt_pk_bf16_f32 v202, v148, v149\n"
"v_mfma_f32_16x16x32_bf16 a[48:51], v[160:163], a[232:235], a[48:51]\n"
"v_exp_f32 v150, v150\n"
"v_mfma_f32_16x16x32_bf16 a[48:51], v[164:167], a[236:239], a[48:51]\n"
"ds_read_b64_tr_b16 a[226:227], v2 offset:5120\n"
"v_add_f32 v33, v33, v150\n"
"v_mfma_f32_16x16x32_bf16 a[52:55], v[172:175], a[236:239], a[52:55]\n"
"v_exp_f32 v151, v151\n"
"v_mfma_f32_16x16x32_bf16 a[52:55], v[168:171], a[232:235], a[52:55]\n"
"ds_read_b128 a[200:203], v1 offset:9216\n"
"v_add_f32 v33, v33, v151\n"
"v_mfma_f32_16x16x32_bf16 a[152:155], v[20:23], v[228:231], a[152:155]\n"
"ds_read_b64_tr_b16 v[24:25], v50 offset:0\n"
"v_cvt_pk_bf16_f32 v203, v150, v151\n"
"v_mfma_f32_16x16x32_bf16 v[156:159], a[208:211], v[112:115], v[140:143]\n"
"v_exp_f32 v152, v152\n"
"v_mfma_f32_16x16x32_bf16 v[156:159], a[212:215], v[116:119], v[156:159]\n"
"ds_read_b128 a[204:207], v1 offset:9728\n"
"v_add_f32 v34, v34, v152\n"
"v_mfma_f32_16x16x32_bf16 v[156:159], a[216:219], v[120:123], v[156:159]\n"
"v_exp_f32 v153, v153\n"
"v_mfma_f32_16x16x32_bf16 v[156:159], a[220:223], v[124:127], v[156:159]\n"
"ds_read_b64_tr_b16 a[228:229], v2 offset:9216\n"
"v_add_f32 v34, v34, v153\n"
"v_mfma_f32_16x16x32_bf16 a[156:159], v[20:23], v[232:235], a[156:159]\n"
"ds_read_b64_tr_b16 v[26:27], v50 offset:512\n"
"v_cvt_pk_bf16_f32 v210, v152, v153\n"
"v_mfma_f32_16x16x32_bf16 a[56:59], v[176:179], a[232:235], a[56:59]\n"
"s_add_u32 m0, s80, 0x0\n"
"v_exp_f32 v154, v154\n"
"v_mfma_f32_16x16x32_bf16 a[56:59], v[180:183], a[236:239], a[56:59]\n"
"ds_read_b64_tr_b16 a[230:231], v2 offset:13312\n"
"v_add_f32 v34, v34, v154\n"
"v_mfma_f32_16x16x32_bf16 a[60:63], v[188:191], a[236:239], a[60:63]\n"
"s_waitcnt lgkmcnt(3)\n"
"v_exp_f32 v155, v155\n"
"v_mfma_f32_16x16x32_bf16 a[60:63], v[184:187], a[232:235], a[60:63]\n"
"ds_read_b64_tr_b16 a[232:233], v3 offset:1024\n"
"v_add_f32 v34, v34, v155\n"
"v_mfma_f32_16x16x32_bf16 a[160:163], v[20:23], v[236:239], a[160:163]\n"
"buffer_load_dwordx4 v[36:39], v12, s[20:23], s83 offen offset:3072 sc1\n"
"v_cvt_pk_bf16_f32 v211, v154, v155\n"
"v_mfma_f32_16x16x32_bf16 v[144:147], a[192:195], v[64:67], v[128:131]\n"
"v_exp_f32 v156, v156\n"
"v_mfma_f32_16x16x32_bf16 v[144:147], a[196:199], v[68:71], v[144:147]\n"
"ds_read_b64_tr_b16 a[234:235], v3 offset:5120\n"
"v_add_f32 v35, v35, v156\n"
"v_mfma_f32_16x16x32_bf16 v[144:147], a[200:203], v[72:75], v[144:147]\n"
"s_waitcnt lgkmcnt(2)\n"
"v_exp_f32 v157, v157\n"
"v_mfma_f32_16x16x32_bf16 v[144:147], a[204:207], v[76:79], v[144:147]\n"
"v_add_f32 v35, v35, v157\n"
"v_cvt_pk_bf16_f32 v218, v156, v157\n"
"v_mfma_f32_16x16x32_bf16 a[64:67], v[160:163], a[224:227], a[64:67]\n"
"v_exp_f32 v158, v158\n"
"v_mfma_f32_16x16x32_bf16 a[64:67], v[164:167], a[228:231], a[64:67]\n"
"buffer_load_dwordx4 v5, s[8:11], s28 offen lds\n"
"v_add_f32 v35, v35, v158\n"
"v_mfma_f32_16x16x32_bf16 a[68:71], v[172:175], a[228:231], a[68:71]\n"
"v_exp_f32 v159, v159\n"
"v_mfma_f32_16x16x32_bf16 a[68:71], v[168:171], a[224:227], a[68:71]\n"
"ds_read_b64_tr_b16 a[236:237], v3 offset:9216\n"
"v_add_f32 v35, v35, v159\n"
"v_mfma_f32_16x16x32_bf16 a[164:167], v[20:23], v[240:243], a[164:167]\n"
"buffer_load_dwordx4 v[40:43], v52, s[20:23], s83 offen sc1\n"
"v_cvt_pk_bf16_f32 v219, v158, v159\n"
"v_mfma_f32_16x16x32_bf16 v[148:151], a[192:195], v[80:83], v[132:135]\n"
"v_exp_f32 v144, v144\n"
"v_mfma_f32_16x16x32_bf16 v[148:151], a[196:199], v[84:87], v[148:151]\n"
"ds_read_b64_tr_b16 a[238:239], v3 offset:13312\n"
"v_add_f32 v32, v32, v144\n"
"v_mfma_f32_16x16x32_bf16 v[148:151], a[200:203], v[88:91], v[148:151]\n"
"v_exp_f32 v145, v145\n"
"v_mfma_f32_16x16x32_bf16 v[148:151], a[204:207], v[92:95], v[148:151]\n"
"v_add_f32 v32, v32, v145\n"
"v_cvt_pk_bf16_f32 v196, v144, v145\n"
"v_mfma_f32_16x16x32_bf16 a[72:75], v[176:179], a[224:227], a[72:75]\n"
"v_exp_f32 v146, v146\n"
"v_mfma_f32_16x16x32_bf16 a[72:75], v[180:183], a[228:231], a[72:75]\n"
"buffer_load_dwordx4 v6, s[8:11], s28 offen offset:2048 lds\n"
"v_add_f32 v32, v32, v146\n"
"v_mfma_f32_16x16x32_bf16 a[76:79], v[188:191], a[228:231], a[76:79]\n"
"s_waitcnt lgkmcnt(5)\n"
"v_exp_f32 v147, v147\n"
"v_mfma_f32_16x16x32_bf16 a[76:79], v[184:187], a[224:227], a[76:79]\n"
"ds_read_b128 a[208:211], v1 offset:12288\n"
"v_add_f32 v32, v32, v147\n"
"v_mfma_f32_16x16x32_bf16 a[168:171], v[24:27], v[224:227], a[168:171]\n"
"v_cvt_pk_bf16_f32 v197, v146, v147\n"
"v_mfma_f32_16x16x32_bf16 v[152:155], a[192:195], v[96:99], v[136:139]\n"
"v_exp_f32 v148, v148\n"
"v_mfma_f32_16x16x32_bf16 v[152:155], a[196:199], v[100:103], v[152:155]\n"
"ds_read_b128 a[212:215], v1 offset:12800\n"
"v_add_f32 v33, v33, v148\n"
"v_mfma_f32_16x16x32_bf16 v[152:155], a[200:203], v[104:107], v[152:155]\n"
"s_waitcnt lgkmcnt(2)\n"
"v_exp_f32 v149, v149\n"
"v_mfma_f32_16x16x32_bf16 v[152:155], a[204:207], v[108:111], v[152:155]\n"
"ds_read_b64_tr_b16 a[224:225], v2 offset:1536\n"
"v_add_f32 v33, v33, v149\n"
"v_mfma_f32_16x16x32_bf16 a[172:175], v[24:27], v[228:231], a[172:175]\n"
"ds_read_b64_tr_b16 v[28:29], v51 offset:0\n"
"v_cvt_pk_bf16_f32 v204, v148, v149\n"
"v_mfma_f32_16x16x32_bf16 a[80:83], v[160:163], a[232:235], a[80:83]\n"
"v_exp_f32 v150, v150\n"
"v_mfma_f32_16x16x32_bf16 a[80:83], v[164:167], a[236:239], a[80:83]\n"
"ds_read_b64_tr_b16 a[226:227], v2 offset:5632\n"
"v_add_f32 v33, v33, v150\n"
"v_mfma_f32_16x16x32_bf16 a[84:87], v[172:175], a[236:239], a[84:87]\n"
"v_exp_f32 v151, v151\n"
"v_mfma_f32_16x16x32_bf16 a[84:87], v[168:171], a[232:235], a[84:87]\n"
"ds_read_b128 a[216:219], v1 offset:13312\n"
"v_add_f32 v33, v33, v151\n"
"v_mfma_f32_16x16x32_bf16 a[176:179], v[24:27], v[232:235], a[176:179]\n"
"ds_read_b64_tr_b16 v[30:31], v51 offset:512\n"
"v_cvt_pk_bf16_f32 v205, v150, v151\n"
"v_mfma_f32_16x16x32_bf16 v[156:159], a[192:195], v[112:115], v[140:143]\n"
"v_exp_f32 v152, v152\n"
"v_mfma_f32_16x16x32_bf16 v[156:159], a[196:199], v[116:119], v[156:159]\n"
"ds_read_b128 a[220:223], v1 offset:13824\n"
"v_add_f32 v34, v34, v152\n"
"v_mfma_f32_16x16x32_bf16 v[156:159], a[200:203], v[120:123], v[156:159]\n"
"v_exp_f32 v153, v153\n"
"v_mfma_f32_16x16x32_bf16 v[156:159], a[204:207], v[124:127], v[156:159]\n"
"ds_read_b64_tr_b16 a[228:229], v2 offset:9728\n"
"v_add_f32 v34, v34, v153\n"
"v_mfma_f32_16x16x32_bf16 a[180:183], v[24:27], v[236:239], a[180:183]\n"
"v_cvt_pk_bf16_f32 v212, v152, v153\n"
"v_add_u32 v1, s79, v4\n"
"v_mfma_f32_16x16x32_bf16 a[88:91], v[176:179], a[232:235], a[88:91]\n"
"s_add_u32 m0, s80, 0x1000\n"
"v_exp_f32 v154, v154\n"
"v_mfma_f32_16x16x32_bf16 a[88:91], v[180:183], a[236:239], a[88:91]\n"
"ds_read_b64_tr_b16 a[230:231], v2 offset:13824\n"
"v_add_f32 v34, v34, v154\n"
"v_mfma_f32_16x16x32_bf16 a[92:95], v[188:191], a[236:239], a[92:95]\n"
"s_waitcnt lgkmcnt(2)\n"
"v_exp_f32 v155, v155\n"
"v_mfma_f32_16x16x32_bf16 a[92:95], v[184:187], a[232:235], a[92:95]\n"
"ds_read_b64_tr_b16 a[232:233], v3 offset:1536\n"
"v_add_f32 v34, v34, v155\n"
"v_mfma_f32_16x16x32_bf16 a[184:187], v[24:27], v[240:243], a[184:187]\n"
"buffer_load_dwordx4 v7, s[8:11], s28 offen lds\n"
"v_cvt_pk_bf16_f32 v213, v154, v155\n"
"v_mfma_f32_16x16x32_bf16 v[144:147], a[208:211], v[64:67], v[128:131]\n"
"v_exp_f32 v156, v156\n"
"v_mfma_f32_16x16x32_bf16 v[144:147], a[212:215], v[68:71], v[144:147]\n"
"ds_read_b64_tr_b16 a[234:235], v3 offset:5632\n"
"v_add_f32 v35, v35, v156\n"
"v_add_u32 v2, s78, v11\n"
"v_mfma_f32_16x16x32_bf16 v[144:147], a[216:219], v[72:75], v[144:147]\n"
"s_waitcnt lgkmcnt(2)\n"
"v_exp_f32 v157, v157\n"
"v_mfma_f32_16x16x32_bf16 v[144:147], a[220:223], v[76:79], v[144:147]\n"
"v_add_f32 v35, v35, v157\n"
"v_cvt_pk_bf16_f32 v220, v156, v157\n"
"v_mfma_f32_16x16x32_bf16 a[96:99], v[160:163], a[224:227], a[96:99]\n"
"s_waitcnt lgkmcnt(5)\n"
"v_exp_f32 v158, v158\n"
"v_mfma_f32_16x16x32_bf16 a[96:99], v[164:167], a[228:231], a[96:99]\n"
"buffer_load_dwordx4 v8, s[8:11], s28 offen offset:2048 lds\n"
"v_add_f32 v35, v35, v158\n"
"v_mfma_f32_16x16x32_bf16 a[100:103], v[172:175], a[228:231], a[100:103]\n"
"s_add_u32 m0, s71, 0x1d000\n"
"v_exp_f32 v159, v159\n"
"v_mfma_f32_16x16x32_bf16 a[100:103], v[168:171], a[224:227], a[100:103]\n"
"ds_read_b64_tr_b16 a[236:237], v3 offset:9728\n"
"v_add_f32 v35, v35, v159\n"
"v_mfma_f32_16x16x32_bf16 a[188:191], v[28:31], v[224:227], a[188:191]\n"
"buffer_load_dwordx4 v44, s[16:19], s82 offen lds\n"
"v_cvt_pk_bf16_f32 v221, v158, v159\n"
"v_mfma_f32_16x16x32_bf16 v[148:151], a[208:211], v[80:83], v[132:135]\n"
"v_exp_f32 v144, v144\n"
"v_mfma_f32_16x16x32_bf16 v[148:151], a[212:215], v[84:87], v[148:151]\n"
"ds_read_b64_tr_b16 a[238:239], v3 offset:13824\n"
"v_add_f32 v32, v32, v144\n"
"v_mfma_f32_16x16x32_bf16 v[148:151], a[216:219], v[88:91], v[148:151]\n"
"s_mov_b32 s81, s78\n"
"v_exp_f32 v145, v145\n"
"v_mfma_f32_16x16x32_bf16 v[148:151], a[220:223], v[92:95], v[148:151]\n"
"v_add_f32 v32, v32, v145\n"
"v_cvt_pk_bf16_f32 v198, v144, v145\n"
"v_mfma_f32_16x16x32_bf16 a[104:107], v[176:179], a[224:227], a[104:107]\n"
"s_mov_b32 s78, s79\n"
"v_exp_f32 v146, v146\n"
"v_mfma_f32_16x16x32_bf16 a[104:107], v[180:183], a[228:231], a[104:107]\n"
"v_add_f32 v32, v32, v146\n"
"v_xor_b32 v3, 32, v2\n"
"v_mfma_f32_16x16x32_bf16 a[108:111], v[188:191], a[228:231], a[108:111]\n"
"s_waitcnt vmcnt(18) lgkmcnt(0)\n"
"s_barrier\n"
"v_exp_f32 v147, v147\n"
"v_mfma_f32_16x16x32_bf16 a[108:111], v[184:187], a[224:227], a[108:111]\n"
"ds_read_b128 a[192:195], v1 offset:0\n"
"v_add_f32 v32, v32, v147\n"
"v_mfma_f32_16x16x32_bf16 a[240:243], v[28:31], v[228:231], a[240:243]\n"
"buffer_load_dwordx4 v45, s[16:19], s82 offen offset:1024 lds\n"
"v_cvt_pk_bf16_f32 v199, v146, v147\n"
"v_mfma_f32_16x16x32_bf16 v[152:155], a[208:211], v[96:99], v[136:139]\n"
"v_exp_f32 v148, v148\n"
"v_mfma_f32_16x16x32_bf16 v[152:155], a[212:215], v[100:103], v[152:155]\n"
"ds_read_b128 a[196:199], v1 offset:512\n"
"v_add_f32 v33, v33, v148\n"
"v_mfma_f32_16x16x32_bf16 v[152:155], a[216:219], v[104:107], v[152:155]\n"
"s_waitcnt lgkmcnt(2)\n"
"v_exp_f32 v149, v149\n"
"v_mfma_f32_16x16x32_bf16 v[152:155], a[220:223], v[108:111], v[152:155]\n"
"ds_read_b64_tr_b16 a[224:225], v2 offset:0\n"
"v_add_f32 v33, v33, v149\n"
"v_mfma_f32_16x16x32_bf16 a[244:247], v[28:31], v[232:235], a[244:247]\n"
"buffer_load_dwordx4 v46, s[16:19], s82 offen offset:2048 lds\n"
"v_cvt_pk_bf16_f32 v206, v148, v149\n"
"v_mfma_f32_16x16x32_bf16 a[112:115], v[160:163], a[232:235], a[112:115]\n"
"s_waitcnt vmcnt(14)\n"
"v_exp_f32 v150, v150\n"
"v_mfma_f32_16x16x32_bf16 a[112:115], v[164:167], a[236:239], a[112:115]\n"
"ds_read_b64_tr_b16 a[226:227], v2 offset:4096\n"
"v_add_f32 v33, v33, v150\n"
"v_mfma_f32_16x16x32_bf16 a[116:119], v[172:175], a[236:239], a[116:119]\n"
"s_mov_b32 s79, s80\n"
"v_exp_f32 v151, v151\n"
"v_mfma_f32_16x16x32_bf16 a[116:119], v[168:171], a[232:235], a[116:119]\n"
"ds_read_b128 a[200:203], v1 offset:1024\n"
"v_add_f32 v33, v33, v151\n"
"v_mfma_f32_16x16x32_bf16 a[248:251], v[28:31], v[236:239], a[248:251]\n"
"ds_read_b64_tr_b16 v[16:17], v48 offset:16384\n"
"v_cvt_pk_bf16_f32 v207, v150, v151\n"
"v_mfma_f32_16x16x32_bf16 v[156:159], a[208:211], v[112:115], v[140:143]\n"
"s_add_u32 s28, s28, 0x4000\n"
"v_exp_f32 v152, v152\n"
"v_mfma_f32_16x16x32_bf16 v[156:159], a[212:215], v[116:119], v[156:159]\n"
"ds_read_b128 a[204:207], v1 offset:1536\n"
"v_add_f32 v34, v34, v152\n"
"v_mfma_f32_16x16x32_bf16 v[156:159], a[216:219], v[120:123], v[156:159]\n"
"s_mov_b32 s80, s81\n"
"v_exp_f32 v153, v153\n"
"v_mfma_f32_16x16x32_bf16 v[156:159], a[220:223], v[124:127], v[156:159]\n"
"ds_read_b64_tr_b16 a[228:229], v2 offset:8192\n"
"v_add_f32 v34, v34, v153\n"
"v_mfma_f32_16x16x32_bf16 a[252:255], v[28:31], v[240:243], a[252:255]\n"
"ds_read_b64_tr_b16 v[18:19], v48 offset:16896\n"
"v_cvt_pk_bf16_f32 v214, v152, v153\n"
"v_mfma_f32_16x16x32_bf16 a[120:123], v[176:179], a[232:235], a[120:123]\n"
"s_add_u32 m0, s79, 0xc000\n"
"v_exp_f32 v154, v154\n"
"v_mfma_f32_16x16x32_bf16 a[120:123], v[180:183], a[236:239], a[120:123]\n"
"ds_read_b64_tr_b16 a[230:231], v2 offset:12288\n"
"v_add_f32 v34, v34, v154\n"
"v_mfma_f32_16x16x32_bf16 a[124:127], v[188:191], a[236:239], a[124:127]\n"
"s_waitcnt lgkmcnt(3)\n"
"v_exp_f32 v155, v155\n"
"v_mfma_f32_16x16x32_bf16 a[124:127], v[184:187], a[232:235], a[124:127]\n"
"ds_read_b64_tr_b16 a[232:233], v3 offset:0\n"
"v_add_f32 v34, v34, v155\n"
"v_cvt_pk_bf16_f32 v215, v154, v155\n"
"v_mfma_f32_16x16x32_bf16 v[144:147], a[192:195], v[64:67], v[128:131]\n"
"v_exp_f32 v156, v156\n"
"v_mfma_f32_16x16x32_bf16 v[144:147], a[196:199], v[68:71], v[144:147]\n"
"ds_read_b64_tr_b16 a[234:235], v3 offset:4096\n"
"v_add_f32 v35, v35, v156\n"
"v_mfma_f32_16x16x32_bf16 v[144:147], a[200:203], v[72:75], v[144:147]\n"
"s_waitcnt lgkmcnt(2)\n"
"v_exp_f32 v157, v157\n"
"v_mfma_f32_16x16x32_bf16 v[144:147], a[204:207], v[76:79], v[144:147]\n"
"v_add_f32 v35, v35, v157\n"
"v_cvt_pk_bf16_f32 v222, v156, v157\n"
"v_mfma_f32_16x16x32_bf16 a[0:3], v[192:195], a[224:227], a[0:3]\n"
"v_exp_f32 v158, v158\n"
"v_mfma_f32_16x16x32_bf16 a[0:3], v[196:199], a[228:231], a[0:3]\n"
"buffer_load_dwordx4 v9, s[12:15], s28 offen lds\n"
"v_add_f32 v35, v35, v158\n"
"v_mfma_f32_16x16x32_bf16 a[4:7], v[204:207], a[228:231], a[4:7]\n"
"v_exp_f32 v159, v159\n"
"v_mfma_f32_16x16x32_bf16 a[4:7], v[200:203], a[224:227], a[4:7]\n"
"ds_read_b64_tr_b16 a[236:237], v3 offset:8192\n"
"v_add_f32 v35, v35, v159\n"
"v_cvt_pk_bf16_f32 v223, v158, v159\n"
"v_mfma_f32_16x16x32_bf16 v[148:151], a[192:195], v[80:83], v[132:135]\n"
"v_exp_f32 v144, v144\n"
"v_mfma_f32_16x16x32_bf16 v[148:151], a[196:199], v[84:87], v[148:151]\n"
"ds_read_b64_tr_b16 a[238:239], v3 offset:12288\n"
"v_add_f32 v32, v32, v144\n"
"v_mfma_f32_16x16x32_bf16 v[148:151], a[200:203], v[88:91], v[148:151]\n"
"v_exp_f32 v145, v145\n"
"v_mfma_f32_16x16x32_bf16 v[148:151], a[204:207], v[92:95], v[148:151]\n"
"v_add_f32 v32, v32, v145\n"
"v_cvt_pk_bf16_f32 v160, v144, v145\n"
"v_mfma_f32_16x16x32_bf16 a[8:11], v[208:211], a[224:227], a[8:11]\n"
"v_exp_f32 v146, v146\n"
"v_mfma_f32_16x16x32_bf16 a[8:11], v[212:215], a[228:231], a[8:11]\n"
"buffer_load_dwordx4 v9, s[12:15], s28 offen offset:2048 lds\n"
"v_add_f32 v32, v32, v146\n"
"v_mfma_f32_16x16x32_bf16 a[12:15], v[220:223], a[228:231], a[12:15]\n"
"v_exp_f32 v147, v147\n"
"v_mfma_f32_16x16x32_bf16 a[12:15], v[216:219], a[224:227], a[12:15]\n"
"ds_read_b128 a[208:211], v1 offset:4096\n"
"v_add_f32 v32, v32, v147\n"
"v_cvt_pk_bf16_f32 v161, v146, v147\n"
"v_mfma_f32_16x16x32_bf16 v[152:155], a[192:195], v[96:99], v[136:139]\n"
"v_exp_f32 v148, v148\n"
"v_mfma_f32_16x16x32_bf16 v[152:155], a[196:199], v[100:103], v[152:155]\n"
"ds_read_b128 a[212:215], v1 offset:4608\n"
"v_add_f32 v33, v33, v148\n"
"v_mfma_f32_16x16x32_bf16 v[152:155], a[200:203], v[104:107], v[152:155]\n"
"s_waitcnt lgkmcnt(2)\n"
"v_exp_f32 v149, v149\n"
"v_mfma_f32_16x16x32_bf16 v[152:155], a[204:207], v[108:111], v[152:155]\n"
"ds_read_b64_tr_b16 a[224:225], v2 offset:512\n"
"v_add_f32 v33, v33, v149\n"
"v_cvt_pk_bf16_f32 v168, v148, v149\n"
"v_mfma_f32_16x16x32_bf16 a[16:19], v[192:195], a[232:235], a[16:19]\n"
"s_add_u32 m0, s71, 0x1d000\n"
"v_exp_f32 v150, v150\n"
"v_mfma_f32_16x16x32_bf16 a[16:19], v[196:199], a[236:239], a[16:19]\n"
"ds_read_b64_tr_b16 a[226:227], v2 offset:4608\n"
"v_add_f32 v33, v33, v150\n"
"v_mfma_f32_16x16x32_bf16 a[20:23], v[204:207], a[236:239], a[20:23]\n"
"s_waitcnt vmcnt(11) lgkmcnt(9)\n"
"v_exp_f32 v151, v151\n"
"v_mfma_f32_16x16x32_bf16 a[20:23], v[200:203], a[232:235], a[20:23]\n"
"ds_read_b128 a[216:219], v1 offset:5120\n"
"v_add_f32 v33, v33, v151\n"
"v_mfma_f32_16x16x32_bf16 a[128:131], v[16:19], v[244:247], a[128:131]\n"
"buffer_load_dwordx4 v47, s[16:19], s82 offen offset:3072 lds\n"
"v_cvt_pk_bf16_f32 v169, v150, v151\n"
"v_mfma_f32_16x16x32_bf16 v[156:159], a[192:195], v[112:115], v[140:143]\n"
"v_exp_f32 v152, v152\n"
"v_mfma_f32_16x16x32_bf16 v[156:159], a[196:199], v[116:119], v[156:159]\n"
"ds_read_b128 a[220:223], v1 offset:5632\n"
"v_add_f32 v34, v34, v152\n"
"v_mfma_f32_16x16x32_bf16 v[156:159], a[200:203], v[120:123], v[156:159]\n"
"v_exp_f32 v153, v153\n"
"v_mfma_f32_16x16x32_bf16 v[156:159], a[204:207], v[124:127], v[156:159]\n"
"ds_read_b64_tr_b16 a[228:229], v2 offset:8704\n"
"v_add_f32 v34, v34, v153\n"
"v_mfma_f32_16x16x32_bf16 a[132:135], v[16:19], v[248:251], a[132:135]\n"
"ds_read_b64_tr_b16 v[20:21], v49 offset:16384\n"
"v_cvt_pk_bf16_f32 v176, v152, v153\n"
"v_mfma_f32_16x16x32_bf16 a[24:27], v[208:211], a[232:235], a[24:27]\n"
"s_add_u32 m0, s79, 0xd000\n"
"v_exp_f32 v154, v154\n"
"v_mfma_f32_16x16x32_bf16 a[24:27], v[212:215], a[236:239], a[24:27]\n"
"ds_read_b64_tr_b16 a[230:231], v2 offset:12800\n"
"v_add_f32 v34, v34, v154\n"
"v_mfma_f32_16x16x32_bf16 a[28:31], v[220:223], a[236:239], a[28:31]\n"
"s_waitcnt lgkmcnt(3)\n"
"v_exp_f32 v155, v155\n"
"v_mfma_f32_16x16x32_bf16 a[28:31], v[216:219], a[232:235], a[28:31]\n"
"ds_read_b64_tr_b16 a[232:233], v3 offset:512\n"
"v_add_f32 v34, v34, v155\n"
"v_mfma_f32_16x16x32_bf16 a[136:139], v[16:19], v[252:255], a[136:139]\n"
"ds_read_b64_tr_b16 v[22:23], v49 offset:16896\n"
"v_cvt_pk_bf16_f32 v177, v154, v155\n"
"v_mfma_f32_16x16x32_bf16 v[144:147], a[208:211], v[64:67], v[128:131]\n"
"s_add_u32 s83, s83, 5120\n"
"v_exp_f32 v156, v156\n"
"v_mfma_f32_16x16x32_bf16 v[144:147], a[212:215], v[68:71], v[144:147]\n"
"ds_read_b64_tr_b16 a[234:235], v3 offset:4608\n"
"v_add_f32 v35, v35, v156\n"
"v_mfma_f32_16x16x32_bf16 v[144:147], a[216:219], v[72:75], v[144:147]\n"
"s_waitcnt lgkmcnt(3)\n"
"v_exp_f32 v157, v157\n"
"v_mfma_f32_16x16x32_bf16 v[144:147], a[220:223], v[76:79], v[144:147]\n"
"v_add_f32 v35, v35, v157\n"
"v_cvt_pk_bf16_f32 v184, v156, v157\n"
"v_mfma_f32_16x16x32_bf16 a[32:35], v[192:195], a[224:227], a[32:35]\n"
"v_exp_f32 v158, v158\n"
"v_mfma_f32_16x16x32_bf16 a[32:35], v[196:199], a[228:231], a[32:35]\n"
"buffer_load_dwordx4 v10, s[12:15], s28 offen lds\n"
"v_add_f32 v35, v35, v158\n"
"v_mfma_f32_16x16x32_bf16 a[36:39], v[204:207], a[228:231], a[36:39]\n"
"s_waitcnt vmcnt(10)\n"
"v_exp_f32 v159, v159\n"
"v_mfma_f32_16x16x32_bf16 a[36:39], v[200:203], a[224:227], a[36:39]\n"
"ds_read_b64_tr_b16 a[236:237], v3 offset:8704\n"
"v_add_f32 v35, v35, v159\n"
"v_mfma_f32_16x16x32_bf16 a[140:143], v[16:19], v[36:39], a[140:143]\n"
"buffer_load_dwordx4 v[224:227], v12, s[20:23], s83 offen sc1\n"
"v_cvt_pk_bf16_f32 v185, v158, v159\n"
"v_mfma_f32_16x16x32_bf16 v[148:151], a[208:211], v[80:83], v[132:135]\n"
"s_add_u32 s82, s82, 110592\n"
"v_exp_f32 v144, v144\n"
"v_mfma_f32_16x16x32_bf16 v[148:151], a[212:215], v[84:87], v[148:151]\n"
"ds_read_b64_tr_b16 a[238:239], v3 offset:12800\n"
"v_add_f32 v32, v32, v144\n"
"v_mfma_f32_16x16x32_bf16 v[148:151], a[216:219], v[88:91], v[148:151]\n"
"v_exp_f32 v145, v145\n"
"v_mfma_f32_16x16x32_bf16 v[148:151], a[220:223], v[92:95], v[148:151]\n"
"v_add_f32 v32, v32, v145\n"
"v_cvt_pk_bf16_f32 v162, v144, v145\n"
"v_mfma_f32_16x16x32_bf16 a[40:43], v[208:211], a[224:227], a[40:43]\n"
"v_exp_f32 v146, v146\n"
"v_mfma_f32_16x16x32_bf16 a[40:43], v[212:215], a[228:231], a[40:43]\n"
"buffer_load_dwordx4 v10, s[12:15], s28 offen offset:2048 lds\n"
"v_add_f32 v32, v32, v146\n"
"v_mfma_f32_16x16x32_bf16 a[44:47], v[220:223], a[228:231], a[44:47]\n"
"v_exp_f32 v147, v147\n"
"v_mfma_f32_16x16x32_bf16 a[44:47], v[216:219], a[224:227], a[44:47]\n"
"ds_read_b128 a[192:195], v1 offset:8192\n"
"v_add_f32 v32, v32, v147\n"
"v_mfma_f32_16x16x32_bf16 a[144:147], v[16:19], v[40:43], a[144:147]\n"
"buffer_load_dwordx4 v[228:231], v12, s[20:23], s83 offen offset:1024 sc1\n"
"v_cvt_pk_bf16_f32 v163, v146, v147\n"
"v_mfma_f32_16x16x32_bf16 v[152:155], a[208:211], v[96:99], v[136:139]\n"
"s_waitcnt lgkmcnt(4)\n"
"v_exp_f32 v148, v148\n"
"v_mfma_f32_16x16x32_bf16 v[152:155], a[212:215], v[100:103], v[152:155]\n"
"ds_read_b128 a[196:199], v1 offset:8704\n"
"v_add_f32 v33, v33, v148\n"
"v_mfma_f32_16x16x32_bf16 v[152:155], a[216:219], v[104:107], v[152:155]\n"
"s_waitcnt lgkmcnt(2)\n"
"v_exp_f32 v149, v149\n"
"v_mfma_f32_16x16x32_bf16 v[152:155], a[220:223], v[108:111], v[152:155]\n"
"ds_read_b64_tr_b16 a[224:225], v2 offset:1024\n"
"v_add_f32 v33, v33, v149\n"
"v_mfma_f32_16x16x32_bf16 a[148:151], v[20:23], v[244:247], a[148:151]\n"
"buffer_load_dwordx4 v[232:235], v12, s[20:23], s83 offen offset:2048 sc1\n"
"v_cvt_pk_bf16_f32 v170, v148, v149\n"
"v_mfma_f32_16x16x32_bf16 a[48:51], v[192:195], a[232:235], a[48:51]\n"
"v_exp_f32 v150, v150\n"
"v_mfma_f32_16x16x32_bf16 a[48:51], v[196:199], a[236:239], a[48:51]\n"
"ds_read_b64_tr_b16 a[226:227], v2 offset:5120\n"
"v_add_f32 v33, v33, v150\n"
"v_mfma_f32_16x16x32_bf16 a[52:55], v[204:207], a[236:239], a[52:55]\n"
"v_exp_f32 v151, v151\n"
"v_mfma_f32_16x16x32_bf16 a[52:55], v[200:203], a[232:235], a[52:55]\n"
"ds_read_b128 a[200:203], v1 offset:9216\n"
"v_add_f32 v33, v33, v151\n"
"v_mfma_f32_16x16x32_bf16 a[152:155], v[20:23], v[248:251], a[152:155]\n"
"ds_read_b64_tr_b16 v[24:25], v50 offset:16384\n"
"v_cvt_pk_bf16_f32 v171, v150, v151\n"
"v_mfma_f32_16x16x32_bf16 v[156:159], a[208:211], v[112:115], v[140:143]\n"
"v_exp_f32 v152, v152\n"
"v_mfma_f32_16x16x32_bf16 v[156:159], a[212:215], v[116:119], v[156:159]\n"
"ds_read_b128 a[204:207], v1 offset:9728\n"
"v_add_f32 v34, v34, v152\n"
"v_mfma_f32_16x16x32_bf16 v[156:159], a[216:219], v[120:123], v[156:159]\n"
"v_exp_f32 v153, v153\n"
"v_mfma_f32_16x16x32_bf16 v[156:159], a[220:223], v[124:127], v[156:159]\n"
"ds_read_b64_tr_b16 a[228:229], v2 offset:9216\n"
"v_add_f32 v34, v34, v153\n"
"v_mfma_f32_16x16x32_bf16 a[156:159], v[20:23], v[252:255], a[156:159]\n"
"ds_read_b64_tr_b16 v[26:27], v50 offset:16896\n"
"v_cvt_pk_bf16_f32 v178, v152, v153\n"
"v_mfma_f32_16x16x32_bf16 a[56:59], v[208:211], a[232:235], a[56:59]\n"
"s_add_u32 m0, s80, 0x0\n"
"v_exp_f32 v154, v154\n"
"v_mfma_f32_16x16x32_bf16 a[56:59], v[212:215], a[236:239], a[56:59]\n"
"ds_read_b64_tr_b16 a[230:231], v2 offset:13312\n"
"v_add_f32 v34, v34, v154\n"
"v_mfma_f32_16x16x32_bf16 a[60:63], v[220:223], a[236:239], a[60:63]\n"
"s_waitcnt lgkmcnt(3)\n"
"v_exp_f32 v155, v155\n"
"v_mfma_f32_16x16x32_bf16 a[60:63], v[216:219], a[232:235], a[60:63]\n"
"ds_read_b64_tr_b16 a[232:233], v3 offset:1024\n"
"v_add_f32 v34, v34, v155\n"
"v_mfma_f32_16x16x32_bf16 a[160:163], v[20:23], v[36:39], a[160:163]\n"
"buffer_load_dwordx4 v[236:239], v12, s[20:23], s83 offen offset:3072 sc1\n"
"v_cvt_pk_bf16_f32 v179, v154, v155\n"
"v_mfma_f32_16x16x32_bf16 v[144:147], a[192:195], v[64:67], v[128:131]\n"
"v_exp_f32 v156, v156\n"
"v_mfma_f32_16x16x32_bf16 v[144:147], a[196:199], v[68:71], v[144:147]\n"
"ds_read_b64_tr_b16 a[234:235], v3 offset:5120\n"
"v_add_f32 v35, v35, v156\n"
"v_mfma_f32_16x16x32_bf16 v[144:147], a[200:203], v[72:75], v[144:147]\n"
"s_waitcnt lgkmcnt(2)\n"
"v_exp_f32 v157, v157\n"
"v_mfma_f32_16x16x32_bf16 v[144:147], a[204:207], v[76:79], v[144:147]\n"
"v_add_f32 v35, v35, v157\n"
"v_cvt_pk_bf16_f32 v186, v156, v157\n"
"v_mfma_f32_16x16x32_bf16 a[64:67], v[192:195], a[224:227], a[64:67]\n"
"v_exp_f32 v158, v158\n"
"v_mfma_f32_16x16x32_bf16 a[64:67], v[196:199], a[228:231], a[64:67]\n"
"buffer_load_dwordx4 v5, s[8:11], s28 offen lds\n"
"v_add_f32 v35, v35, v158\n"
"v_mfma_f32_16x16x32_bf16 a[68:71], v[204:207], a[228:231], a[68:71]\n"
"v_exp_f32 v159, v159\n"
"v_mfma_f32_16x16x32_bf16 a[68:71], v[200:203], a[224:227], a[68:71]\n"
"ds_read_b64_tr_b16 a[236:237], v3 offset:9216\n"
"v_add_f32 v35, v35, v159\n"
"v_mfma_f32_16x16x32_bf16 a[164:167], v[20:23], v[40:43], a[164:167]\n"
"buffer_load_dwordx4 v[240:243], v52, s[20:23], s83 offen sc1\n"
"v_cvt_pk_bf16_f32 v187, v158, v159\n"
"v_mfma_f32_16x16x32_bf16 v[148:151], a[192:195], v[80:83], v[132:135]\n"
"v_exp_f32 v144, v144\n"
"v_mfma_f32_16x16x32_bf16 v[148:151], a[196:199], v[84:87], v[148:151]\n"
"ds_read_b64_tr_b16 a[238:239], v3 offset:13312\n"
"v_add_f32 v32, v32, v144\n"
"v_mfma_f32_16x16x32_bf16 v[148:151], a[200:203], v[88:91], v[148:151]\n"
"v_exp_f32 v145, v145\n"
"v_mfma_f32_16x16x32_bf16 v[148:151], a[204:207], v[92:95], v[148:151]\n"
"v_add_f32 v32, v32, v145\n"
"v_cvt_pk_bf16_f32 v164, v144, v145\n"
"v_mfma_f32_16x16x32_bf16 a[72:75], v[208:211], a[224:227], a[72:75]\n"
"v_exp_f32 v146, v146\n"
"v_mfma_f32_16x16x32_bf16 a[72:75], v[212:215], a[228:231], a[72:75]\n"
"buffer_load_dwordx4 v6, s[8:11], s28 offen offset:2048 lds\n"
"v_add_f32 v32, v32, v146\n"
"v_mfma_f32_16x16x32_bf16 a[76:79], v[220:223], a[228:231], a[76:79]\n"
"s_waitcnt lgkmcnt(5)\n"
"v_exp_f32 v147, v147\n"
"v_mfma_f32_16x16x32_bf16 a[76:79], v[216:219], a[224:227], a[76:79]\n"
"ds_read_b128 a[208:211], v1 offset:12288\n"
"v_add_f32 v32, v32, v147\n"
"v_mfma_f32_16x16x32_bf16 a[168:171], v[24:27], v[244:247], a[168:171]\n"
"v_cvt_pk_bf16_f32 v165, v146, v147\n"
"v_mfma_f32_16x16x32_bf16 v[152:155], a[192:195], v[96:99], v[136:139]\n"
"v_exp_f32 v148, v148\n"
"v_mfma_f32_16x16x32_bf16 v[152:155], a[196:199], v[100:103], v[152:155]\n"
"ds_read_b128 a[212:215], v1 offset:12800\n"
"v_add_f32 v33, v33, v148\n"
"v_mfma_f32_16x16x32_bf16 v[152:155], a[200:203], v[104:107], v[152:155]\n"
"s_waitcnt lgkmcnt(2)\n"
"v_exp_f32 v149, v149\n"
"v_mfma_f32_16x16x32_bf16 v[152:155], a[204:207], v[108:111], v[152:155]\n"
"ds_read_b64_tr_b16 a[224:225], v2 offset:1536\n"
"v_add_f32 v33, v33, v149\n"
"v_mfma_f32_16x16x32_bf16 a[172:175], v[24:27], v[248:251], a[172:175]\n"
"ds_read_b64_tr_b16 v[28:29], v51 offset:16384\n"
"v_cvt_pk_bf16_f32 v172, v148, v149\n"
"v_mfma_f32_16x16x32_bf16 a[80:83], v[192:195], a[232:235], a[80:83]\n"
"v_exp_f32 v150, v150\n"
"v_mfma_f32_16x16x32_bf16 a[80:83], v[196:199], a[236:239], a[80:83]\n"
"ds_read_b64_tr_b16 a[226:227], v2 offset:5632\n"
"v_add_f32 v33, v33, v150\n"
"v_mfma_f32_16x16x32_bf16 a[84:87], v[204:207], a[236:239], a[84:87]\n"
"v_exp_f32 v151, v151\n"
"v_mfma_f32_16x16x32_bf16 a[84:87], v[200:203], a[232:235], a[84:87]\n"
"ds_read_b128 a[216:219], v1 offset:13312\n"
"v_add_f32 v33, v33, v151\n"
"v_mfma_f32_16x16x32_bf16 a[176:179], v[24:27], v[252:255], a[176:179]\n"
"ds_read_b64_tr_b16 v[30:31], v51 offset:16896\n"
"v_cvt_pk_bf16_f32 v173, v150, v151\n"
"v_mfma_f32_16x16x32_bf16 v[156:159], a[192:195], v[112:115], v[140:143]\n"
"v_exp_f32 v152, v152\n"
"v_mfma_f32_16x16x32_bf16 v[156:159], a[196:199], v[116:119], v[156:159]\n"
"ds_read_b128 a[220:223], v1 offset:13824\n"
"v_add_f32 v34, v34, v152\n"
"v_mfma_f32_16x16x32_bf16 v[156:159], a[200:203], v[120:123], v[156:159]\n"
"v_exp_f32 v153, v153\n"
"v_mfma_f32_16x16x32_bf16 v[156:159], a[204:207], v[124:127], v[156:159]\n"
"ds_read_b64_tr_b16 a[228:229], v2 offset:9728\n"
"v_add_f32 v34, v34, v153\n"
"v_mfma_f32_16x16x32_bf16 a[180:183], v[24:27], v[36:39], a[180:183]\n"
"v_cvt_pk_bf16_f32 v180, v152, v153\n"
"v_add_u32 v1, s79, v4\n"
"v_mfma_f32_16x16x32_bf16 a[88:91], v[208:211], a[232:235], a[88:91]\n"
"s_add_u32 m0, s80, 0x1000\n"
"v_exp_f32 v154, v154\n"
"v_mfma_f32_16x16x32_bf16 a[88:91], v[212:215], a[236:239], a[88:91]\n"
"ds_read_b64_tr_b16 a[230:231], v2 offset:13824\n"
"v_add_f32 v34, v34, v154\n"
"v_mfma_f32_16x16x32_bf16 a[92:95], v[220:223], a[236:239], a[92:95]\n"
"s_waitcnt lgkmcnt(2)\n"
"v_exp_f32 v155, v155\n"
"v_mfma_f32_16x16x32_bf16 a[92:95], v[216:219], a[232:235], a[92:95]\n"
"ds_read_b64_tr_b16 a[232:233], v3 offset:1536\n"
"v_add_f32 v34, v34, v155\n"
"v_mfma_f32_16x16x32_bf16 a[184:187], v[24:27], v[40:43], a[184:187]\n"
"buffer_load_dwordx4 v7, s[8:11], s28 offen lds\n"
"v_cvt_pk_bf16_f32 v181, v154, v155\n"
"v_mfma_f32_16x16x32_bf16 v[144:147], a[208:211], v[64:67], v[128:131]\n"
"v_exp_f32 v156, v156\n"
"v_mfma_f32_16x16x32_bf16 v[144:147], a[212:215], v[68:71], v[144:147]\n"
"ds_read_b64_tr_b16 a[234:235], v3 offset:5632\n"
"v_add_f32 v35, v35, v156\n"
"v_add_u32 v2, s78, v11\n"
"v_mfma_f32_16x16x32_bf16 v[144:147], a[216:219], v[72:75], v[144:147]\n"
"s_waitcnt lgkmcnt(2)\n"
"v_exp_f32 v157, v157\n"
"v_mfma_f32_16x16x32_bf16 v[144:147], a[220:223], v[76:79], v[144:147]\n"
"v_add_f32 v35, v35, v157\n"
"v_cvt_pk_bf16_f32 v188, v156, v157\n"
"v_mfma_f32_16x16x32_bf16 a[96:99], v[192:195], a[224:227], a[96:99]\n"
"s_waitcnt lgkmcnt(5)\n"
"v_exp_f32 v158, v158\n"
"v_mfma_f32_16x16x32_bf16 a[96:99], v[196:199], a[228:231], a[96:99]\n"
"buffer_load_dwordx4 v8, s[8:11], s28 offen offset:2048 lds\n"
"v_add_f32 v35, v35, v158\n"
"v_mfma_f32_16x16x32_bf16 a[100:103], v[204:207], a[228:231], a[100:103]\n"
"s_add_u32 m0, s71, 0x21000\n"
"v_exp_f32 v159, v159\n"
"v_mfma_f32_16x16x32_bf16 a[100:103], v[200:203], a[224:227], a[100:103]\n"
"ds_read_b64_tr_b16 a[236:237], v3 offset:9728\n"
"v_add_f32 v35, v35, v159\n"
"v_mfma_f32_16x16x32_bf16 a[188:191], v[28:31], v[244:247], a[188:191]\n"
"buffer_load_dwordx4 v44, s[16:19], s82 offen lds\n"
"v_cvt_pk_bf16_f32 v189, v158, v159\n"
"v_mfma_f32_16x16x32_bf16 v[148:151], a[208:211], v[80:83], v[132:135]\n"
"v_exp_f32 v144, v144\n"
"v_mfma_f32_16x16x32_bf16 v[148:151], a[212:215], v[84:87], v[148:151]\n"
"ds_read_b64_tr_b16 a[238:239], v3 offset:13824\n"
"v_add_f32 v32, v32, v144\n"
"v_mfma_f32_16x16x32_bf16 v[148:151], a[216:219], v[88:91], v[148:151]\n"
"s_mov_b32 s81, s78\n"
"v_exp_f32 v145, v145\n"
"v_mfma_f32_16x16x32_bf16 v[148:151], a[220:223], v[92:95], v[148:151]\n"
"v_add_f32 v32, v32, v145\n"
"v_cvt_pk_bf16_f32 v166, v144, v145\n"
"v_mfma_f32_16x16x32_bf16 a[104:107], v[208:211], a[224:227], a[104:107]\n"
"s_mov_b32 s78, s79\n"
"v_exp_f32 v146, v146\n"
"v_mfma_f32_16x16x32_bf16 a[104:107], v[212:215], a[228:231], a[104:107]\n"
"v_add_f32 v32, v32, v146\n"
"v_xor_b32 v3, 32, v2\n"
"v_mfma_f32_16x16x32_bf16 a[108:111], v[220:223], a[228:231], a[108:111]\n"
"s_waitcnt vmcnt(18) lgkmcnt(0)\n"
"s_barrier\n"
"v_exp_f32 v147, v147\n"
"v_mfma_f32_16x16x32_bf16 a[108:111], v[216:219], a[224:227], a[108:111]\n"
"ds_read_b128 a[192:195], v1 offset:0\n"
"v_add_f32 v32, v32, v147\n"
"v_mfma_f32_16x16x32_bf16 a[240:243], v[28:31], v[248:251], a[240:243]\n"
"buffer_load_dwordx4 v45, s[16:19], s82 offen offset:1024 lds\n"
"v_cvt_pk_bf16_f32 v167, v146, v147\n"
"v_mfma_f32_16x16x32_bf16 v[152:155], a[208:211], v[96:99], v[136:139]\n"
"v_exp_f32 v148, v148\n"
"v_mfma_f32_16x16x32_bf16 v[152:155], a[212:215], v[100:103], v[152:155]\n"
"ds_read_b128 a[196:199], v1 offset:512\n"
"v_add_f32 v33, v33, v148\n"
"v_mfma_f32_16x16x32_bf16 v[152:155], a[216:219], v[104:107], v[152:155]\n"
"s_waitcnt lgkmcnt(2)\n"
"v_exp_f32 v149, v149\n"
"v_mfma_f32_16x16x32_bf16 v[152:155], a[220:223], v[108:111], v[152:155]\n"
"ds_read_b64_tr_b16 a[224:225], v2 offset:0\n"
"v_add_f32 v33, v33, v149\n"
"v_mfma_f32_16x16x32_bf16 a[244:247], v[28:31], v[252:255], a[244:247]\n"
"buffer_load_dwordx4 v46, s[16:19], s82 offen offset:2048 lds\n"
"v_cvt_pk_bf16_f32 v174, v148, v149\n"
"v_mfma_f32_16x16x32_bf16 a[112:115], v[192:195], a[232:235], a[112:115]\n"
"s_waitcnt vmcnt(14)\n"
"v_exp_f32 v150, v150\n"
"v_mfma_f32_16x16x32_bf16 a[112:115], v[196:199], a[236:239], a[112:115]\n"
"ds_read_b64_tr_b16 a[226:227], v2 offset:4096\n"
"v_add_f32 v33, v33, v150\n"
"v_mfma_f32_16x16x32_bf16 a[116:119], v[204:207], a[236:239], a[116:119]\n"
"s_mov_b32 s79, s80\n"
"v_exp_f32 v151, v151\n"
"v_mfma_f32_16x16x32_bf16 a[116:119], v[200:203], a[232:235], a[116:119]\n"
"ds_read_b128 a[200:203], v1 offset:1024\n"
"v_add_f32 v33, v33, v151\n"
"v_mfma_f32_16x16x32_bf16 a[248:251], v[28:31], v[36:39], a[248:251]\n"
"ds_read_b64_tr_b16 v[16:17], v48 offset:0\n"
"v_cvt_pk_bf16_f32 v175, v150, v151\n"
"v_mfma_f32_16x16x32_bf16 v[156:159], a[208:211], v[112:115], v[140:143]\n"
"s_add_u32 s28, s28, 0x4000\n"
"v_exp_f32 v152, v152\n"
"v_mfma_f32_16x16x32_bf16 v[156:159], a[212:215], v[116:119], v[156:159]\n"
"ds_read_b128 a[204:207], v1 offset:1536\n"
"v_add_f32 v34, v34, v152\n"
"v_mfma_f32_16x16x32_bf16 v[156:159], a[216:219], v[120:123], v[156:159]\n"
"s_mov_b32 s80, s81\n"
"v_exp_f32 v153, v153\n"
"v_mfma_f32_16x16x32_bf16 v[156:159], a[220:223], v[124:127], v[156:159]\n"
"ds_read_b64_tr_b16 a[228:229], v2 offset:8192\n"
"v_add_f32 v34, v34, v153\n"
"v_mfma_f32_16x16x32_bf16 a[252:255], v[28:31], v[40:43], a[252:255]\n"
"ds_read_b64_tr_b16 v[18:19], v48 offset:512\n"
"v_cvt_pk_bf16_f32 v182, v152, v153\n"
"v_mfma_f32_16x16x32_bf16 a[120:123], v[208:211], a[232:235], a[120:123]\n"
"s_add_u32 m0, s79, 0xc000\n"
"v_exp_f32 v154, v154\n"
"v_mfma_f32_16x16x32_bf16 a[120:123], v[212:215], a[236:239], a[120:123]\n"
"ds_read_b64_tr_b16 a[230:231], v2 offset:12288\n"
"v_add_f32 v34, v34, v154\n"
"v_mfma_f32_16x16x32_bf16 a[124:127], v[220:223], a[236:239], a[124:127]\n"
"s_waitcnt lgkmcnt(3)\n"
"v_exp_f32 v155, v155\n"
"v_mfma_f32_16x16x32_bf16 a[124:127], v[216:219], a[232:235], a[124:127]\n"
"s_cmp_lt_u32 s28, 0x44000\n"
"s_cbranch_scc1 L_LOOPA0%=\n"
"L_XA%=:\n"
"ds_read_b64_tr_b16 a[232:233], v3 offset:0\n"
"v_add_f32 v34, v34, v155\n"
"v_cvt_pk_bf16_f32 v183, v154, v155\n"
"v_accvgpr_read_b32 v244, a128\n"
"v_mfma_f32_16x16x32_bf16 v[144:147], a[192:195], v[64:67], v[128:131]\n"
"v_exp_f32 v156, v156\n"
"v_accvgpr_read_b32 v245, a129\n"
"v_mfma_f32_16x16x32_bf16 v[144:147], a[196:199], v[68:71], v[144:147]\n"
"ds_read_b64_tr_b16 a[234:235], v3 offset:4096\n"
"v_add_f32 v35, v35, v156\n"
"v_accvgpr_read_b32 v246, a130\n"
"v_mfma_f32_16x16x32_bf16 v[144:147], a[200:203], v[72:75], v[144:147]\n"
"s_waitcnt lgkmcnt(2)\n"
"v_exp_f32 v157, v157\n"
"v_accvgpr_read_b32 v247, a131\n"
"v_mfma_f32_16x16x32_bf16 v[144:147], a[204:207], v[76:79], v[144:147]\n"
"v_add_f32 v35, v35, v157\n"
"v_cvt_pk_bf16_f32 v190, v156, v157\n"
"v_accvgpr_read_b32 v248, a132\n"
"v_mfma_f32_16x16x32_bf16 a[0:3], v[160:163], a[224:227], a[0:3]\n"
"v_exp_f32 v158, v158\n"
"v_accvgpr_read_b32 v249, a133\n"
"v_mfma_f32_16x16x32_bf16 a[0:3], v[164:167], a[228:231], a[0:3]\n"
"buffer_load_dwordx4 v9, s[12:15], s28 offen lds\n"
"v_add_f32 v35, v35, v158\n"
"v_accvgpr_read_b32 v250, a134\n"
"v_mfma_f32_16x16x32_bf16 a[4:7], v[172:175], a[228:231], a[4:7]\n"
"v_exp_f32 v159, v159\n"
"v_mfma_f32_16x16x32_bf16 a[4:7], v[168:171], a[224:227], a[4:7]\n"
"ds_read_b64_tr_b16 a[236:237], v3 offset:8192\n"
"v_add_f32 v35, v35, v159\n"
"v_cvt_pk_bf16_f32 v191, v158, v159\n"
"v_accvgpr_read_b32 v251, a135\n"
"v_mfma_f32_16x16x32_bf16 v[148:151], a[192:195], v[80:83], v[132:135]\n"
"v_exp_f32 v144, v144\n"
"v_accvgpr_read_b32 v252, a136\n"
"v_mfma_f32_16x16x32_bf16 v[148:151], a[196:199], v[84:87], v[148:151]\n"
"ds_read_b64_tr_b16 a[238:239], v3 offset:12288\n"
"v_accvgpr_read_b32 v253, a137\n"
"v_mfma_f32_16x16x32_bf16 v[148:151], a[200:203], v[88:91], v[148:151]\n"
"v_exp_f32 v145, v145\n"
"v_accvgpr_read_b32 v254, a138\n"
"v_mfma_f32_16x16x32_bf16 v[148:151], a[204:207], v[92:95], v[148:151]\n"
"v_cvt_pk_bf16_f32 v192, v144, v145\n"
"v_accvgpr_read_b32 v255, a139\n"
"v_mfma_f32_16x16x32_bf16 a[8:11], v[176:179], a[224:227], a[8:11]\n"
"v_exp_f32 v146, v146\n"
"v_accvgpr_read_b32 v36, a140\n"
"v_mfma_f32_16x16x32_bf16 a[8:11], v[180:183], a[228:231], a[8:11]\n"
"buffer_load_dwordx4 v9, s[12:15], s28 offen offset:2048 lds\n"
"v_mfma_f32_16x16x32_bf16 a[12:15], v[188:191], a[228:231], a[12:15]\n"
"s_add_u32 m0, s63, 98304\n"
"v_exp_f32 v147, v147\n"
"v_accvgpr_read_b32 v37, a141\n"
"v_mfma_f32_16x16x32_bf16 a[12:15], v[184:187], a[224:227], a[12:15]\n"
"ds_read_b128 a[208:211], v1 offset:4096\n"
"v_cvt_pk_bf16_f32 v193, v146, v147\n"
"v_accvgpr_read_b32 v38, a142\n"
"v_mfma_f32_16x16x32_bf16 v[152:155], a[192:195], v[96:99], v[136:139]\n"
"buffer_load_dwordx4 v58, s[72:75], 0 offen lds\n"
"v_exp_f32 v148, v148\n"
"v_accvgpr_read_b32 v39, a143\n"
"v_mfma_f32_16x16x32_bf16 v[152:155], a[196:199], v[100:103], v[152:155]\n"
"ds_read_b128 a[212:215], v1 offset:4608\n"
"v_accvgpr_read_b32 v40, a144\n"
"v_mfma_f32_16x16x32_bf16 v[152:155], a[200:203], v[104:107], v[152:155]\n"
"s_waitcnt lgkmcnt(2)\n"
"v_exp_f32 v149, v149\n"
"v_accvgpr_read_b32 v41, a145\n"
"v_mfma_f32_16x16x32_bf16 v[152:155], a[204:207], v[108:111], v[152:155]\n"
"ds_read_b64_tr_b16 a[224:225], v2 offset:512\n"
"v_cvt_pk_bf16_f32 v200, v148, v149\n"
"v_accvgpr_read_b32 v42, a146\n"
"v_mfma_f32_16x16x32_bf16 a[16:19], v[160:163], a[232:235], a[16:19]\n"
"buffer_load_dwordx4 v59, s[72:75], 0 offen offset:1024 lds\n"
"v_exp_f32 v150, v150\n"
"v_mfma_f32_16x16x32_bf16 a[16:19], v[164:167], a[236:239], a[16:19]\n"
"ds_read_b64_tr_b16 a[226:227], v2 offset:4608\n"
"v_accvgpr_read_b32 v43, a147\n"
"v_mfma_f32_16x16x32_bf16 a[20:23], v[172:175], a[236:239], a[20:23]\n"
"s_mul_i32 s60, s90, 10240\n"
"s_add_u32 s60, s60, 118784\n"
"v_exp_f32 v151, v151\n"
"v_add_u32 v16, s60, v12\n"
"v_mfma_f32_16x16x32_bf16 a[20:23], v[168:171], a[232:235], a[20:23]\n"
"ds_read_b128 a[216:219], v1 offset:5120\n"
"v_cvt_pk_bf16_f32 v201, v150, v151\n"
"v_mfma_f32_16x16x32_bf16 v[156:159], a[192:195], v[112:115], v[140:143]\n"
"buffer_load_dwordx4 v60, s[72:75], 0 offen offset:2048 lds\n"
"s_sub_u32 s60, s90, 1\n"
"s_and_b32 s60, s60, 3\n"
"s_mul_i32 s60, s60, 10240\n"
"s_add_u32 s60, s60, 118784\n"
"v_exp_f32 v152, v152\n"
"v_mfma_f32_16x16x32_bf16 v[156:159], a[196:199], v[116:119], v[156:159]\n"
"ds_read_b128 a[220:223], v1 offset:5632\n"
"v_add_u32 v18, s60, v12\n"
"v_mfma_f32_16x16x32_bf16 v[156:159], a[200:203], v[120:123], v[156:159]\n"
"buffer_load_dwordx4 v61, s[72:75], 0 offen offset:3072 lds\n"
"s_sub_u32 s60, s90, 2\n"
"s_and_b32 s60, s60, 3\n"
"s_mul_i32 s60, s60, 10240\n"
"v_exp_f32 v153, v153\n"
"v_mfma_f32_16x16x32_bf16 v[156:159], a[204:207], v[124:127], v[156:159]\n"
"ds_read_b64_tr_b16 a[228:229], v2 offset:8704\n"
"s_add_u32 s60, s60, 123904\n"
"v_cvt_pk_bf16_f32 v208, v152, v153\n"
"v_add_u32 v19, s60, v12\n"
"v_mfma_f32_16x16x32_bf16 a[24:27], v[176:179], a[232:235], a[24:27]\n"
"s_add_u32 m0, s79, 0xd000\n"
"s_sub_u32 s60, s90, 3\n"
"s_and_b32 s60, s60, 3\n"
"v_exp_f32 v154, v154\n"
"v_mfma_f32_16x16x32_bf16 a[24:27], v[180:183], a[236:239], a[24:27]\n"
"ds_read_b64_tr_b16 a[230:231], v2 offset:12800\n"
"s_mul_i32 s60, s60, 10240\n"
"s_add_u32 s60, s60, 118784\n"
"v_add_u32 v20, s60, v12\n"
"v_mfma_f32_16x16x32_bf16 a[28:31], v[188:191], a[236:239], a[28:31]\n"
"s_waitcnt lgkmcnt(2)\n"
"v_exp_f32 v155, v155\n"
"v_mfma_f32_16x16x32_bf16 a[28:31], v[184:187], a[232:235], a[28:31]\n"
"ds_read_b64_tr_b16 a[232:233], v3 offset:512\n"
"v_cvt_pk_bf16_f32 v209, v154, v155\n"
"v_mfma_f32_16x16x32_bf16 v[144:147], a[208:211], v[64:67], v[128:131]\n"
"s_waitcnt vmcnt(6)\n"
"s_barrier\n"
"v_exp_f32 v156, v156\n"
"v_mfma_f32_16x16x32_bf16 v[144:147], a[212:215], v[68:71], v[144:147]\n"
"ds_read_b64_tr_b16 a[234:235], v3 offset:4608\n"
"ds_write_b128 v16, a[148:151] offset:0\n"
"v_mfma_f32_16x16x32_bf16 v[144:147], a[216:219], v[72:75], v[144:147]\n"
"s_waitcnt lgkmcnt(3)\n"
"s_lshl_b32 s58, s90, 10\n"
"s_sub_u32 s58, s58, 118784\n"
"v_exp_f32 v157, v157\n"
"v_add_u32 v48, s58, v48\n"
"v_mfma_f32_16x16x32_bf16 v[144:147], a[220:223], v[76:79], v[144:147]\n"
"v_cvt_pk_bf16_f32 v216, v156, v157\n"
"v_add_u32 v49, s58, v49\n"
"v_mfma_f32_16x16x32_bf16 a[32:35], v[160:163], a[224:227], a[32:35]\n"
"v_exp_f32 v158, v158\n"
"v_add_u32 v50, s58, v50\n"
"v_mfma_f32_16x16x32_bf16 a[32:35], v[164:167], a[228:231], a[32:35]\n"
"buffer_load_dwordx4 v10, s[12:15], s28 offen lds\n"
"ds_write_b128 v16, a[152:155] offset:1024\n"
"v_mfma_f32_16x16x32_bf16 a[36:39], v[172:175], a[228:231], a[36:39]\n"
"v_exp_f32 v159, v159\n"
"v_add_u32 v51, s58, v51\n"
"v_mfma_f32_16x16x32_bf16 a[36:39], v[168:171], a[224:227], a[36:39]\n"
"ds_read_b64_tr_b16 a[236:237], v3 offset:8704\n"
"v_cvt_pk_bf16_f32 v217, v158, v159\n"
"v_lshrrev_b32 v28, 3, v13\n"
"v_mfma_f32_16x16x32_bf16 v[148:151], a[208:211], v[80:83], v[132:135]\n"
"v_exp_f32 v144, v144\n"
"v_mul_u32_u24 v28, 640, v28\n"
"v_mfma_f32_16x16x32_bf16 v[148:151], a[212:215], v[84:87], v[148:151]\n"
"ds_read_b64_tr_b16 a[238:239], v3 offset:12800\n"
"v_add_u32 v44, v28, v44\n"
"v_add_u32 v28, 5120, v28\n"
"v_mfma_f32_16x16x32_bf16 v[148:151], a[216:219], v[88:91], v[148:151]\n"
"ds_write_b128 v16, a[156:159] offset:2048\n"
"v_exp_f32 v145, v145\n"
"v_mfma_f32_16x16x32_bf16 v[148:151], a[220:223], v[92:95], v[148:151]\n"
"v_cvt_pk_bf16_f32 v194, v144, v145\n"
"v_mfma_f32_16x16x32_bf16 a[40:43], v[176:179], a[224:227], a[40:43]\n"
"v_exp_f32 v146, v146\n"
"v_add_u32 v45, v28, v45\n"
"v_mfma_f32_16x16x32_bf16 a[40:43], v[180:183], a[228:231], a[40:43]\n"
"buffer_load_dwordx4 v10, s[12:15], s28 offen offset:2048 lds\n"
"v_add_u32 v28, 5120, v28\n"
"v_add_u32 v46, v28, v46\n"
"v_mfma_f32_16x16x32_bf16 a[44:47], v[188:191], a[228:231], a[44:47]\n"
"s_add_u32 m0, s63, 102400\n"
"v_exp_f32 v147, v147\n"
"v_add_u32 v28, 5120, v28\n"
"v_mfma_f32_16x16x32_bf16 a[44:47], v[184:187], a[224:227], a[44:47]\n"
"ds_read_b128 a[192:195], v1 offset:8192\n"
"v_cvt_pk_bf16_f32 v195, v146, v147\n"
"v_add_u32 v47, v28, v47\n"
"v_mfma_f32_16x16x32_bf16 v[152:155], a[208:211], v[96:99], v[136:139]\n"
"buffer_load_dwordx4 v62, s[72:75], 0 offen lds\n"
"ds_write_b128 v16, a[160:163] offset:3072\n"
"v_exp_f32 v148, v148\n"
"v_mfma_f32_16x16x32_bf16 v[152:155], a[212:215], v[100:103], v[152:155]\n"
"ds_read_b128 a[196:199], v1 offset:8704\n"
"s_mul_i32 s58, s90, 28\n"
"v_min_u32 v28, 27, v13\n"
"v_mfma_f32_16x16x32_bf16 v[152:155], a[216:219], v[104:107], v[152:155]\n"
"s_waitcnt lgkmcnt(4)\n"
"v_exp_f32 v149, v149\n"
"v_add_u32 v28, s58, v28\n"
"v_mfma_f32_16x16x32_bf16 v[152:155], a[220:223], v[108:111], v[152:155]\n"
"ds_read_b64_tr_b16 a[224:225], v2 offset:1024\n"
"v_cvt_pk_bf16_f32 v202, v148, v149\n"
"v_min_u32 v28, 107, v28\n"
"v_mfma_f32_16x16x32_bf16 a[48:51], v[160:163], a[232:235], a[48:51]\n"
"s_lshl_b32 s58, s99, 10\n"
"ds_write_b128 v16, a[164:167] offset:4096\n"
"v_exp_f32 v150, v150\n"
"v_mfma_f32_16x16x32_bf16 a[48:51], v[164:167], a[236:239], a[48:51]\n"
"ds_read_b64_tr_b16 a[226:227], v2 offset:5120\n"
"v_mfma_f32_16x16x32_bf16 a[52:55], v[172:175], a[236:239], a[52:55]\n"
"s_add_u32 s58, s58, 0x6000\n"
"v_exp_f32 v151, v151\n"
"v_lshlrev_b32 v28, 3, v28\n"
"v_mfma_f32_16x16x32_bf16 a[52:55], v[168:171], a[232:235], a[52:55]\n"
"ds_read_b128 a[200:203], v1 offset:9216\n"
"v_cvt_pk_bf16_f32 v203, v150, v151\n"
"v_add_u32 v53, s58, v28\n"
"s_lshl_b32 s58, s98, 7\n"
"s_mul_i32 s59, s90, 1835008\n"
"v_mfma_f32_16x16x32_bf16 v[156:159], a[208:211], v[112:115], v[140:143]\n"
"ds_write_b128 v16, a[168:171] offset:5120\n"
"v_exp_f32 v152, v152\n"
"v_mfma_f32_16x16x32_bf16 v[156:159], a[212:215], v[116:119], v[156:159]\n"
"ds_read_b128 a[204:207], v1 offset:9728\n"
"s_add_u32 s58, s58, s59\n"
"s_sub_u32 s59, 7077888, s58\n"
"v_mfma_f32_16x16x32_bf16 v[156:159], a[216:219], v[120:123], v[156:159]\n"
"s_add_u32 s16, s50, s58\n"
"s_addc_u32 s17, s51, 0\n"
"s_mov_b32 s18, s59\n"
"s_mov_b32 s19, 0x20000\n"
"ds_write_b128 v16, a[172:175] offset:6144\n"
"v_exp_f32 v153, v153\n"
"v_mfma_f32_16x16x32_bf16 v[156:159], a[220:223], v[124:127], v[156:159]\n"
"ds_read_b64_tr_b16 a[228:229], v2 offset:9216\n"
"v_cvt_pk_bf16_f32 v210, v152, v153\n"
"v_mfma_f32_16x16x32_bf16 a[56:59], v[176:179], a[232:235], a[56:59]\n"
"s_add_u32 m0, s80, 0x0\n"
"s_mul_i32 s58, s99, 276480\n"
"s_mul_i32 s59, s90, 71680\n"
"s_add_u32 s58, s58, s59\n"
"v_exp_f32 v154, v154\n"
"v_mfma_f32_16x16x32_bf16 a[56:59], v[180:183], a[236:239], a[56:59]\n"
"ds_read_b64_tr_b16 a[230:231], v2 offset:13312\n"
"s_add_u32 s58, s58, 0x540000\n"
"s_sub_u32 s59, 276480, s59\n"
"ds_write_b128 v16, a[176:179] offset:7168\n"
"v_mfma_f32_16x16x32_bf16 a[60:63], v[188:191], a[236:239], a[60:63]\n"
"s_waitcnt lgkmcnt(4)\n"
"v_exp_f32 v155, v155\n"
"v_mfma_f32_16x16x32_bf16 a[60:63], v[184:187], a[232:235], a[60:63]\n"
"ds_read_b64_tr_b16 a[232:233], v3 offset:1024\n"
"s_add_u32 s20, s56, s58\n"
"s_addc_u32 s21, s57, 0\n"
"s_mov_b32 s22, s59\n"
"s_mov_b32 s23, 0x20000\n"
"s_lshl_b32 s58, %1, 2\n"
"s_add_u32 s58, s58, s90\n"
"v_cvt_pk_bf16_f32 v211, v154, v155\n"
"v_mfma_f32_16x16x32_bf16 v[144:147], a[192:195], v[64:67], v[128:131]\n"
"ds_write_b128 v16, a[180:183] offset:8192\n"
"v_exp_f32 v156, v156\n"
"v_mfma_f32_16x16x32_bf16 v[144:147], a[196:199], v[68:71], v[144:147]\n"
"ds_read_b64_tr_b16 a[234:235], v3 offset:5120\n"
"s_lshl_b32 s68, s58, 10\n"
"s_mov_b32 s69, 589824\n"
"v_mfma_f32_16x16x32_bf16 v[144:147], a[200:203], v[72:75], v[144:147]\n"
"s_waitcnt lgkmcnt(4)\n"
"s_mov_b32 s64, s50\n"
"s_mov_b32 s65, s51\n"
"s_mov_b32 s66, 7077888\n"
"ds_write_b128 v16, a[184:187] offset:9216\n"
"v_exp_f32 v157, v157\n"
"v_mfma_f32_16x16x32_bf16 v[144:147], a[204:207], v[76:79], v[144:147]\n"
"v_cvt_pk_bf16_f32 v218, v156, v157\n"
"v_mfma_f32_16x16x32_bf16 a[64:67], v[160:163], a[224:227], a[64:67]\n"
"s_sub_u32 s59, s58, 576\n"
"s_lshl_b32 s59, s59, 10\n"
"s_cmp_ge_u32 s58, 576\n"
"s_cselect_b32 s64, s54, s64\n"
"s_cselect_b32 s65, s55, s65\n"
"s_cselect_b32 s68, s59, s68\n"
"s_cselect_b32 s69, 219136, s69\n"
"s_cselect_b32 s66, 2621440, s66\n"
"v_exp_f32 v158, v158\n"
"v_mfma_f32_16x16x32_bf16 a[64:67], v[164:167], a[228:231], a[64:67]\n"
"buffer_load_dwordx4 v5, s[8:11], s28 offen lds\n"
"s_cmp_ge_u32 s58, 790\n"
"s_cselect_b32 s66, 0, s66\n"
"v_mfma_f32_16x16x32_bf16 a[68:71], v[172:175], a[228:231], a[68:71]\n"
"v_exp_f32 v159, v159\n"
"v_and_b32 v63, 7, v13\n"
"v_mfma_f32_16x16x32_bf16 a[68:71], v[168:171], a[224:227], a[68:71]\n"
"ds_read_b64_tr_b16 a[236:237], v3 offset:9216\n"
"v_cvt_pk_bf16_f32 v219, v158, v159\n"
"v_mfma_f32_16x16x32_bf16 v[148:151], a[192:195], v[80:83], v[132:135]\n"
"s_waitcnt lgkmcnt(1)\n"
"s_barrier\n"
"v_exp_f32 v144, v144\n"
"v_mfma_f32_16x16x32_bf16 v[148:151], a[196:199], v[84:87], v[148:151]\n"
"ds_read_b64_tr_b16 a[238:239], v3 offset:13312\n"
"ds_read_b128 v[224:227], v18 offset:0\n"
"v_mfma_f32_16x16x32_bf16 v[148:151], a[200:203], v[88:91], v[148:151]\n"
"ds_read_b128 v[228:231], v18 offset:1024\n"
"v_exp_f32 v145, v145\n"
"v_mfma_f32_16x16x32_bf16 v[148:151], a[204:207], v[92:95], v[148:151]\n"
"ds_read_b128 v[232:235], v18 offset:2048\n"
"v_cvt_pk_bf16_f32 v196, v144, v145\n"
"v_mfma_f32_16x16x32_bf16 a[72:75], v[176:179], a[224:227], a[72:75]\n"
"v_exp_f32 v146, v146\n"
"v_mfma_f32_16x16x32_bf16 a[72:75], v[180:183], a[228:231], a[72:75]\n"
"buffer_load_dwordx4 v6, s[8:11], s28 offen offset:2048 lds\n"
"ds_read_b128 v[236:239], v18 offset:3072\n"
"v_mfma_f32_16x16x32_bf16 a[76:79], v[188:191], a[228:231], a[76:79]\n"
"ds_read_b128 v[240:243], v18 offset:4096\n"
"v_exp_f32 v147, v147\n"
"v_mfma_f32_16x16x32_bf16 a[76:79], v[184:187], a[224:227], a[76:79]\n"
"ds_read_b128 a[208:211], v1 offset:12288\n"
"v_cvt_pk_bf16_f32 v197, v146, v147\n"
"v_mfma_f32_16x16x32_bf16 v[152:155], a[192:195], v[96:99], v[136:139]\n"
"v_exp_f32 v148, v148\n"
"v_lshlrev_b32 v63, 7, v63\n"
"v_mfma_f32_16x16x32_bf16 v[152:155], a[196:199], v[100:103], v[152:155]\n"
"ds_read_b128 a[212:215], v1 offset:12800\n"
"s_waitcnt lgkmcnt(2)\n"
"v_add_f32 v244, v244, v224\n"
"v_add_f32 v245, v245, v225\n"
"v_mfma_f32_16x16x32_bf16 v[152:155], a[200:203], v[104:107], v[152:155]\n"
"s_waitcnt lgkmcnt(7)\n"
"v_exp_f32 v149, v149\n"
"v_add_f32 v246, v246, v226\n"
"v_mfma_f32_16x16x32_bf16 v[152:155], a[204:207], v[108:111], v[152:155]\n"
"ds_read_b64_tr_b16 a[224:225], v2 offset:1536\n"
"v_cvt_pk_bf16_f32 v204, v148, v149\n"
"v_add_f32 v247, v247, v227\n"
"v_mfma_f32_16x16x32_bf16 a[80:83], v[160:163], a[232:235], a[80:83]\n"
"ds_read_b128 v[224:227], v19 offset:0\n"
"v_exp_f32 v150, v150\n"
"v_mfma_f32_16x16x32_bf16 a[80:83], v[164:167], a[236:239], a[80:83]\n"
"ds_read_b64_tr_b16 a[226:227], v2 offset:5632\n"
"v_add_f32 v248, v248, v228\n"
"v_mfma_f32_16x16x32_bf16 a[84:87], v[172:175], a[236:239], a[84:87]\n"
"v_exp_f32 v151, v151\n"
"v_add_f32 v249, v249, v229\n"
"v_mfma_f32_16x16x32_bf16 a[84:87], v[168:171], a[232:235], a[84:87]\n"
"ds_read_b128 a[216:219], v1 offset:13312\n"
"v_cvt_pk_bf16_f32 v205, v150, v151\n"
"v_add_f32 v250, v250, v230\n"
"v_add_f32 v251, v251, v231\n"
"v_mfma_f32_16x16x32_bf16 v[156:159], a[192:195], v[112:115], v[140:143]\n"
"ds_read_b128 v[228:231], v19 offset:1024\n"
"v_exp_f32 v152, v152\n"
"v_mfma_f32_16x16x32_bf16 v[156:159], a[196:199], v[116:119], v[156:159]\n"
"ds_read_b128 a[220:223], v1 offset:13824\n"
"v_add_f32 v252, v252, v232\n"
"v_mfma_f32_16x16x32_bf16 v[156:159], a[200:203], v[120:123], v[156:159]\n"
"v_exp_f32 v153, v153\n"
"v_add_f32 v253, v253, v233\n"
"v_mfma_f32_16x16x32_bf16 v[156:159], a[204:207], v[124:127], v[156:159]\n"
"ds_read_b64_tr_b16 a[228:229], v2 offset:9728\n"
"v_cvt_pk_bf16_f32 v212, v152, v153\n"
"v_add_u32 v1, s79, v4\n"
"v_add_f32 v254, v254, v234\n"
"v_mfma_f32_16x16x32_bf16 a[88:91], v[176:179], a[232:235], a[88:91]\n"
"s_add_u32 m0, s80, 0x1000\n"
"v_exp_f32 v154, v154\n"
"v_add_f32 v255, v255, v235\n"
"v_mfma_f32_16x16x32_bf16 a[88:91], v[180:183], a[236:239], a[88:91]\n"
"ds_read_b64_tr_b16 a[230:231], v2 offset:13824\n"
"ds_read_b128 v[232:235], v19 offset:2048\n"
"v_mfma_f32_16x16x32_bf16 a[92:95], v[188:191], a[236:239], a[92:95]\n"
"s_waitcnt lgkmcnt(3)\n"
"v_exp_f32 v155, v155\n"
"v_add_f32 v36, v36, v236\n"
"v_mfma_f32_16x16x32_bf16 a[92:95], v[184:187], a[232:235], a[92:95]\n"
"ds_read_b64_tr_b16 a[232:233], v3 offset:1536\n"
"v_cvt_pk_bf16_f32 v213, v154, v155\n"
"v_add_f32 v37, v37, v237\n"
"v_add_f32 v38, v38, v238\n"
"v_mfma_f32_16x16x32_bf16 v[144:147], a[208:211], v[64:67], v[128:131]\n"
"v_exp_f32 v156, v156\n"
"v_add_f32 v39, v39, v239\n"
"v_mfma_f32_16x16x32_bf16 v[144:147], a[212:215], v[68:71], v[144:147]\n"
"ds_read_b64_tr_b16 a[234:235], v3 offset:5632\n"
"ds_read_b128 v[236:239], v19 offset:3072\n"
"v_add_u32 v2, s78, v11\n"
"v_mfma_f32_16x16x32_bf16 v[144:147], a[216:219], v[72:75], v[144:147]\n"
"s_waitcnt lgkmcnt(4)\n"
"v_exp_f32 v157, v157\n"
"v_add_f32 v40, v40, v240\n"
"v_mfma_f32_16x16x32_bf16 v[144:147], a[220:223], v[76:79], v[144:147]\n"
"v_cvt_pk_bf16_f32 v220, v156, v157\n"
"v_add_f32 v41, v41, v241\n"
"v_mfma_f32_16x16x32_bf16 a[96:99], v[160:163], a[224:227], a[96:99]\n"
"v_exp_f32 v158, v158\n"
"v_add_f32 v42, v42, v242\n"
"v_add_f32 v43, v43, v243\n"
"v_mfma_f32_16x16x32_bf16 a[96:99], v[164:167], a[228:231], a[96:99]\n"
"buffer_load_dwordx4 v7, s[8:11], s28 offen lds\n"
"ds_read_b128 v[240:243], v19 offset:4096\n"
"v_mfma_f32_16x16x32_bf16 a[100:103], v[172:175], a[228:231], a[100:103]\n"
"v_exp_f32 v159, v159\n"
"v_mfma_f32_16x16x32_bf16 a[100:103], v[168:171], a[224:227], a[100:103]\n"
"ds_read_b64_tr_b16 a[236:237], v3 offset:9728\n"
"v_cvt_pk_bf16_f32 v221, v158, v159\n"
"v_mfma_f32_16x16x32_bf16 v[148:151], a[208:211], v[80:83], v[132:135]\n"
"v_exp_f32 v144, v144\n"
"v_mfma_f32_16x16x32_bf16 v[148:151], a[212:215], v[84:87], v[148:151]\n"
"ds_read_b64_tr_b16 a[238:239], v3 offset:13824\n"
"v_mfma_f32_16x16x32_bf16 v[148:151], a[216:219], v[88:91], v[148:151]\n"
"s_mov_b32 s81, s78\n"
"v_exp_f32 v145, v145\n"
"v_mfma_f32_16x16x32_bf16 v[148:151], a[220:223], v[92:95], v[148:151]\n"
"v_cvt_pk_bf16_f32 v198, v144, v145\n"
"v_mfma_f32_16x16x32_bf16 a[104:107], v[176:179], a[224:227], a[104:107]\n"
"s_mov_b32 s78, s79\n"
"v_exp_f32 v146, v146\n"
"v_mfma_f32_16x16x32_bf16 a[104:107], v[180:183], a[228:231], a[104:107]\n"
"buffer_load_dwordx4 v8, s[8:11], s28 offen offset:2048 lds\n"
"v_xor_b32 v3, 32, v2\n"
"v_mfma_f32_16x16x32_bf16 a[108:111], v[188:191], a[228:231], a[108:111]\n"
"s_waitcnt vmcnt(16) lgkmcnt(0)\n"
"s_barrier\n"
"v_exp_f32 v147, v147\n"
"v_mfma_f32_16x16x32_bf16 a[108:111], v[184:187], a[224:227], a[108:111]\n"
"ds_read_b128 a[192:195], v1 offset:0\n"
"ds_write_b128 v16, a[188:191] offset:0\n"
"v_cvt_pk_bf16_f32 v199, v146, v147\n"
"v_mfma_f32_16x16x32_bf16 v[152:155], a[208:211], v[96:99], v[136:139]\n"
"v_exp_f32 v148, v148\n"
"v_add_f32 v244, v244, v224\n"
"v_mfma_f32_16x16x32_bf16 v[152:155], a[212:215], v[100:103], v[152:155]\n"
"ds_read_b128 a[196:199], v1 offset:512\n"
"v_add_f32 v245, v245, v225\n"
"v_mfma_f32_16x16x32_bf16 v[152:155], a[216:219], v[104:107], v[152:155]\n"
"s_waitcnt lgkmcnt(3)\n"
"v_exp_f32 v149, v149\n"
"v_add_f32 v246, v246, v226\n"
"v_mfma_f32_16x16x32_bf16 v[152:155], a[220:223], v[108:111], v[152:155]\n"
"ds_read_b64_tr_b16 a[224:225], v2 offset:0\n"
"v_cvt_pk_bf16_f32 v206, v148, v149\n"
"v_add_f32 v247, v247, v227\n"
"v_mfma_f32_16x16x32_bf16 a[112:115], v[160:163], a[232:235], a[112:115]\n"
"ds_write_b128 v16, a[240:243] offset:1024\n"
"v_exp_f32 v150, v150\n"
"v_mfma_f32_16x16x32_bf16 a[112:115], v[164:167], a[236:239], a[112:115]\n"
"ds_read_b64_tr_b16 a[226:227], v2 offset:4096\n"
"v_mfma_f32_16x16x32_bf16 a[116:119], v[172:175], a[236:239], a[116:119]\n"
"s_mov_b32 s79, s80\n"
"v_exp_f32 v151, v151\n"
"v_add_f32 v248, v248, v228\n"
"v_mfma_f32_16x16x32_bf16 a[116:119], v[168:171], a[232:235], a[116:119]\n"
"ds_read_b128 a[200:203], v1 offset:1024\n"
"v_cvt_pk_bf16_f32 v207, v150, v151\n"
"v_add_f32 v249, v249, v229\n"
"v_mfma_f32_16x16x32_bf16 v[156:159], a[208:211], v[112:115], v[140:143]\n"
"s_add_u32 s28, s28, 0x4000\n"
"v_exp_f32 v152, v152\n"
"v_add_f32 v250, v250, v230\n"
"v_mfma_f32_16x16x32_bf16 v[156:159], a[212:215], v[116:119], v[156:159]\n"
"ds_read_b128 a[204:207], v1 offset:1536\n"
"v_add_f32 v251, v251, v231\n"
"v_mfma_f32_16x16x32_bf16 v[156:159], a[216:219], v[120:123], v[156:159]\n"
"s_mov_b32 s80, s81\n"
"ds_write_b128 v16, a[244:247] offset:2048\n"
"v_exp_f32 v153, v153\n"
"v_mfma_f32_16x16x32_bf16 v[156:159], a[220:223], v[124:127], v[156:159]\n"
"ds_read_b64_tr_b16 a[228:229], v2 offset:8192\n"
"v_cvt_pk_bf16_f32 v214, v152, v153\n"
"v_mfma_f32_16x16x32_bf16 a[120:123], v[176:179], a[232:235], a[120:123]\n"
"s_add_u32 m0, s79, 0xc000\n"
"v_exp_f32 v154, v154\n"
"v_add_f32 v252, v252, v232\n"
"v_mfma_f32_16x16x32_bf16 a[120:123], v[180:183], a[236:239], a[120:123]\n"
"ds_read_b64_tr_b16 a[230:231], v2 offset:12288\n"
"v_add_f32 v253, v253, v233\n"
"v_mfma_f32_16x16x32_bf16 a[124:127], v[188:191], a[236:239], a[124:127]\n"
"s_waitcnt lgkmcnt(3)\n"
"v_exp_f32 v155, v155\n"
"v_add_f32 v254, v254, v234\n"
"v_mfma_f32_16x16x32_bf16 a[124:127], v[184:187], a[232:235], a[124:127]\n"
"ds_read_b64_tr_b16 a[232:233], v3 offset:0\n"
"v_cvt_pk_bf16_f32 v215, v154, v155\n"
"v_add_f32 v255, v255, v235\n"
"v_mfma_f32_16x16x32_bf16 v[144:147], a[192:195], v[64:67], v[128:131]\n"
"ds_write_b128 v16, a[248:251] offset:3072\n"
"v_exp_f32 v156, v156\n"
"v_mfma_f32_16x16x32_bf16 v[144:147], a[196:199], v[68:71], v[144:147]\n"
"ds_read_b64_tr_b16 a[234:235], v3 offset:4096\n"
"v_mfma_f32_16x16x32_bf16 v[144:147], a[200:203], v[72:75], v[144:147]\n"
"s_waitcnt lgkmcnt(3)\n"
"v_exp_f32 v157, v157\n"
"v_add_f32 v36, v36, v236\n"
"v_mfma_f32_16x16x32_bf16 v[144:147], a[204:207], v[76:79], v[144:147]\n"
"v_cvt_pk_bf16_f32 v222, v156, v157\n"
"v_add_f32 v37, v37, v237\n"
"v_mfma_f32_16x16x32_bf16 a[0:3], v[192:195], a[224:227], a[0:3]\n"
"v_exp_f32 v158, v158\n"
"v_add_f32 v38, v38, v238\n"
"v_mfma_f32_16x16x32_bf16 a[0:3], v[196:199], a[228:231], a[0:3]\n"
"buffer_load_dwordx4 v9, s[12:15], s28 offen lds\n"
"v_add_f32 v39, v39, v239\n"
"v_mfma_f32_16x16x32_bf16 a[4:7], v[204:207], a[228:231], a[4:7]\n"
"ds_write_b128 v16, a[252:255] offset:4096\n"
"v_exp_f32 v159, v159\n"
"v_mfma_f32_16x16x32_bf16 a[4:7], v[200:203], a[224:227], a[4:7]\n"
"ds_read_b64_tr_b16 a[236:237], v3 offset:8192\n"
"v_cvt_pk_bf16_f32 v223, v158, v159\n"
"v_mfma_f32_16x16x32_bf16 v[148:151], a[192:195], v[80:83], v[132:135]\n"
"v_exp_f32 v144, v144\n"
"v_add_f32 v40, v40, v240\n"
"v_mfma_f32_16x16x32_bf16 v[148:151], a[196:199], v[84:87], v[148:151]\n"
"ds_read_b64_tr_b16 a[238:239], v3 offset:12288\n"
"v_add_f32 v41, v41, v241\n"
"v_mfma_f32_16x16x32_bf16 v[148:151], a[200:203], v[88:91], v[148:151]\n"
"v_exp_f32 v145, v145\n"
"v_add_f32 v42, v42, v242\n"
"v_mfma_f32_16x16x32_bf16 v[148:151], a[204:207], v[92:95], v[148:151]\n"
"v_cvt_pk_bf16_f32 v160, v144, v145\n"
"v_add_f32 v43, v43, v243\n"
"v_mfma_f32_16x16x32_bf16 a[8:11], v[208:211], a[224:227], a[8:11]\n"
"s_mul_i32 s60, s96, 276480\n"
"s_lshl_b32 s61, s97, 1\n"
"v_exp_f32 v146, v146\n"
"v_mfma_f32_16x16x32_bf16 a[8:11], v[212:215], a[228:231], a[8:11]\n"
"buffer_load_dwordx4 v9, s[12:15], s28 offen offset:2048 lds\n"
"s_lshr_b32 s62, s90, 1\n"
"s_add_u32 s61, s61, s62\n"
"s_mul_i32 s61, s61, 5120\n"
"s_add_u32 s60, s60, s61\n"
"v_mfma_f32_16x16x32_bf16 a[12:15], v[220:223], a[228:231], a[12:15]\n"
"s_add_u32 s60, s60, 0x540000\n"
"s_cmp_lt_u32 s97, 27\n"
"s_cselect_b32 s61, 5120, 0\n"
"s_add_u32 s72, s56, s60\n"
"s_addc_u32 s73, s57, 0\n"
"v_exp_f32 v147, v147\n"
"v_mfma_f32_16x16x32_bf16 a[12:15], v[216:219], a[224:227], a[12:15]\n"
"ds_read_b128 a[208:211], v1 offset:4096\n"
"s_mov_b32 s74, s61\n"
"s_mov_b32 s75, 0x20000\n"
"v_cvt_pk_bf16_f32 v161, v146, v147\n"
"v_mov_b32 v28, 0x3f803f80\n"
"v_mfma_f32_16x16x32_bf16 v[152:155], a[192:195], v[96:99], v[136:139]\n"
"v_exp_f32 v148, v148\n"
"v_and_b32 v29, 15, v13\n"
"v_mfma_f32_16x16x32_bf16 v[152:155], a[196:199], v[100:103], v[152:155]\n"
"ds_read_b128 a[212:215], v1 offset:4608\n"
"v_cmp_eq_u32 vcc, 0, v29\n"
"v_mfma_f32_16x16x32_bf16 v[152:155], a[200:203], v[104:107], v[152:155]\n"
"s_waitcnt lgkmcnt(2)\n"
"s_nop 1\n"
"v_exp_f32 v149, v149\n"
"v_mfma_f32_16x16x32_bf16 v[152:155], a[204:207], v[108:111], v[152:155]\n"
"ds_read_b64_tr_b16 a[224:225], v2 offset:512\n"
"v_cvt_pk_bf16_f32 v168, v148, v149\n"
"v_cndmask_b32 v28, 0, v28, vcc\n"
"v_mfma_f32_16x16x32_bf16 a[16:19], v[192:195], a[232:235], a[16:19]\n"
"v_exp_f32 v150, v150\n"
"v_mov_b32 v29, v28\n"
"v_mfma_f32_16x16x32_bf16 a[16:19], v[196:199], a[236:239], a[16:19]\n"
"ds_read_b64_tr_b16 a[226:227], v2 offset:4608\n"
"v_mov_b32 v30, v28\n"
"v_mfma_f32_16x16x32_bf16 a[20:23], v[204:207], a[236:239], a[20:23]\n"
"v_exp_f32 v151, v151\n"
"v_mov_b32 v31, v28\n"
"v_mfma_f32_16x16x32_bf16 a[20:23], v[200:203], a[232:235], a[20:23]\n"
"ds_read_b128 a[216:219], v1 offset:5120\n"
"v_cvt_pk_bf16_f32 v169, v150, v151\n"
"v_mfma_f32_16x16x32_bf16 v[156:159], a[192:195], v[112:115], v[140:143]\n"
"s_waitcnt lgkmcnt(7)\n"
"s_barrier\n"
"v_exp_f32 v152, v152\n"
"v_mfma_f32_16x16x32_bf16 v[156:159], a[196:199], v[116:119], v[156:159]\n"
"ds_read_b128 a[220:223], v1 offset:5632\n"
"ds_read_b128 v[224:227], v20 offset:0\n"
"ds_read_b128 v[228:231], v20 offset:1024\n"
"v_mfma_f32_16x16x32_bf16 v[156:159], a[200:203], v[120:123], v[156:159]\n"
"ds_read_b128 v[232:235], v20 offset:2048\n"
"v_exp_f32 v153, v153\n"
"v_mfma_f32_16x16x32_bf16 v[156:159], a[204:207], v[124:127], v[156:159]\n"
"ds_read_b64_tr_b16 a[228:229], v2 offset:8704\n"
"ds_read_b128 v[236:239], v20 offset:3072\n"
"ds_read_b128 v[240:243], v20 offset:4096\n"
"v_cvt_pk_bf16_f32 v176, v152, v153\n"
"v_mfma_f32_16x16x32_bf16 a[24:27], v[208:211], a[232:235], a[24:27]\n"
"s_add_u32 m0, s79, 0xd000\n"
"v_exp_f32 v154, v154\n"
"v_mov_b32 v24, 0xc0135761\n"
"v_mov_b32 v25, 0xbdd2d3e8\n"
"v_mfma_f32_16x16x32_bf16 a[24:27], v[212:215], a[236:239], a[24:27]\n"
"ds_read_b64_tr_b16 a[230:231], v2 offset:12800\n"
"s_waitcnt vmcnt(6)\n"
"v_bfe_u32 v21, v13, 4, 1\n"
"v_mul_u32_u24 v21, 2560, v21\n"
"v_mfma_f32_16x16x32_bf16 a[28:31], v[220:223], a[236:239], a[28:31]\n"
"s_waitcnt lgkmcnt(7)\n"
"v_exp_f32 v155, v155\n"
"v_sub_u32 v22, v0, v21\n"
"v_add_u32 v21, v0, v21\n"
"v_mfma_f32_16x16x32_bf16 a[28:31], v[216:219], a[232:235], a[28:31]\n"
"ds_read_b64_tr_b16 a[232:233], v3 offset:512\n"
"ds_read_b64_tr_b16 a[128:129], v21 offset:0\n"
"ds_read_b64_tr_b16 a[130:131], v22 offset:2560\n"
"v_cvt_pk_bf16_f32 v177, v154, v155\n"
"v_mfma_f32_16x16x32_bf16 v[144:147], a[208:211], v[64:67], v[128:131]\n"
"ds_read_b64_tr_b16 a[132:133], v21 offset:32\n"
"v_exp_f32 v156, v156\n"
"v_mfma_f32_16x16x32_bf16 v[144:147], a[212:215], v[68:71], v[144:147]\n"
"ds_read_b64_tr_b16 a[234:235], v3 offset:4608\n"
"ds_read_b64_tr_b16 a[134:135], v22 offset:2592\n"
"ds_read_b64_tr_b16 a[136:137], v21 offset:64\n"
"v_mfma_f32_16x16x32_bf16 v[144:147], a[216:219], v[72:75], v[144:147]\n"
"s_waitcnt lgkmcnt(7)\n"
"ds_read_b64_tr_b16 a[138:139], v22 offset:2624\n"
"v_exp_f32 v157, v157\n"
"v_mfma_f32_16x16x32_bf16 v[144:147], a[220:223], v[76:79], v[144:147]\n"
"ds_read_b64_tr_b16 a[140:141], v21 offset:96\n"
"ds_read_b64_tr_b16 a[142:143], v22 offset:2656\n"
"v_cvt_pk_bf16_f32 v184, v156, v157\n"
"v_mfma_f32_16x16x32_bf16 a[32:35], v[192:195], a[224:227], a[32:35]\n"
"ds_read_b64_tr_b16 a[144:145], v21 offset:128\n"
"v_exp_f32 v158, v158\n"
"v_mfma_f32_16x16x32_bf16 a[32:35], v[196:199], a[228:231], a[32:35]\n"
"buffer_load_dwordx4 v10, s[12:15], s28 offen lds\n"
"ds_read_b64_tr_b16 a[146:147], v22 offset:2688\n"
"v_mfma_f32_16x16x32_bf16 a[36:39], v[204:207], a[228:231], a[36:39]\n"
"s_waitcnt lgkmcnt(13)\n"
"v_exp_f32 v159, v159\n"
"v_add_f32 v244, v244, v224\n"
"v_add_f32 v245, v245, v225\n"
"v_add_f32 v246, v246, v226\n"
"v_mfma_f32_16x16x32_bf16 a[36:39], v[200:203], a[224:227], a[36:39]\n"
"ds_read_b64_tr_b16 a[236:237], v3 offset:8704\n"
"v_cvt_pk_bf16_f32 v185, v158, v159\n"
"v_add_f32 v247, v247, v227\n"
"v_add_f32 v248, v248, v228\n"
"v_add_f32 v249, v249, v229\n"
"v_mfma_f32_16x16x32_bf16 v[148:151], a[208:211], v[80:83], v[132:135]\n"
"v_exp_f32 v144, v144\n"
"v_add_f32 v250, v250, v230\n"
"v_add_f32 v251, v251, v231\n"
"v_mfma_f32_16x16x32_bf16 v[148:151], a[212:215], v[84:87], v[148:151]\n"
"ds_read_b64_tr_b16 a[238:239], v3 offset:12800\n"
"v_add_f32 v252, v252, v232\n"
"v_add_f32 v253, v253, v233\n"
"v_add_f32 v254, v254, v234\n"
"v_mfma_f32_16x16x32_bf16 v[148:151], a[216:219], v[88:91], v[148:151]\n"
"v_exp_f32 v145, v145\n"
"v_add_f32 v255, v255, v235\n"
"v_add_f32 v36, v36, v236\n"
"v_add_f32 v37, v37, v237\n"
"v_mfma_f32_16x16x32_bf16 v[148:151], a[220:223], v[92:95], v[148:151]\n"
"v_cvt_pk_bf16_f32 v162, v144, v145\n"
"v_add_f32 v38, v38, v238\n"
"v_add_f32 v39, v39, v239\n"
"v_add_f32 v40, v40, v240\n"
"v_mfma_f32_16x16x32_bf16 a[40:43], v[208:211], a[224:227], a[40:43]\n"
"v_exp_f32 v146, v146\n"
"v_add_f32 v41, v41, v241\n"
"v_add_f32 v42, v42, v242\n"
"v_mfma_f32_16x16x32_bf16 a[40:43], v[212:215], a[228:231], a[40:43]\n"
"buffer_load_dwordx4 v10, s[12:15], s28 offen offset:2048 lds\n"
"v_add_f32 v43, v43, v243\n"
"s_waitcnt lgkmcnt(2)\n"
"v_mul_f32 v224, v244, v244\n"
"v_mul_f32 v225, v245, v245\n"
"v_mfma_f32_16x16x32_bf16 a[44:47], v[220:223], a[228:231], a[44:47]\n"
"v_exp_f32 v147, v147\n"
"v_mul_f32 v226, v246, v246\n"
"v_mul_f32 v227, v247, v247\n"
"v_mfma_f32_16x16x32_bf16 a[44:47], v[216:219], a[224:227], a[44:47]\n"
"ds_read_b128 a[192:195], v1 offset:8192\n"
"buffer_store_dwordx4 a[128:131], v12, s[84:87], 0 offen sc1\n"
"v_cvt_pk_bf16_f32 v163, v146, v147\n"
"v_mul_f32 v228, v248, v248\n"
"v_mul_f32 v229, v249, v249\n"
"v_mfma_f32_16x16x32_bf16 a[240:243], v[28:31], v[192:195], 0\n"
"v_mfma_f32_16x16x32_bf16 a[240:243], v[28:31], v[196:199], a[240:243]\n"
"v_mfma_f32_16x16x32_bf16 v[152:155], a[208:211], v[96:99], v[136:139]\n"
"v_exp_f32 v148, v148\n"
"v_mul_f32 v230, v250, v250\n"
"v_mul_f32 v231, v251, v251\n"
"v_mfma_f32_16x16x32_bf16 v[152:155], a[212:215], v[100:103], v[152:155]\n"
"ds_read_b128 a[196:199], v1 offset:8704\n"
"buffer_store_dwordx4 a[132:135], v12, s[84:87], 0 offen offset:1024 sc1\n"
"v_mul_f32 v232, v252, v252\n"
"v_mfma_f32_16x16x32_bf16 v[152:155], a[216:219], v[104:107], v[152:155]\n"
"s_waitcnt lgkmcnt(2)\n"
"v_exp_f32 v149, v149\n"
"v_mul_f32 v233, v253, v253\n"
"v_mul_f32 v234, v254, v254\n"
"v_mul_f32 v235, v255, v255\n"
"v_mfma_f32_16x16x32_bf16 v[152:155], a[220:223], v[108:111], v[152:155]\n"
"ds_read_b64_tr_b16 a[224:225], v2 offset:1024\n"
"buffer_store_dwordx4 a[136:139], v12, s[84:87], 0 offen offset:2048 sc1\n"
"v_cvt_pk_bf16_f32 v170, v148, v149\n"
"v_mul_f32 v236, v36, v36\n"
"v_mfma_f32_16x16x32_bf16 a[48:51], v[192:195], a[232:235], a[48:51]\n"
"v_exp_f32 v150, v150\n"
"v_mul_f32 v237, v37, v37\n"
"v_mul_f32 v238, v38, v38\n"
"v_mfma_f32_16x16x32_bf16 a[48:51], v[196:199], a[236:239], a[48:51]\n"
"ds_read_b64_tr_b16 a[226:227], v2 offset:5120\n"
"v_mul_f32 v239, v39, v39\n"
"v_mfma_f32_16x16x32_bf16 a[52:55], v[204:207], a[236:239], a[52:55]\n"
"buffer_store_dwordx4 a[140:143], v12, s[84:87], 0 offen offset:3072 sc1\n"
"v_exp_f32 v151, v151\n"
"v_mul_f32 v240, v40, v40\n"
"v_mul_f32 v241, v41, v41\n"
"v_mul_f32 v242, v42, v42\n"
"v_mfma_f32_16x16x32_bf16 a[52:55], v[200:203], a[232:235], a[52:55]\n"
"ds_read_b128 a[200:203], v1 offset:9216\n"
"v_cvt_pk_bf16_f32 v171, v150, v151\n"
"v_mul_f32 v243, v43, v43\n"
"v_mfma_f32_16x16x32_bf16 v[156:159], a[208:211], v[112:115], v[140:143]\n"
"buffer_store_dwordx4 a[144:147], v52, s[84:87], 0 offen sc1\n"
"v_exp_f32 v152, v152\n"
"v_fma_f32 v224, v224, v25, v24\n"
"v_fma_f32 v225, v225, v25, v24\n"
"v_mfma_f32_16x16x32_bf16 v[156:159], a[212:215], v[116:119], v[156:159]\n"
"ds_read_b128 a[204:207], v1 offset:9728\n"
"v_fma_f32 v226, v226, v25, v24\n"
"v_fma_f32 v227, v227, v25, v24\n"
"v_fma_f32 v228, v228, v25, v24\n"
"v_mfma_f32_16x16x32_bf16 v[156:159], a[216:219], v[120:123], v[156:159]\n"
"v_exp_f32 v153, v153\n"
"v_fma_f32 v229, v229, v25, v24\n"
"v_fma_f32 v230, v230, v25, v24\n"
"v_fma_f32 v231, v231, v25, v24\n"
"v_mfma_f32_16x16x32_bf16 v[156:159], a[220:223], v[124:127], v[156:159]\n"
"ds_read_b64_tr_b16 a[228:229], v2 offset:9216\n"
"v_cvt_pk_bf16_f32 v178, v152, v153\n"
"v_fma_f32 v232, v232, v25, v24\n"
"v_fma_f32 v233, v233, v25, v24\n"
"v_fma_f32 v234, v234, v25, v24\n"
"v_mfma_f32_16x16x32_bf16 a[56:59], v[208:211], a[232:235], a[56:59]\n"
"s_add_u32 m0, s80, 0x0\n"
"v_exp_f32 v154, v154\n"
"v_fma_f32 v235, v235, v25, v24\n"
"v_fma_f32 v236, v236, v25, v24\n"
"v_fma_f32 v237, v237, v25, v24\n"
"v_mfma_f32_16x16x32_bf16 a[56:59], v[212:215], a[236:239], a[56:59]\n"
"ds_read_b64_tr_b16 a[230:231], v2 offset:13312\n"
"v_fma_f32 v238, v238, v25, v24\n"
"v_fma_f32 v239, v239, v25, v24\n"
"v_mfma_f32_16x16x32_bf16 a[60:63], v[220:223], a[236:239], a[60:63]\n"
"s_waitcnt lgkmcnt(2)\n"
"v_exp_f32 v155, v155\n"
"v_fma_f32 v240, v240, v25, v24\n"
"v_fma_f32 v241, v241, v25, v24\n"
"v_fma_f32 v242, v242, v25, v24\n"
"v_mfma_f32_16x16x32_bf16 a[60:63], v[216:219], a[232:235], a[60:63]\n"
"ds_read_b64_tr_b16 a[232:233], v3 offset:1024\n"
"v_cvt_pk_bf16_f32 v179, v154, v155\n"
"v_fma_f32 v243, v243, v25, v24\n"
"v_mul_f32 v224, v224, v244\n"
"v_mul_f32 v225, v225, v245\n"
"v_mfma_f32_16x16x32_bf16 a[244:247], v[28:31], v[200:203], 0\n"
"v_mfma_f32_16x16x32_bf16 a[244:247], v[28:31], v[204:207], a[244:247]\n"
"v_mfma_f32_16x16x32_bf16 v[144:147], a[192:195], v[64:67], v[128:131]\n"
"v_exp_f32 v156, v156\n"
"v_mul_f32 v226, v226, v246\n"
"v_mul_f32 v227, v227, v247\n"
"v_mul_f32 v228, v228, v248\n"
"v_mfma_f32_16x16x32_bf16 v[144:147], a[196:199], v[68:71], v[144:147]\n"
"ds_read_b64_tr_b16 a[234:235], v3 offset:5120\n"
"v_mul_f32 v229, v229, v249\n"
"v_mul_f32 v230, v230, v250\n"
"v_mfma_f32_16x16x32_bf16 v[144:147], a[200:203], v[72:75], v[144:147]\n"
"s_waitcnt lgkmcnt(2)\n"
"v_exp_f32 v157, v157\n"
"v_mul_f32 v231, v231, v251\n"
"v_mul_f32 v232, v232, v252\n"
"v_mul_f32 v233, v233, v253\n"
"v_mfma_f32_16x16x32_bf16 v[144:147], a[204:207], v[76:79], v[144:147]\n"
"v_cvt_pk_bf16_f32 v186, v156, v157\n"
"v_mul_f32 v234, v234, v254\n"
"v_mul_f32 v235, v235, v255\n"
"v_mul_f32 v236, v236, v36\n"
"v_mfma_f32_16x16x32_bf16 a[64:67], v[192:195], a[224:227], a[64:67]\n"
"v_exp_f32 v158, v158\n"
"v_mul_f32 v237, v237, v37\n"
"v_mul_f32 v238, v238, v38\n"
"v_mul_f32 v239, v239, v39\n"
"v_mfma_f32_16x16x32_bf16 a[64:67], v[196:199], a[228:231], a[64:67]\n"
"buffer_load_dwordx4 v5, s[8:11], s28 offen lds\n"
"v_mul_f32 v240, v240, v40\n"
"v_mul_f32 v241, v241, v41\n"
"v_mfma_f32_16x16x32_bf16 a[68:71], v[204:207], a[228:231], a[68:71]\n"
"v_exp_f32 v159, v159\n"
"v_mul_f32 v242, v242, v42\n"
"v_mul_f32 v243, v243, v43\n"
"v_exp_f32 v224, v224\n"
"v_mfma_f32_16x16x32_bf16 a[68:71], v[200:203], a[224:227], a[68:71]\n"
"ds_read_b64_tr_b16 a[236:237], v3 offset:9216\n"
"v_cvt_pk_bf16_f32 v187, v158, v159\n"
"v_exp_f32 v225, v225\n"
"v_mfma_f32_16x16x32_bf16 v[148:151], a[192:195], v[80:83], v[132:135]\n"
"v_exp_f32 v144, v144\n"
"v_exp_f32 v226, v226\n"
"v_exp_f32 v227, v227\n"
"v_mfma_f32_16x16x32_bf16 v[148:151], a[196:199], v[84:87], v[148:151]\n"
"ds_read_b64_tr_b16 a[238:239], v3 offset:13312\n"
"v_exp_f32 v228, v228\n"
"v_mfma_f32_16x16x32_bf16 v[148:151], a[200:203], v[88:91], v[148:151]\n"
"v_exp_f32 v145, v145\n"
"v_exp_f32 v229, v229\n"
"v_mfma_f32_16x16x32_bf16 v[148:151], a[204:207], v[92:95], v[148:151]\n"
"v_cvt_pk_bf16_f32 v164, v144, v145\n"
"v_exp_f32 v230, v230\n"
"v_exp_f32 v231, v231\n"
"v_mfma_f32_16x16x32_bf16 a[72:75], v[208:211], a[224:227], a[72:75]\n"
"v_exp_f32 v146, v146\n"
"v_exp_f32 v232, v232\n"
"v_mfma_f32_16x16x32_bf16 a[72:75], v[212:215], a[228:231], a[72:75]\n"
"buffer_load_dwordx4 v6, s[8:11], s28 offen offset:2048 lds\n"
"v_exp_f32 v233, v233\n"
"v_exp_f32 v234, v234\n"
"v_mfma_f32_16x16x32_bf16 a[76:79], v[220:223], a[228:231], a[76:79]\n"
"v_exp_f32 v147, v147\n"
"v_exp_f32 v235, v235\n"
"v_mfma_f32_16x16x32_bf16 a[76:79], v[216:219], a[224:227], a[76:79]\n"
"ds_read_b128 a[208:211], v1 offset:12288\n"
"v_cvt_pk_bf16_f32 v165, v146, v147\n"
"v_exp_f32 v236, v236\n"
"v_mfma_f32_16x16x32_bf16 a[248:251], v[28:31], v[208:211], 0\n"
"v_mfma_f32_16x16x32_bf16 a[248:251], v[28:31], v[212:215], a[248:251]\n"
"v_mfma_f32_16x16x32_bf16 v[152:155], a[192:195], v[96:99], v[136:139]\n"
"v_exp_f32 v148, v148\n"
"v_exp_f32 v237, v237\n"
"v_exp_f32 v238, v238\n"
"v_mfma_f32_16x16x32_bf16 v[152:155], a[196:199], v[100:103], v[152:155]\n"
"ds_read_b128 a[212:215], v1 offset:12800\n"
"v_exp_f32 v239, v239\n"
"v_mfma_f32_16x16x32_bf16 v[152:155], a[200:203], v[104:107], v[152:155]\n"
"s_waitcnt lgkmcnt(2)\n"
"v_exp_f32 v149, v149\n"
"v_exp_f32 v240, v240\n"
"v_exp_f32 v241, v241\n"
"v_mfma_f32_16x16x32_bf16 v[152:155], a[204:207], v[108:111], v[152:155]\n"
"ds_read_b64_tr_b16 a[224:225], v2 offset:1536\n"
"v_cvt_pk_bf16_f32 v172, v148, v149\n"
"v_exp_f32 v242, v242\n"
"v_mfma_f32_16x16x32_bf16 a[80:83], v[192:195], a[232:235], a[80:83]\n"
"v_exp_f32 v150, v150\n"
"v_exp_f32 v243, v243\n"
"v_mfma_f32_16x16x32_bf16 a[80:83], v[196:199], a[236:239], a[80:83]\n"
"ds_read_b64_tr_b16 a[226:227], v2 offset:5632\n"
"v_add_f32 v224, 1.0, v224\n"
"v_add_f32 v225, 1.0, v225\n"
"v_add_f32 v226, 1.0, v226\n"
"v_mfma_f32_16x16x32_bf16 a[84:87], v[204:207], a[236:239], a[84:87]\n"
"v_exp_f32 v151, v151\n"
"v_add_f32 v227, 1.0, v227\n"
"v_add_f32 v228, 1.0, v228\n"
"v_add_f32 v229, 1.0, v229\n"
"v_mfma_f32_16x16x32_bf16 a[84:87], v[200:203], a[232:235], a[84:87]\n"
"ds_read_b128 a[216:219], v1 offset:13312\n"
"v_cvt_pk_bf16_f32 v173, v150, v151\n"
"v_add_f32 v230, 1.0, v230\n"
"v_add_f32 v231, 1.0, v231\n"
"v_mfma_f32_16x16x32_bf16 v[156:159], a[192:195], v[112:115], v[140:143]\n"
"v_exp_f32 v152, v152\n"
"v_add_f32 v232, 1.0, v232\n"
"v_add_f32 v233, 1.0, v233\n"
"v_add_f32 v234, 1.0, v234\n"
"v_mfma_f32_16x16x32_bf16 v[156:159], a[196:199], v[116:119], v[156:159]\n"
"ds_read_b128 a[220:223], v1 offset:13824\n"
"v_add_f32 v235, 1.0, v235\n"
"v_add_f32 v236, 1.0, v236\n"
"v_add_f32 v237, 1.0, v237\n"
"v_mfma_f32_16x16x32_bf16 v[156:159], a[200:203], v[120:123], v[156:159]\n"
"v_exp_f32 v153, v153\n"
"v_add_f32 v238, 1.0, v238\n"
"v_add_f32 v239, 1.0, v239\n"
"v_add_f32 v240, 1.0, v240\n"
"v_mfma_f32_16x16x32_bf16 v[156:159], a[204:207], v[124:127], v[156:159]\n"
"ds_read_b64_tr_b16 a[228:229], v2 offset:9728\n"
"v_cvt_pk_bf16_f32 v180, v152, v153\n"
"v_add_u32 v1, s79, v4\n"
"v_add_f32 v241, 1.0, v241\n"
"v_add_f32 v242, 1.0, v242\n"
"v_mfma_f32_16x16x32_bf16 a[88:91], v[208:211], a[232:235], a[88:91]\n"
"s_add_u32 m0, s80, 0x1000\n"
"v_exp_f32 v154, v154\n"
"v_add_f32 v243, 1.0, v243\n"
"v_rcp_f32 v224, v224\n"
"v_mfma_f32_16x16x32_bf16 a[88:91], v[212:215], a[236:239], a[88:91]\n"
"ds_read_b64_tr_b16 a[230:231], v2 offset:13824\n"
"v_rcp_f32 v225, v225\n"
"v_rcp_f32 v226, v226\n"
"v_mfma_f32_16x16x32_bf16 a[92:95], v[220:223], a[236:239], a[92:95]\n"
"s_waitcnt lgkmcnt(2)\n"
"v_exp_f32 v155, v155\n"
"v_rcp_f32 v227, v227\n"
"v_mfma_f32_16x16x32_bf16 a[92:95], v[216:219], a[232:235], a[92:95]\n"
"ds_read_b64_tr_b16 a[232:233], v3 offset:1536\n"
"v_cvt_pk_bf16_f32 v181, v154, v155\n"
"v_rcp_f32 v228, v228\n"
"v_rcp_f32 v229, v229\n"
"v_mfma_f32_16x16x32_bf16 v[144:147], a[208:211], v[64:67], v[128:131]\n"
"v_exp_f32 v156, v156\n"
"v_rcp_f32 v230, v230\n"
"v_mfma_f32_16x16x32_bf16 v[144:147], a[212:215], v[68:71], v[144:147]\n"
"ds_read_b64_tr_b16 a[234:235], v3 offset:5632\n"
"v_add_u32 v2, s78, v11\n"
"v_rcp_f32 v231, v231\n"
"v_mfma_f32_16x16x32_bf16 v[144:147], a[216:219], v[72:75], v[144:147]\n"
"s_waitcnt lgkmcnt(2)\n"
"v_exp_f32 v157, v157\n"
"v_rcp_f32 v232, v232\n"
"v_rcp_f32 v233, v233\n"
"v_mfma_f32_16x16x32_bf16 v[144:147], a[220:223], v[76:79], v[144:147]\n"
"v_cvt_pk_bf16_f32 v188, v156, v157\n"
"v_rcp_f32 v234, v234\n"
"v_mfma_f32_16x16x32_bf16 a[96:99], v[192:195], a[224:227], a[96:99]\n"
"v_exp_f32 v158, v158\n"
"v_rcp_f32 v235, v235\n"
"v_mfma_f32_16x16x32_bf16 a[96:99], v[196:199], a[228:231], a[96:99]\n"
"buffer_load_dwordx4 v7, s[8:11], s28 offen lds\n"
"v_rcp_f32 v236, v236\n"
"v_rcp_f32 v237, v237\n"
"v_mfma_f32_16x16x32_bf16 a[100:103], v[204:207], a[228:231], a[100:103]\n"
"v_exp_f32 v159, v159\n"
"v_rcp_f32 v238, v238\n"
"v_mfma_f32_16x16x32_bf16 a[100:103], v[200:203], a[224:227], a[100:103]\n"
"ds_read_b64_tr_b16 a[236:237], v3 offset:9728\n"
"v_cvt_pk_bf16_f32 v189, v158, v159\n"
"v_rcp_f32 v239, v239\n"
"v_rcp_f32 v240, v240\n"
"v_mfma_f32_16x16x32_bf16 v[148:151], a[208:211], v[80:83], v[132:135]\n"
"v_exp_f32 v144, v144\n"
"v_rcp_f32 v241, v241\n"
"v_mfma_f32_16x16x32_bf16 v[148:151], a[212:215], v[84:87], v[148:151]\n"
"ds_read_b64_tr_b16 a[238:239], v3 offset:13824\n"
"v_rcp_f32 v242, v242\n"
"v_mfma_f32_16x16x32_bf16 v[148:151], a[216:219], v[88:91], v[148:151]\n"
"s_mov_b32 s81, s78\n"
"v_exp_f32 v145, v145\n"
"v_rcp_f32 v243, v243\n"
"s_and_b32 s60, s90, 1\n"
"s_lshl_b32 s60, s60, 9\n"
"v_and_b32 v26, 31, v13\n"
"v_mfma_f32_16x16x32_bf16 v[148:151], a[220:223], v[92:95], v[148:151]\n"
"v_cvt_pk_bf16_f32 v166, v144, v145\n"
"v_lshlrev_b32 v26, 4, v26\n"
"v_add_u32 v26, s60, v26\n"
"v_mfma_f32_16x16x32_bf16 a[104:107], v[208:211], a[224:227], a[104:107]\n"
"s_mov_b32 s78, s79\n"
"v_exp_f32 v146, v146\n"
"v_lshrrev_b32 v27, 5, v13\n"
"v_lshl_add_u32 v26, v27, 10, v26\n"
"s_mov_b32 s62, 0x1000\n"
"v_mul_f32 v224, v224, v244\n"
"v_mfma_f32_16x16x32_bf16 a[104:107], v[212:215], a[228:231], a[104:107]\n"
"buffer_load_dwordx4 v8, s[8:11], s28 offen offset:2048 lds\n"
"v_xor_b32 v3, 32, v2\n"
"v_mul_f32 v225, v225, v245\n"
"v_mul_f32 v226, v226, v246\n"
"v_mul_f32 v227, v227, v247\n"
"v_mfma_f32_16x16x32_bf16 a[108:111], v[220:223], a[228:231], a[108:111]\n"
"s_waitcnt vmcnt(13) lgkmcnt(0)\n"
"s_barrier\n"
"v_exp_f32 v147, v147\n"
"v_cvt_pk_bf16_f32 v16, v224, v225\n"
"v_cvt_pk_bf16_f32 v17, v226, v227\n"
"v_mfma_f32_16x16x32_bf16 a[108:111], v[216:219], a[224:227], a[108:111]\n"
"ds_read_b128 a[192:195], v1 offset:0\n"
"v_cvt_pk_bf16_f32 v167, v146, v147\n"
"v_mul_f32 v228, v228, v248\n"
"v_mul_f32 v229, v229, v249\n"
"v_mul_f32 v230, v230, v250\n"
"v_mfma_f32_16x16x32_bf16 a[252:255], v[28:31], v[216:219], 0\n"
"v_mfma_f32_16x16x32_bf16 a[252:255], v[28:31], v[220:223], a[252:255]\n"
"v_mfma_f32_16x16x32_bf16 v[152:155], a[208:211], v[96:99], v[136:139]\n"
"v_exp_f32 v148, v148\n"
"v_mul_f32 v231, v231, v251\n"
"v_cvt_pk_bf16_f32 v18, v228, v229\n"
"v_cvt_pk_bf16_f32 v19, v230, v231\n"
"v_mfma_f32_16x16x32_bf16 v[152:155], a[212:215], v[100:103], v[152:155]\n"
"ds_read_b128 a[196:199], v1 offset:512\n"
"v_permlane32_swap_b32 v16, v18\n"
"v_permlane32_swap_b32 v17, v19\n"
"s_nop 0\n"
"v_permlane16_swap_b32 v16, v18\n"
"v_mfma_f32_16x16x32_bf16 v[152:155], a[216:219], v[104:107], v[152:155]\n"
"s_waitcnt lgkmcnt(2)\n"
"v_exp_f32 v149, v149\n"
"v_permlane16_swap_b32 v17, v19\n"
"v_mfma_f32_16x16x32_bf16 v[152:155], a[220:223], v[108:111], v[152:155]\n"
"ds_read_b64_tr_b16 a[224:225], v2 offset:0\n"
"buffer_store_dwordx4 v[16:19], v26, s[72:75], 0 offen sc1\n"
"v_cvt_pk_bf16_f32 v174, v148, v149\n"
"v_mul_f32 v232, v232, v252\n"
"v_mul_f32 v233, v233, v253\n"
"v_mfma_f32_16x16x32_bf16 a[112:115], v[192:195], a[232:235], a[112:115]\n"
"v_exp_f32 v150, v150\n"
"v_mul_f32 v234, v234, v254\n"
"v_mul_f32 v235, v235, v255\n"
"v_cvt_pk_bf16_f32 v20, v232, v233\n"
"v_mfma_f32_16x16x32_bf16 a[112:115], v[196:199], a[236:239], a[112:115]\n"
"ds_read_b64_tr_b16 a[226:227], v2 offset:4096\n"
"v_cvt_pk_bf16_f32 v21, v234, v235\n"
"v_mul_f32 v236, v236, v36\n"
"v_mul_f32 v237, v237, v37\n"
"v_mfma_f32_16x16x32_bf16 a[116:119], v[204:207], a[236:239], a[116:119]\n"
"s_mov_b32 s79, s80\n"
"v_exp_f32 v151, v151\n"
"v_mul_f32 v238, v238, v38\n"
"v_mul_f32 v239, v239, v39\n"
"v_cvt_pk_bf16_f32 v22, v236, v237\n"
"v_mfma_f32_16x16x32_bf16 a[116:119], v[200:203], a[232:235], a[116:119]\n"
"ds_read_b128 a[200:203], v1 offset:1024\n"
"v_cvt_pk_bf16_f32 v175, v150, v151\n"
"v_cvt_pk_bf16_f32 v23, v238, v239\n"
"v_permlane32_swap_b32 v20, v22\n"
"v_mfma_f32_16x16x32_bf16 v[156:159], a[208:211], v[112:115], v[140:143]\n"
"s_add_u32 s28, s28, 0x4000\n"
"v_exp_f32 v152, v152\n"
"v_permlane32_swap_b32 v21, v23\n"
"v_permlane16_swap_b32 v20, v22\n"
"s_nop 0\n"
"v_permlane16_swap_b32 v21, v23\n"
"v_mfma_f32_16x16x32_bf16 v[156:159], a[212:215], v[116:119], v[156:159]\n"
"ds_read_b128 a[204:207], v1 offset:1536\n"
"buffer_store_dwordx4 v[20:23], v26, s[72:75], 0 offen offset:2048 sc1\n"
"v_mul_f32 v240, v240, v40\n"
"v_mfma_f32_16x16x32_bf16 v[156:159], a[216:219], v[120:123], v[156:159]\n"
"s_mov_b32 s80, s81\n"
"v_exp_f32 v153, v153\n"
"v_mul_f32 v241, v241, v41\n"
"v_mul_f32 v242, v242, v42\n"
"v_mul_f32 v243, v243, v43\n"
"v_mfma_f32_16x16x32_bf16 v[156:159], a[220:223], v[124:127], v[156:159]\n"
"ds_read_b64_tr_b16 a[228:229], v2 offset:8192\n"
"v_cvt_pk_bf16_f32 v182, v152, v153\n"
"v_cvt_pk_bf16_f32 v16, v240, v241\n"
"v_cvt_pk_bf16_f32 v17, v242, v243\n"
"v_mfma_f32_16x16x32_bf16 a[120:123], v[208:211], a[232:235], a[120:123]\n"
"s_add_u32 m0, s79, 0xc000\n"
"v_exp_f32 v154, v154\n"
"v_permlane32_swap_b32 v16, v18\n"
"v_permlane32_swap_b32 v17, v19\n"
"s_nop 0\n"
"v_permlane16_swap_b32 v16, v18\n"
"v_mfma_f32_16x16x32_bf16 a[120:123], v[212:215], a[236:239], a[120:123]\n"
"ds_read_b64_tr_b16 a[230:231], v2 offset:12288\n"
"v_permlane16_swap_b32 v17, v19\n"
"v_mfma_f32_16x16x32_bf16 a[124:127], v[220:223], a[236:239], a[124:127]\n"
"s_waitcnt lgkmcnt(2)\n"
"buffer_store_dwordx4 v[16:19], v26, s[72:75], s62 offen sc1\n"
"s_lshl_b32 s60, s96, 10\n"
"s_lshl_b32 s61, s97, 2\n"
"s_add_u32 s61, s61, s90\n"
"s_lshl_b32 s61, s61, 3\n"
"s_add_u32 s60, s60, s61\n"
"s_add_u32 s60, s60, 0x6000\n"
"s_cmp_lt_u32 s97, 27\n"
"s_cselect_b32 s82, s60, 0x7fff0000\n"
"s_mov_b32 s83, s70\n"
"v_exp_f32 v155, v155\n"
"v_mfma_f32_16x16x32_bf16 a[124:127], v[216:219], a[232:235], a[124:127]\n"
"L_LOOPB0%=:\n"
"ds_read_b64_tr_b16 a[232:233], v3 offset:0\n"
"v_cvt_pk_bf16_f32 v183, v154, v155\n"
"v_mfma_f32_16x16x32_bf16 v[144:147], a[192:195], v[64:67], v[128:131]\n"
"v_exp_f32 v156, v156\n"
"v_mfma_f32_16x16x32_bf16 v[144:147], a[196:199], v[68:71], v[144:147]\n"
"ds_read_b64_tr_b16 a[234:235], v3 offset:4096\n"
"v_mfma_f32_16x16x32_bf16 v[144:147], a[200:203], v[72:75], v[144:147]\n"
"s_waitcnt lgkmcnt(2)\n"
"v_exp_f32 v157, v157\n"
"v_mfma_f32_16x16x32_bf16 v[144:147], a[204:207], v[76:79], v[144:147]\n"
"v_cvt_pk_bf16_f32 v190, v156, v157\n"
"v_mfma_f32_16x16x32_bf16 a[0:3], v[160:163], a[224:227], a[0:3]\n"
"v_exp_f32 v158, v158\n"
"v_mfma_f32_16x16x32_bf16 a[0:3], v[164:167], a[228:231], a[0:3]\n"
"buffer_load_dwordx4 v9, s[12:15], s28 offen lds\n"
"v_mfma_f32_16x16x32_bf16 a[4:7], v[172:175], a[228:231], a[4:7]\n"
"v_exp_f32 v159, v159\n"
"v_mfma_f32_16x16x32_bf16 a[4:7], v[168:171], a[224:227], a[4:7]\n"
"ds_read_b64_tr_b16 a[236:237], v3 offset:8192\n"
"v_cvt_pk_bf16_f32 v191, v158, v159\n"
"v_mfma_f32_16x16x32_bf16 v[148:151], a[192:195], v[80:83], v[132:135]\n"
"v_exp_f32 v144, v144\n"
"v_mfma_f32_16x16x32_bf16 v[148:151], a[196:199], v[84:87], v[148:151]\n"
"ds_read_b64_tr_b16 a[238:239], v3 offset:12288\n"
"v_mfma_f32_16x16x32_bf16 v[148:151], a[200:203], v[88:91], v[148:151]\n"
"v_exp_f32 v145, v145\n"
"v_mfma_f32_16x16x32_bf16 v[148:151], a[204:207], v[92:95], v[148:151]\n"
"v_cvt_pk_bf16_f32 v192, v144, v145\n"
"v_mfma_f32_16x16x32_bf16 a[8:11], v[176:179], a[224:227], a[8:11]\n"
"v_exp_f32 v146, v146\n"
"v_mfma_f32_16x16x32_bf16 a[8:11], v[180:183], a[228:231], a[8:11]\n"
"buffer_load_dwordx4 v9, s[12:15], s28 offen offset:2048 lds\n"
"v_mfma_f32_16x16x32_bf16 a[12:15], v[188:191], a[228:231], a[12:15]\n"
"v_exp_f32 v147, v147\n"
"v_mfma_f32_16x16x32_bf16 a[12:15], v[184:187], a[224:227], a[12:15]\n"
"ds_read_b128 a[208:211], v1 offset:4096\n"
"v_cvt_pk_bf16_f32 v193, v146, v147\n"
"v_mfma_f32_16x16x32_bf16 a[240:243], v[28:31], v[160:163], a[240:243]\n"
"v_mfma_f32_16x16x32_bf16 a[240:243], v[28:31], v[164:167], a[240:243]\n"
"v_mfma_f32_16x16x32_bf16 v[152:155], a[192:195], v[96:99], v[136:139]\n"
"v_exp_f32 v148, v148\n"
"v_mfma_f32_16x16x32_bf16 v[152:155], a[196:199], v[100:103], v[152:155]\n"
"ds_read_b128 a[212:215], v1 offset:4608\n"
"v_mfma_f32_16x16x32_bf16 v[152:155], a[200:203], v[104:107], v[152:155]\n"
"s_waitcnt lgkmcnt(2)\n"
"v_exp_f32 v149, v149\n"
"v_mfma_f32_16x16x32_bf16 v[152:155], a[204:207], v[108:111], v[152:155]\n"
"ds_read_b64_tr_b16 a[224:225], v2 offset:512\n"
"v_cvt_pk_bf16_f32 v200, v148, v149\n"
"v_mfma_f32_16x16x32_bf16 a[16:19], v[160:163], a[232:235], a[16:19]\n"
"v_exp_f32 v150, v150\n"
"v_mfma_f32_16x16x32_bf16 a[16:19], v[164:167], a[236:239], a[16:19]\n"
"ds_read_b64_tr_b16 a[226:227], v2 offset:4608\n"
"v_mfma_f32_16x16x32_bf16 a[20:23], v[172:175], a[236:239], a[20:23]\n"
"v_exp_f32 v151, v151\n"
"v_mfma_f32_16x16x32_bf16 a[20:23], v[168:171], a[232:235], a[20:23]\n"
"ds_read_b128 a[216:219], v1 offset:5120\n"
"v_cvt_pk_bf16_f32 v201, v150, v151\n"
"v_mfma_f32_16x16x32_bf16 v[156:159], a[192:195], v[112:115], v[140:143]\n"
"v_exp_f32 v152, v152\n"
"v_mfma_f32_16x16x32_bf16 v[156:159], a[196:199], v[116:119], v[156:159]\n"
"ds_read_b128 a[220:223], v1 offset:5632\n"
"v_mfma_f32_16x16x32_bf16 v[156:159], a[200:203], v[120:123], v[156:159]\n"
"v_exp_f32 v153, v153\n"
"v_mfma_f32_16x16x32_bf16 v[156:159], a[204:207], v[124:127], v[156:159]\n"
"ds_read_b64_tr_b16 a[228:229], v2 offset:8704\n"
"v_cvt_pk_bf16_f32 v208, v152, v153\n"
"v_mfma_f32_16x16x32_bf16 a[24:27], v[176:179], a[232:235], a[24:27]\n"
"s_add_u32 m0, s79, 0xd000\n"
"v_exp_f32 v154, v154\n"
"v_mfma_f32_16x16x32_bf16 a[24:27], v[180:183], a[236:239], a[24:27]\n"
"ds_read_b64_tr_b16 a[230:231], v2 offset:12800\n"
"v_mfma_f32_16x16x32_bf16 a[28:31], v[188:191], a[236:239], a[28:31]\n"
"s_waitcnt lgkmcnt(2)\n"
"v_exp_f32 v155, v155\n"
"v_mfma_f32_16x16x32_bf16 a[28:31], v[184:187], a[232:235], a[28:31]\n"
"ds_read_b64_tr_b16 a[232:233], v3 offset:512\n"
"v_cvt_pk_bf16_f32 v209, v154, v155\n"
"v_mfma_f32_16x16x32_bf16 v[144:147], a[208:211], v[64:67], v[128:131]\n"
"v_exp_f32 v156, v156\n"
"v_mfma_f32_16x16x32_bf16 v[144:147], a[212:215], v[68:71], v[144:147]\n"
"ds_read_b64_tr_b16 a[234:235], v3 offset:4608\n"
"v_mfma_f32_16x16x32_bf16 v[144:147], a[216:219], v[72:75], v[144:147]\n"
"s_waitcnt lgkmcnt(2)\n"
"v_exp_f32 v157, v157\n"
"v_mfma_f32_16x16x32_bf16 v[144:147], a[220:223], v[76:79], v[144:147]\n"
"v_cvt_pk_bf16_f32 v216, v156, v157\n"
"v_mfma_f32_16x16x32_bf16 a[32:35], v[160:163], a[224:227], a[32:35]\n"
"v_exp_f32 v158, v158\n"
"v_mfma_f32_16x16x32_bf16 a[32:35], v[164:167], a[228:231], a[32:35]\n"
"buffer_load_dwordx4 v10, s[12:15], s28 offen lds\n"
"v_mfma_f32_16x16x32_bf16 a[36:39], v[172:175], a[228:231], a[36:39]\n"
"v_exp_f32 v159, v159\n"
"v_mfma_f32_16x16x32_bf16 a[36:39], v[168:171], a[224:227], a[36:39]\n"
"ds_read_b64_tr_b16 a[236:237], v3 offset:8704\n"
"v_cvt_pk_bf16_f32 v217, v158, v159\n"
"v_mfma_f32_16x16x32_bf16 v[148:151], a[208:211], v[80:83], v[132:135]\n"
"v_exp_f32 v144, v144\n"
"v_mfma_f32_16x16x32_bf16 v[148:151], a[212:215], v[84:87], v[148:151]\n"
"ds_read_b64_tr_b16 a[238:239], v3 offset:12800\n"
"v_mfma_f32_16x16x32_bf16 v[148:151], a[216:219], v[88:91], v[148:151]\n"
"v_exp_f32 v145, v145\n"
"v_mfma_f32_16x16x32_bf16 v[148:151], a[220:223], v[92:95], v[148:151]\n"
"v_cvt_pk_bf16_f32 v194, v144, v145\n"
"v_mfma_f32_16x16x32_bf16 a[40:43], v[176:179], a[224:227], a[40:43]\n"
"v_exp_f32 v146, v146\n"
"v_mfma_f32_16x16x32_bf16 a[40:43], v[180:183], a[228:231], a[40:43]\n"
"buffer_load_dwordx4 v10, s[12:15], s28 offen offset:2048 lds\n"
"v_mfma_f32_16x16x32_bf16 a[44:47], v[188:191], a[228:231], a[44:47]\n"
"v_exp_f32 v147, v147\n"
"v_mfma_f32_16x16x32_bf16 a[44:47], v[184:187], a[224:227], a[44:47]\n"
"ds_read_b128 a[192:195], v1 offset:8192\n"
"v_cvt_pk_bf16_f32 v195, v146, v147\n"
"v_mfma_f32_16x16x32_bf16 a[244:247], v[28:31], v[168:171], a[244:247]\n"
"v_mfma_f32_16x16x32_bf16 a[244:247], v[28:31], v[172:175], a[244:247]\n"
"v_mfma_f32_16x16x32_bf16 v[152:155], a[208:211], v[96:99], v[136:139]\n"
"v_exp_f32 v148, v148\n"
"v_mfma_f32_16x16x32_bf16 v[152:155], a[212:215], v[100:103], v[152:155]\n"
"ds_read_b128 a[196:199], v1 offset:8704\n"
"v_mfma_f32_16x16x32_bf16 v[152:155], a[216:219], v[104:107], v[152:155]\n"
"s_waitcnt lgkmcnt(2)\n"
"v_exp_f32 v149, v149\n"
"v_mfma_f32_16x16x32_bf16 v[152:155], a[220:223], v[108:111], v[152:155]\n"
"ds_read_b64_tr_b16 a[224:225], v2 offset:1024\n"
"v_cvt_pk_bf16_f32 v202, v148, v149\n"
"v_mfma_f32_16x16x32_bf16 a[48:51], v[160:163], a[232:235], a[48:51]\n"
"v_exp_f32 v150, v150\n"
"v_mfma_f32_16x16x32_bf16 a[48:51], v[164:167], a[236:239], a[48:51]\n"
"ds_read_b64_tr_b16 a[226:227], v2 offset:5120\n"
"v_mfma_f32_16x16x32_bf16 a[52:55], v[172:175], a[236:239], a[52:55]\n"
"v_exp_f32 v151, v151\n"
"v_mfma_f32_16x16x32_bf16 a[52:55], v[168:171], a[232:235], a[52:55]\n"
"ds_read_b128 a[200:203], v1 offset:9216\n"
"v_cvt_pk_bf16_f32 v203, v150, v151\n"
"v_mfma_f32_16x16x32_bf16 v[156:159], a[208:211], v[112:115], v[140:143]\n"
"v_exp_f32 v152, v152\n"
"v_mfma_f32_16x16x32_bf16 v[156:159], a[212:215], v[116:119], v[156:159]\n"
"ds_read_b128 a[204:207], v1 offset:9728\n"
"v_mfma_f32_16x16x32_bf16 v[156:159], a[216:219], v[120:123], v[156:159]\n"
"v_exp_f32 v153, v153\n"
"v_mfma_f32_16x16x32_bf16 v[156:159], a[220:223], v[124:127], v[156:159]\n"
"ds_read_b64_tr_b16 a[228:229], v2 offset:9216\n"
"v_cvt_pk_bf16_f32 v210, v152, v153\n"
"v_mfma_f32_16x16x32_bf16 a[56:59], v[176:179], a[232:235], a[56:59]\n"
"s_add_u32 m0, s80, 0x0\n"
"v_exp_f32 v154, v154\n"
"v_mfma_f32_16x16x32_bf16 a[56:59], v[180:183], a[236:239], a[56:59]\n"
"ds_read_b64_tr_b16 a[230:231], v2 offset:13312\n"
"v_mfma_f32_16x16x32_bf16 a[60:63], v[188:191], a[236:239], a[60:63]\n"
"s_waitcnt lgkmcnt(2)\n"
"v_exp_f32 v155, v155\n"
"v_mfma_f32_16x16x32_bf16 a[60:63], v[184:187], a[232:235], a[60:63]\n"
"ds_read_b64_tr_b16 a[232:233], v3 offset:1024\n"
"v_cvt_pk_bf16_f32 v211, v154, v155\n"
"v_mfma_f32_16x16x32_bf16 v[144:147], a[192:195], v[64:67], v[128:131]\n"
"v_exp_f32 v156, v156\n"
"v_mfma_f32_16x16x32_bf16 v[144:147], a[196:199], v[68:71], v[144:147]\n"
"ds_read_b64_tr_b16 a[234:235], v3 offset:5120\n"
"v_mfma_f32_16x16x32_bf16 v[144:147], a[200:203], v[72:75], v[144:147]\n"
"s_waitcnt lgkmcnt(2)\n"
"v_exp_f32 v157, v157\n"
"v_mfma_f32_16x16x32_bf16 v[144:147], a[204:207], v[76:79], v[144:147]\n"
"v_cvt_pk_bf16_f32 v218, v156, v157\n"
"v_mfma_f32_16x16x32_bf16 a[64:67], v[160:163], a[224:227], a[64:67]\n"
"v_exp_f32 v158, v158\n"
"v_mfma_f32_16x16x32_bf16 a[64:67], v[164:167], a[228:231], a[64:67]\n"
"buffer_load_dwordx4 v5, s[8:11], s28 offen lds\n"
"v_mfma_f32_16x16x32_bf16 a[68:71], v[172:175], a[228:231], a[68:71]\n"
"v_exp_f32 v159, v159\n"
"v_mfma_f32_16x16x32_bf16 a[68:71], v[168:171], a[224:227], a[68:71]\n"
"ds_read_b64_tr_b16 a[236:237], v3 offset:9216\n"
"v_cvt_pk_bf16_f32 v219, v158, v159\n"
"v_mfma_f32_16x16x32_bf16 v[148:151], a[192:195], v[80:83], v[132:135]\n"
"v_exp_f32 v144, v144\n"
"v_mfma_f32_16x16x32_bf16 v[148:151], a[196:199], v[84:87], v[148:151]\n"
"ds_read_b64_tr_b16 a[238:239], v3 offset:13312\n"
"v_mfma_f32_16x16x32_bf16 v[148:151], a[200:203], v[88:91], v[148:151]\n"
"v_exp_f32 v145, v145\n"
"v_mfma_f32_16x16x32_bf16 v[148:151], a[204:207], v[92:95], v[148:151]\n"
"buffer_load_dword v15, v63, s[64:67], s68 offen\n"
"v_cvt_pk_bf16_f32 v196, v144, v145\n"
"v_mfma_f32_16x16x32_bf16 a[72:75], v[176:179], a[224:227], a[72:75]\n"
"s_add_u32 s68, s68, s69\n"
"v_exp_f32 v146, v146\n"
"v_mfma_f32_16x16x32_bf16 a[72:75], v[180:183], a[228:231], a[72:75]\n"
"buffer_load_dwordx4 v6, s[8:11], s28 offen offset:2048 lds\n"
"v_mfma_f32_16x16x32_bf16 a[76:79], v[188:191], a[228:231], a[76:79]\n"
"v_exp_f32 v147, v147\n"
"v_mfma_f32_16x16x32_bf16 a[76:79], v[184:187], a[224:227], a[76:79]\n"
"ds_read_b128 a[208:211], v1 offset:12288\n"
"v_cvt_pk_bf16_f32 v197, v146, v147\n"
"v_mfma_f32_16x16x32_bf16 a[248:251], v[28:31], v[176:179], a[248:251]\n"
"v_mfma_f32_16x16x32_bf16 a[248:251], v[28:31], v[180:183], a[248:251]\n"
"v_mfma_f32_16x16x32_bf16 v[152:155], a[192:195], v[96:99], v[136:139]\n"
"v_exp_f32 v148, v148\n"
"v_mfma_f32_16x16x32_bf16 v[152:155], a[196:199], v[100:103], v[152:155]\n"
"ds_read_b128 a[212:215], v1 offset:12800\n"
"v_mfma_f32_16x16x32_bf16 v[152:155], a[200:203], v[104:107], v[152:155]\n"
"s_waitcnt lgkmcnt(2)\n"
"v_exp_f32 v149, v149\n"
"v_mfma_f32_16x16x32_bf16 v[152:155], a[204:207], v[108:111], v[152:155]\n"
"ds_read_b64_tr_b16 a[224:225], v2 offset:1536\n"
"v_cvt_pk_bf16_f32 v204, v148, v149\n"
"v_mfma_f32_16x16x32_bf16 a[80:83], v[160:163], a[232:235], a[80:83]\n"
"v_exp_f32 v150, v150\n"
"v_mfma_f32_16x16x32_bf16 a[80:83], v[164:167], a[236:239], a[80:83]\n"
"ds_read_b64_tr_b16 a[226:227], v2 offset:5632\n"
"v_mfma_f32_16x16x32_bf16 a[84:87], v[172:175], a[236:239], a[84:87]\n"
"v_exp_f32 v151, v151\n"
"v_mfma_f32_16x16x32_bf16 a[84:87], v[168:171], a[232:235], a[84:87]\n"
"ds_read_b128 a[216:219], v1 offset:13312\n"
"v_cvt_pk_bf16_f32 v205, v150, v151\n"
"v_mfma_f32_16x16x32_bf16 v[156:159], a[192:195], v[112:115], v[140:143]\n"
"v_exp_f32 v152, v152\n"
"v_mfma_f32_16x16x32_bf16 v[156:159], a[196:199], v[116:119], v[156:159]\n"
"ds_read_b128 a[220:223], v1 offset:13824\n"
"v_mfma_f32_16x16x32_bf16 v[156:159], a[200:203], v[120:123], v[156:159]\n"
"v_exp_f32 v153, v153\n"
"v_mfma_f32_16x16x32_bf16 v[156:159], a[204:207], v[124:127], v[156:159]\n"
"ds_read_b64_tr_b16 a[228:229], v2 offset:9728\n"
"v_cvt_pk_bf16_f32 v212, v152, v153\n"
"v_add_u32 v1, s79, v4\n"
"v_mfma_f32_16x16x32_bf16 a[88:91], v[176:179], a[232:235], a[88:91]\n"
"s_add_u32 m0, s80, 0x1000\n"
"v_exp_f32 v154, v154\n"
"v_mfma_f32_16x16x32_bf16 a[88:91], v[180:183], a[236:239], a[88:91]\n"
"ds_read_b64_tr_b16 a[230:231], v2 offset:13824\n"
"v_mfma_f32_16x16x32_bf16 a[92:95], v[188:191], a[236:239], a[92:95]\n"
"s_waitcnt lgkmcnt(2)\n"
"v_exp_f32 v155, v155\n"
"v_mfma_f32_16x16x32_bf16 a[92:95], v[184:187], a[232:235], a[92:95]\n"
"ds_read_b64_tr_b16 a[232:233], v3 offset:1536\n"
"v_cvt_pk_bf16_f32 v213, v154, v155\n"
"v_mfma_f32_16x16x32_bf16 v[144:147], a[208:211], v[64:67], v[128:131]\n"
"v_exp_f32 v156, v156\n"
"v_mfma_f32_16x16x32_bf16 v[144:147], a[212:215], v[68:71], v[144:147]\n"
"ds_read_b64_tr_b16 a[234:235], v3 offset:5632\n"
"v_add_u32 v2, s78, v11\n"
"v_mfma_f32_16x16x32_bf16 v[144:147], a[216:219], v[72:75], v[144:147]\n"
"s_waitcnt lgkmcnt(2)\n"
"v_exp_f32 v157, v157\n"
"v_mfma_f32_16x16x32_bf16 v[144:147], a[220:223], v[76:79], v[144:147]\n"
"v_cvt_pk_bf16_f32 v220, v156, v157\n"
"v_mfma_f32_16x16x32_bf16 a[96:99], v[160:163], a[224:227], a[96:99]\n"
"v_exp_f32 v158, v158\n"
"v_mfma_f32_16x16x32_bf16 a[96:99], v[164:167], a[228:231], a[96:99]\n"
"buffer_load_dwordx4 v7, s[8:11], s28 offen lds\n"
"v_mfma_f32_16x16x32_bf16 a[100:103], v[172:175], a[228:231], a[100:103]\n"
"v_exp_f32 v159, v159\n"
"v_mfma_f32_16x16x32_bf16 a[100:103], v[168:171], a[224:227], a[100:103]\n"
"ds_read_b64_tr_b16 a[236:237], v3 offset:9728\n"
"v_cvt_pk_bf16_f32 v221, v158, v159\n"
"v_mfma_f32_16x16x32_bf16 v[148:151], a[208:211], v[80:83], v[132:135]\n"
"v_exp_f32 v144, v144\n"
"v_mfma_f32_16x16x32_bf16 v[148:151], a[212:215], v[84:87], v[148:151]\n"
"ds_read_b64_tr_b16 a[238:239], v3 offset:13824\n"
"v_mfma_f32_16x16x32_bf16 v[148:151], a[216:219], v[88:91], v[148:151]\n"
"s_mov_b32 s81, s78\n"
"v_exp_f32 v145, v145\n"
"v_mfma_f32_16x16x32_bf16 v[148:151], a[220:223], v[92:95], v[148:151]\n"
"v_cvt_pk_bf16_f32 v198, v144, v145\n"
"v_mfma_f32_16x16x32_bf16 a[104:107], v[176:179], a[224:227], a[104:107]\n"
"s_mov_b32 s78, s79\n"
"v_exp_f32 v146, v146\n"
"v_mfma_f32_16x16x32_bf16 a[104:107], v[180:183], a[228:231], a[104:107]\n"
"buffer_load_dwordx4 v8, s[8:11], s28 offen offset:2048 lds\n"
"v_xor_b32 v3, 32, v2\n"
"v_mfma_f32_16x16x32_bf16 a[108:111], v[188:191], a[228:231], a[108:111]\n"
"s_waitcnt vmcnt(11) lgkmcnt(0)\n"
"s_barrier\n"
"v_exp_f32 v147, v147\n"
"v_mfma_f32_16x16x32_bf16 a[108:111], v[184:187], a[224:227], a[108:111]\n"
"ds_read_b128 a[192:195], v1 offset:0\n"
"v_cvt_pk_bf16_f32 v199, v146, v147\n"
"v_mfma_f32_16x16x32_bf16 a[252:255], v[28:31], v[184:187], a[252:255]\n"
"v_mfma_f32_16x16x32_bf16 a[252:255], v[28:31], v[188:191], a[252:255]\n"
"v_mfma_f32_16x16x32_bf16 v[152:155], a[208:211], v[96:99], v[136:139]\n"
"v_exp_f32 v148, v148\n"
"v_mfma_f32_16x16x32_bf16 v[152:155], a[212:215], v[100:103], v[152:155]\n"
"ds_read_b128 a[196:199], v1 offset:512\n"
"v_mfma_f32_16x16x32_bf16 v[152:155], a[216:219], v[104:107], v[152:155]\n"
"s_waitcnt lgkmcnt(2)\n"
"v_exp_f32 v149, v149\n"
"v_mfma_f32_16x16x32_bf16 v[152:155], a[220:223], v[108:111], v[152:155]\n"
"ds_read_b64_tr_b16 a[224:225], v2 offset:0\n"
"v_cvt_pk_bf16_f32 v206, v148, v149\n"
"v_mfma_f32_16x16x32_bf16 a[112:115], v[160:163], a[232:235], a[112:115]\n"
"v_exp_f32 v150, v150\n"
"v_mfma_f32_16x16x32_bf16 a[112:115], v[164:167], a[236:239], a[112:115]\n"
"ds_read_b64_tr_b16 a[226:227], v2 offset:4096\n"
"v_mfma_f32_16x16x32_bf16 a[116:119], v[172:175], a[236:239], a[116:119]\n"
"s_mov_b32 s79, s80\n"
"v_exp_f32 v151, v151\n"
"v_mfma_f32_16x16x32_bf16 a[116:119], v[168:171], a[232:235], a[116:119]\n"
"ds_read_b128 a[200:203], v1 offset:1024\n"
"v_cvt_pk_bf16_f32 v207, v150, v151\n"
"v_mfma_f32_16x16x32_bf16 v[156:159], a[208:211], v[112:115], v[140:143]\n"
"s_add_u32 s28, s28, 0x4000\n"
"v_exp_f32 v152, v152\n"
"v_mfma_f32_16x16x32_bf16 v[156:159], a[212:215], v[116:119], v[156:159]\n"
"ds_read_b128 a[204:207], v1 offset:1536\n"
"v_mfma_f32_16x16x32_bf16 v[156:159], a[216:219], v[120:123], v[156:159]\n"
"s_mov_b32 s80, s81\n"
"v_exp_f32 v153, v153\n"
"v_mfma_f32_16x16x32_bf16 v[156:159], a[220:223], v[124:127], v[156:159]\n"
"ds_read_b64_tr_b16 a[228:229], v2 offset:8192\n"
"v_cvt_pk_bf16_f32 v214, v152, v153\n"
"v_mfma_f32_16x16x32_bf16 a[120:123], v[176:179], a[232:235], a[120:123]\n"
"s_add_u32 m0, s79, 0xc000\n"
"v_exp_f32 v154, v154\n"
"v_mfma_f32_16x16x32_bf16 a[120:123], v[180:183], a[236:239], a[120:123]\n"
"ds_read_b64_tr_b16 a[230:231], v2 offset:12288\n"
"v_mfma_f32_16x16x32_bf16 a[124:127], v[188:191], a[236:239], a[124:127]\n"
"s_waitcnt lgkmcnt(2)\n"
"v_exp_f32 v155, v155\n"
"v_mfma_f32_16x16x32_bf16 a[124:127], v[184:187], a[232:235], a[124:127]\n"
"ds_read_b64_tr_b16 a[232:233], v3 offset:0\n"
"v_cvt_pk_bf16_f32 v215, v154, v155\n"
"v_mfma_f32_16x16x32_bf16 v[144:147], a[192:195], v[64:67], v[128:131]\n"
"v_exp_f32 v156, v156\n"
"v_mfma_f32_16x16x32_bf16 v[144:147], a[196:199], v[68:71], v[144:147]\n"
"ds_read_b64_tr_b16 a[234:235], v3 offset:4096\n"
"v_mfma_f32_16x16x32_bf16 v[144:147], a[200:203], v[72:75], v[144:147]\n"
"s_waitcnt lgkmcnt(2)\n"
"v_exp_f32 v157, v157\n"
"v_mfma_f32_16x16x32_bf16 v[144:147], a[204:207], v[76:79], v[144:147]\n"
"v_cvt_pk_bf16_f32 v222, v156, v157\n"
"v_mfma_f32_16x16x32_bf16 a[0:3], v[192:195], a[224:227], a[0:3]\n"
"v_exp_f32 v158, v158\n"
"v_mfma_f32_16x16x32_bf16 a[0:3], v[196:199], a[228:231], a[0:3]\n"
"buffer_load_dwordx4 v9, s[12:15], s28 offen lds\n"
"v_mfma_f32_16x16x32_bf16 a[4:7], v[204:207], a[228:231], a[4:7]\n"
"v_exp_f32 v159, v159\n"
"v_mfma_f32_16x16x32_bf16 a[4:7], v[200:203], a[224:227], a[4:7]\n"
"ds_read_b64_tr_b16 a[236:237], v3 offset:8192\n"
"v_cvt_pk_bf16_f32 v223, v158, v159\n"
"v_mfma_f32_16x16x32_bf16 v[148:151], a[192:195], v[80:83], v[132:135]\n"
"v_exp_f32 v144, v144\n"
"v_mfma_f32_16x16x32_bf16 v[148:151], a[196:199], v[84:87], v[148:151]\n"
"ds_read_b64_tr_b16 a[238:239], v3 offset:12288\n"
"v_mfma_f32_16x16x32_bf16 v[148:151], a[200:203], v[88:91], v[148:151]\n"
"v_exp_f32 v145, v145\n"
"v_mfma_f32_16x16x32_bf16 v[148:151], a[204:207], v[92:95], v[148:151]\n"
"v_cvt_pk_bf16_f32 v160, v144, v145\n"
"v_mfma_f32_16x16x32_bf16 a[8:11], v[208:211], a[224:227], a[8:11]\n"
"v_exp_f32 v146, v146\n"
"v_mfma_f32_16x16x32_bf16 a[8:11], v[212:215], a[228:231], a[8:11]\n"
"buffer_load_dwordx4 v9, s[12:15], s28 offen offset:2048 lds\n"
"v_mfma_f32_16x16x32_bf16 a[12:15], v[220:223], a[228:231], a[12:15]\n"
"v_exp_f32 v147, v147\n"
"v_mfma_f32_16x16x32_bf16 a[12:15], v[216:219], a[224:227], a[12:15]\n"
"ds_read_b128 a[208:211], v1 offset:4096\n"
"v_cvt_pk_bf16_f32 v161, v146, v147\n"
"v_mfma_f32_16x16x32_bf16 a[240:243], v[28:31], v[192:195], a[240:243]\n"
"v_mfma_f32_16x16x32_bf16 a[240:243], v[28:31], v[196:199], a[240:243]\n"
"v_mfma_f32_16x16x32_bf16 v[152:155], a[192:195], v[96:99], v[136:139]\n"
"v_exp_f32 v148, v148\n"
"v_mfma_f32_16x16x32_bf16 v[152:155], a[196:199], v[100:103], v[152:155]\n"
"ds_read_b128 a[212:215], v1 offset:4608\n"
"v_mfma_f32_16x16x32_bf16 v[152:155], a[200:203], v[104:107], v[152:155]\n"
"s_waitcnt lgkmcnt(2)\n"
"v_exp_f32 v149, v149\n"
"v_mfma_f32_16x16x32_bf16 v[152:155], a[204:207], v[108:111], v[152:155]\n"
"ds_read_b64_tr_b16 a[224:225], v2 offset:512\n"
"v_cvt_pk_bf16_f32 v168, v148, v149\n"
"v_mfma_f32_16x16x32_bf16 a[16:19], v[192:195], a[232:235], a[16:19]\n"
"v_exp_f32 v150, v150\n"
"v_mfma_f32_16x16x32_bf16 a[16:19], v[196:199], a[236:239], a[16:19]\n"
"ds_read_b64_tr_b16 a[226:227], v2 offset:4608\n"
"v_mfma_f32_16x16x32_bf16 a[20:23], v[204:207], a[236:239], a[20:23]\n"
"v_exp_f32 v151, v151\n"
"v_mfma_f32_16x16x32_bf16 a[20:23], v[200:203], a[232:235], a[20:23]\n"
"ds_read_b128 a[216:219], v1 offset:5120\n"
"v_cvt_pk_bf16_f32 v169, v150, v151\n"
"v_mfma_f32_16x16x32_bf16 v[156:159], a[192:195], v[112:115], v[140:143]\n"
"v_exp_f32 v152, v152\n"
"v_mfma_f32_16x16x32_bf16 v[156:159], a[196:199], v[116:119], v[156:159]\n"
"ds_read_b128 a[220:223], v1 offset:5632\n"
"v_mfma_f32_16x16x32_bf16 v[156:159], a[200:203], v[120:123], v[156:159]\n"
"v_exp_f32 v153, v153\n"
"v_mfma_f32_16x16x32_bf16 v[156:159], a[204:207], v[124:127], v[156:159]\n"
"ds_read_b64_tr_b16 a[228:229], v2 offset:8704\n"
"v_cvt_pk_bf16_f32 v176, v152, v153\n"
"v_mfma_f32_16x16x32_bf16 a[24:27], v[208:211], a[232:235], a[24:27]\n"
"s_add_u32 m0, s79, 0xd000\n"
"v_exp_f32 v154, v154\n"
"v_mfma_f32_16x16x32_bf16 a[24:27], v[212:215], a[236:239], a[24:27]\n"
"ds_read_b64_tr_b16 a[230:231], v2 offset:12800\n"
"v_mfma_f32_16x16x32_bf16 a[28:31], v[220:223], a[236:239], a[28:31]\n"
"s_waitcnt lgkmcnt(2)\n"
"v_exp_f32 v155, v155\n"
"v_mfma_f32_16x16x32_bf16 a[28:31], v[216:219], a[232:235], a[28:31]\n"
"ds_read_b64_tr_b16 a[232:233], v3 offset:512\n"
"v_cvt_pk_bf16_f32 v177, v154, v155\n"
"v_mfma_f32_16x16x32_bf16 v[144:147], a[208:211], v[64:67], v[128:131]\n"
"v_exp_f32 v156, v156\n"
"v_mfma_f32_16x16x32_bf16 v[144:147], a[212:215], v[68:71], v[144:147]\n"
"ds_read_b64_tr_b16 a[234:235], v3 offset:4608\n"
"v_mfma_f32_16x16x32_bf16 v[144:147], a[216:219], v[72:75], v[144:147]\n"
"s_waitcnt lgkmcnt(2)\n"
"v_exp_f32 v157, v157\n"
"v_mfma_f32_16x16x32_bf16 v[144:147], a[220:223], v[76:79], v[144:147]\n"
"v_cvt_pk_bf16_f32 v184, v156, v157\n"
"v_mfma_f32_16x16x32_bf16 a[32:35], v[192:195], a[224:227], a[32:35]\n"
"v_exp_f32 v158, v158\n"
"v_mfma_f32_16x16x32_bf16 a[32:35], v[196:199], a[228:231], a[32:35]\n"
"buffer_load_dwordx4 v10, s[12:15], s28 offen lds\n"
"v_mfma_f32_16x16x32_bf16 a[36:39], v[204:207], a[228:231], a[36:39]\n"
"v_exp_f32 v159, v159\n"
"v_mfma_f32_16x16x32_bf16 a[36:39], v[200:203], a[224:227], a[36:39]\n"
"ds_read_b64_tr_b16 a[236:237], v3 offset:8704\n"
"v_cvt_pk_bf16_f32 v185, v158, v159\n"
"v_mfma_f32_16x16x32_bf16 v[148:151], a[208:211], v[80:83], v[132:135]\n"
"v_exp_f32 v144, v144\n"
"v_mfma_f32_16x16x32_bf16 v[148:151], a[212:215], v[84:87], v[148:151]\n"
"ds_read_b64_tr_b16 a[238:239], v3 offset:12800\n"
"v_mfma_f32_16x16x32_bf16 v[148:151], a[216:219], v[88:91], v[148:151]\n"
"v_exp_f32 v145, v145\n"
"v_mfma_f32_16x16x32_bf16 v[148:151], a[220:223], v[92:95], v[148:151]\n"
"v_cvt_pk_bf16_f32 v162, v144, v145\n"
"v_mfma_f32_16x16x32_bf16 a[40:43], v[208:211], a[224:227], a[40:43]\n"
"v_exp_f32 v146, v146\n"
"v_mfma_f32_16x16x32_bf16 a[40:43], v[212:215], a[228:231], a[40:43]\n"
"buffer_load_dwordx4 v10, s[12:15], s28 offen offset:2048 lds\n"
"v_mfma_f32_16x16x32_bf16 a[44:47], v[220:223], a[228:231], a[44:47]\n"
"v_exp_f32 v147, v147\n"
"v_mfma_f32_16x16x32_bf16 a[44:47], v[216:219], a[224:227], a[44:47]\n"
"ds_read_b128 a[192:195], v1 offset:8192\n"
"v_cvt_pk_bf16_f32 v163, v146, v147\n"
"v_mfma_f32_16x16x32_bf16 a[244:247], v[28:31], v[200:203], a[244:247]\n"
"v_mfma_f32_16x16x32_bf16 a[244:247], v[28:31], v[204:207], a[244:247]\n"
"v_mfma_f32_16x16x32_bf16 v[152:155], a[208:211], v[96:99], v[136:139]\n"
"v_exp_f32 v148, v148\n"
"v_mfma_f32_16x16x32_bf16 v[152:155], a[212:215], v[100:103], v[152:155]\n"
"ds_read_b128 a[196:199], v1 offset:8704\n"
"v_mfma_f32_16x16x32_bf16 v[152:155], a[216:219], v[104:107], v[152:155]\n"
"s_waitcnt lgkmcnt(2)\n"
"v_exp_f32 v149, v149\n"
"v_mfma_f32_16x16x32_bf16 v[152:155], a[220:223], v[108:111], v[152:155]\n"
"ds_read_b64_tr_b16 a[224:225], v2 offset:1024\n"
"v_cvt_pk_bf16_f32 v170, v148, v149\n"
"v_mfma_f32_16x16x32_bf16 a[48:51], v[192:195], a[232:235], a[48:51]\n"
"v_exp_f32 v150, v150\n"
"v_mfma_f32_16x16x32_bf16 a[48:51], v[196:199], a[236:239], a[48:51]\n"
"ds_read_b64_tr_b16 a[226:227], v2 offset:5120\n"
"v_mfma_f32_16x16x32_bf16 a[52:55], v[204:207], a[236:239], a[52:55]\n"
"v_exp_f32 v151, v151\n"
"v_mfma_f32_16x16x32_bf16 a[52:55], v[200:203], a[232:235], a[52:55]\n"
"ds_read_b128 a[200:203], v1 offset:9216\n"
"v_cvt_pk_bf16_f32 v171, v150, v151\n"
"v_mfma_f32_16x16x32_bf16 v[156:159], a[208:211], v[112:115], v[140:143]\n"
"v_exp_f32 v152, v152\n"
"v_mfma_f32_16x16x32_bf16 v[156:159], a[212:215], v[116:119], v[156:159]\n"
"ds_read_b128 a[204:207], v1 offset:9728\n"
"v_mfma_f32_16x16x32_bf16 v[156:159], a[216:219], v[120:123], v[156:159]\n"
"v_exp_f32 v153, v153\n"
"v_mfma_f32_16x16x32_bf16 v[156:159], a[220:223], v[124:127], v[156:159]\n"
"ds_read_b64_tr_b16 a[228:229], v2 offset:9216\n"
"v_cvt_pk_bf16_f32 v178, v152, v153\n"
"v_mfma_f32_16x16x32_bf16 a[56:59], v[208:211], a[232:235], a[56:59]\n"
"s_add_u32 m0, s80, 0x0\n"
"v_exp_f32 v154, v154\n"
"v_mfma_f32_16x16x32_bf16 a[56:59], v[212:215], a[236:239], a[56:59]\n"
"ds_read_b64_tr_b16 a[230:231], v2 offset:13312\n"
"v_mfma_f32_16x16x32_bf16 a[60:63], v[220:223], a[236:239], a[60:63]\n"
"s_waitcnt lgkmcnt(2)\n"
"v_exp_f32 v155, v155\n"
"v_mfma_f32_16x16x32_bf16 a[60:63], v[216:219], a[232:235], a[60:63]\n"
"ds_read_b64_tr_b16 a[232:233], v3 offset:1024\n"
"v_cvt_pk_bf16_f32 v179, v154, v155\n"
"v_mfma_f32_16x16x32_bf16 v[144:147], a[192:195], v[64:67], v[128:131]\n"
"v_exp_f32 v156, v156\n"
"v_mfma_f32_16x16x32_bf16 v[144:147], a[196:199], v[68:71], v[144:147]\n"
"ds_read_b64_tr_b16 a[234:235], v3 offset:5120\n"
"v_mfma_f32_16x16x32_bf16 v[144:147], a[200:203], v[72:75], v[144:147]\n"
"s_waitcnt lgkmcnt(2)\n"
"v_exp_f32 v157, v157\n"
"v_mfma_f32_16x16x32_bf16 v[144:147], a[204:207], v[76:79], v[144:147]\n"
"v_cvt_pk_bf16_f32 v186, v156, v157\n"
"v_mfma_f32_16x16x32_bf16 a[64:67], v[192:195], a[224:227], a[64:67]\n"
"v_exp_f32 v158, v158\n"
"v_mfma_f32_16x16x32_bf16 a[64:67], v[196:199], a[228:231], a[64:67]\n"
"buffer_load_dwordx4 v5, s[8:11], s28 offen lds\n"
"v_mfma_f32_16x16x32_bf16 a[68:71], v[204:207], a[228:231], a[68:71]\n"
"v_exp_f32 v159, v159\n"
"v_mfma_f32_16x16x32_bf16 a[68:71], v[200:203], a[224:227], a[68:71]\n"
"ds_read_b64_tr_b16 a[236:237], v3 offset:9216\n"
"v_cvt_pk_bf16_f32 v187, v158, v159\n"
"v_mfma_f32_16x16x32_bf16 v[148:151], a[192:195], v[80:83], v[132:135]\n"
"v_exp_f32 v144, v144\n"
"v_mfma_f32_16x16x32_bf16 v[148:151], a[196:199], v[84:87], v[148:151]\n"
"ds_read_b64_tr_b16 a[238:239], v3 offset:13312\n"
"v_mfma_f32_16x16x32_bf16 v[148:151], a[200:203], v[88:91], v[148:151]\n"
"v_exp_f32 v145, v145\n"
"v_mfma_f32_16x16x32_bf16 v[148:151], a[204:207], v[92:95], v[148:151]\n"
"buffer_load_dword v15, v63, s[64:67], s68 offen\n"
"v_cvt_pk_bf16_f32 v164, v144, v145\n"
"v_mfma_f32_16x16x32_bf16 a[72:75], v[208:211], a[224:227], a[72:75]\n"
"s_add_u32 s68, s68, s69\n"
"v_exp_f32 v146, v146\n"
"v_mfma_f32_16x16x32_bf16 a[72:75], v[212:215], a[228:231], a[72:75]\n"
"buffer_load_dwordx4 v6, s[8:11], s28 offen offset:2048 lds\n"
"v_mfma_f32_16x16x32_bf16 a[76:79], v[220:223], a[228:231], a[76:79]\n"
"v_exp_f32 v147, v147\n"
"v_mfma_f32_16x16x32_bf16 a[76:79], v[216:219], a[224:227], a[76:79]\n"
"ds_read_b128 a[208:211], v1 offset:12288\n"
"v_cvt_pk_bf16_f32 v165, v146, v147\n"
"v_mfma_f32_16x16x32_bf16 a[248:251], v[28:31], v[208:211], a[248:251]\n"
"v_mfma_f32_16x16x32_bf16 a[248:251], v[28:31], v[212:215], a[248:251]\n"
"v_mfma_f32_16x16x32_bf16 v[152:155], a[192:195], v[96:99], v[136:139]\n"
"v_exp_f32 v148, v148\n"
"v_mfma_f32_16x16x32_bf16 v[152:155], a[196:199], v[100:103], v[152:155]\n"
"ds_read_b128 a[212:215], v1 offset:12800\n"
"v_mfma_f32_16x16x32_bf16 v[152:155], a[200:203], v[104:107], v[152:155]\n"
"s_waitcnt lgkmcnt(2)\n"
"v_exp_f32 v149, v149\n"
"v_mfma_f32_16x16x32_bf16 v[152:155], a[204:207], v[108:111], v[152:155]\n"
"ds_read_b64_tr_b16 a[224:225], v2 offset:1536\n"
"v_cvt_pk_bf16_f32 v172, v148, v149\n"
"v_mfma_f32_16x16x32_bf16 a[80:83], v[192:195], a[232:235], a[80:83]\n"
"v_exp_f32 v150, v150\n"
"v_mfma_f32_16x16x32_bf16 a[80:83], v[196:199], a[236:239], a[80:83]\n"
"ds_read_b64_tr_b16 a[226:227], v2 offset:5632\n"
"v_mfma_f32_16x16x32_bf16 a[84:87], v[204:207], a[236:239], a[84:87]\n"
"v_exp_f32 v151, v151\n"
"v_mfma_f32_16x16x32_bf16 a[84:87], v[200:203], a[232:235], a[84:87]\n"
"ds_read_b128 a[216:219], v1 offset:13312\n"
"v_cvt_pk_bf16_f32 v173, v150, v151\n"
"v_mfma_f32_16x16x32_bf16 v[156:159], a[192:195], v[112:115], v[140:143]\n"
"v_exp_f32 v152, v152\n"
"v_mfma_f32_16x16x32_bf16 v[156:159], a[196:199], v[116:119], v[156:159]\n"
"ds_read_b128 a[220:223], v1 offset:13824\n"
"v_mfma_f32_16x16x32_bf16 v[156:159], a[200:203], v[120:123], v[156:159]\n"
"v_exp_f32 v153, v153\n"
"v_mfma_f32_16x16x32_bf16 v[156:159], a[204:207], v[124:127], v[156:159]\n"
"ds_read_b64_tr_b16 a[228:229], v2 offset:9728\n"
"v_cvt_pk_bf16_f32 v180, v152, v153\n"
"v_add_u32 v1, s79, v4\n"
"v_mfma_f32_16x16x32_bf16 a[88:91], v[208:211], a[232:235], a[88:91]\n"
"s_add_u32 m0, s80, 0x1000\n"
"v_exp_f32 v154, v154\n"
"v_mfma_f32_16x16x32_bf16 a[88:91], v[212:215], a[236:239], a[88:91]\n"
"ds_read_b64_tr_b16 a[230:231], v2 offset:13824\n"
"v_mfma_f32_16x16x32_bf16 a[92:95], v[220:223], a[236:239], a[92:95]\n"
"s_waitcnt lgkmcnt(2)\n"
"v_exp_f32 v155, v155\n"
"v_mfma_f32_16x16x32_bf16 a[92:95], v[216:219], a[232:235], a[92:95]\n"
"ds_read_b64_tr_b16 a[232:233], v3 offset:1536\n"
"v_cvt_pk_bf16_f32 v181, v154, v155\n"
"v_mfma_f32_16x16x32_bf16 v[144:147], a[208:211], v[64:67], v[128:131]\n"
"v_exp_f32 v156, v156\n"
"v_mfma_f32_16x16x32_bf16 v[144:147], a[212:215], v[68:71], v[144:147]\n"
"ds_read_b64_tr_b16 a[234:235], v3 offset:5632\n"
"v_add_u32 v2, s78, v11\n"
"v_mfma_f32_16x16x32_bf16 v[144:147], a[216:219], v[72:75], v[144:147]\n"
"s_waitcnt lgkmcnt(2)\n"
"v_exp_f32 v157, v157\n"
"v_mfma_f32_16x16x32_bf16 v[144:147], a[220:223], v[76:79], v[144:147]\n"
"v_cvt_pk_bf16_f32 v188, v156, v157\n"
"v_mfma_f32_16x16x32_bf16 a[96:99], v[192:195], a[224:227], a[96:99]\n"
"v_exp_f32 v158, v158\n"
"v_mfma_f32_16x16x32_bf16 a[96:99], v[196:199], a[228:231], a[96:99]\n"
"buffer_load_dwordx4 v7, s[8:11], s28 offen lds\n"
"v_mfma_f32_16x16x32_bf16 a[100:103], v[204:207], a[228:231], a[100:103]\n"
"v_exp_f32 v159, v159\n"
"v_mfma_f32_16x16x32_bf16 a[100:103], v[200:203], a[224:227], a[100:103]\n"
"ds_read_b64_tr_b16 a[236:237], v3 offset:9728\n"
"v_cvt_pk_bf16_f32 v189, v158, v159\n"
"v_mfma_f32_16x16x32_bf16 v[148:151], a[208:211], v[80:83], v[132:135]\n"
"v_exp_f32 v144, v144\n"
"v_mfma_f32_16x16x32_bf16 v[148:151], a[212:215], v[84:87], v[148:151]\n"
"ds_read_b64_tr_b16 a[238:239], v3 offset:13824\n"
"v_mfma_f32_16x16x32_bf16 v[148:151], a[216:219], v[88:91], v[148:151]\n"
"s_mov_b32 s81, s78\n"
"v_exp_f32 v145, v145\n"
"v_mfma_f32_16x16x32_bf16 v[148:151], a[220:223], v[92:95], v[148:151]\n"
"v_cvt_pk_bf16_f32 v166, v144, v145\n"
"v_mfma_f32_16x16x32_bf16 a[104:107], v[208:211], a[224:227], a[104:107]\n"
"s_mov_b32 s78, s79\n"
"v_exp_f32 v146, v146\n"
"v_mfma_f32_16x16x32_bf16 a[104:107], v[212:215], a[228:231], a[104:107]\n"
"buffer_load_dwordx4 v8, s[8:11], s28 offen offset:2048 lds\n"
"v_xor_b32 v3, 32, v2\n"
"v_mfma_f32_16x16x32_bf16 a[108:111], v[220:223], a[228:231], a[108:111]\n"
"s_waitcnt vmcnt(9) lgkmcnt(0)\n"
"s_barrier\n"
"v_exp_f32 v147, v147\n"
"v_mfma_f32_16x16x32_bf16 a[108:111], v[216:219], a[224:227], a[108:111]\n"
"ds_read_b128 a[192:195], v1 offset:0\n"
"v_cvt_pk_bf16_f32 v167, v146, v147\n"
"v_mfma_f32_16x16x32_bf16 a[252:255], v[28:31], v[216:219], a[252:255]\n"
"v_mfma_f32_16x16x32_bf16 a[252:255], v[28:31], v[220:223], a[252:255]\n"
"v_mfma_f32_16x16x32_bf16 v[152:155], a[208:211], v[96:99], v[136:139]\n"
"s_mov_b64 exec, 1\n"
"buffer_store_dwordx2 v[56:57], off, s[92:95], s82 sc1\n"
"s_mov_b64 exec, -1\n"
"s_mov_b32 s82, 0x7fff0000\n"
"v_exp_f32 v148, v148\n"
"v_mfma_f32_16x16x32_bf16 v[152:155], a[212:215], v[100:103], v[152:155]\n"
"ds_read_b128 a[196:199], v1 offset:512\n"
"v_mfma_f32_16x16x32_bf16 v[152:155], a[216:219], v[104:107], v[152:155]\n"
"s_waitcnt lgkmcnt(2)\n"
"v_exp_f32 v149, v149\n"
"v_mfma_f32_16x16x32_bf16 v[152:155], a[220:223], v[108:111], v[152:155]\n"
"ds_read_b64_tr_b16 a[224:225], v2 offset:0\n"
"v_cvt_pk_bf16_f32 v174, v148, v149\n"
"v_mfma_f32_16x16x32_bf16 a[112:115], v[192:195], a[232:235], a[112:115]\n"
"s_mov_b64 exec, 1\n"
"buffer_store_dwordx2 v[56:57], off, s[92:95], s83 sc1\n"
"s_mov_b64 exec, -1\n"
"s_mov_b32 s83, 0x7fff0000\n"
"v_exp_f32 v150, v150\n"
"v_mfma_f32_16x16x32_bf16 a[112:115], v[196:199], a[236:239], a[112:115]\n"
"ds_read_b64_tr_b16 a[226:227], v2 offset:4096\n"
"v_mfma_f32_16x16x32_bf16 a[116:119], v[204:207], a[236:239], a[116:119]\n"
"s_mov_b32 s79, s80\n"
"v_exp_f32 v151, v151\n"
"v_mfma_f32_16x16x32_bf16 a[116:119], v[200:203], a[232:235], a[116:119]\n"
"ds_read_b128 a[200:203], v1 offset:1024\n"
"v_cvt_pk_bf16_f32 v175, v150, v151\n"
"v_mfma_f32_16x16x32_bf16 v[156:159], a[208:211], v[112:115], v[140:143]\n"
"s_add_u32 s28, s28, 0x4000\n"
"v_exp_f32 v152, v152\n"
"v_mfma_f32_16x16x32_bf16 v[156:159], a[212:215], v[116:119], v[156:159]\n"
"ds_read_b128 a[204:207], v1 offset:1536\n"
"v_mfma_f32_16x16x32_bf16 v[156:159], a[216:219], v[120:123], v[156:159]\n"
"s_mov_b32 s80, s81\n"
"v_exp_f32 v153, v153\n"
"v_mfma_f32_16x16x32_bf16 v[156:159], a[220:223], v[124:127], v[156:159]\n"
"ds_read_b64_tr_b16 a[228:229], v2 offset:8192\n"
"v_cvt_pk_bf16_f32 v182, v152, v153\n"
"v_mfma_f32_16x16x32_bf16 a[120:123], v[208:211], a[232:235], a[120:123]\n"
"s_add_u32 m0, s79, 0xc000\n"
"v_exp_f32 v154, v154\n"
"v_mfma_f32_16x16x32_bf16 a[120:123], v[212:215], a[236:239], a[120:123]\n"
"ds_read_b64_tr_b16 a[230:231], v2 offset:12288\n"
"v_mfma_f32_16x16x32_bf16 a[124:127], v[220:223], a[236:239], a[124:127]\n"
"s_waitcnt lgkmcnt(2)\n"
"v_exp_f32 v155, v155\n"
"v_mfma_f32_16x16x32_bf16 a[124:127], v[216:219], a[232:235], a[124:127]\n"
"s_cmp_lt_u32 s28, 0x7c000\n"
"s_cbranch_scc1 L_LOOPB0%=\n"
"L_XB%=:\n"
"ds_read_b64_tr_b16 a[232:233], v3 offset:0\n"
"v_cvt_pk_bf16_f32 v183, v154, v155\n"
"v_mfma_f32_16x16x32_bf16 v[144:147], a[192:195], v[64:67], v[128:131]\n"
"v_exp_f32 v156, v156\n"
"v_mfma_f32_16x16x32_bf16 v[144:147], a[196:199], v[68:71], v[144:147]\n"
"ds_read_b64_tr_b16 a[234:235], v3 offset:4096\n"
"v_mfma_f32_16x16x32_bf16 v[144:147], a[200:203], v[72:75], v[144:147]\n"
"s_waitcnt lgkmcnt(2)\n"
"v_exp_f32 v157, v157\n"
"v_mfma_f32_16x16x32_bf16 v[144:147], a[204:207], v[76:79], v[144:147]\n"
"v_cvt_pk_bf16_f32 v190, v156, v157\n"
"v_mfma_f32_16x16x32_bf16 a[0:3], v[160:163], a[224:227], a[0:3]\n"
"v_exp_f32 v158, v158\n"
"v_mfma_f32_16x16x32_bf16 a[0:3], v[164:167], a[228:231], a[0:3]\n"
"buffer_load_dwordx2 v[54:55], v53, s[92:95], 0 offen sc1\n"
"v_mfma_f32_16x16x32_bf16 a[4:7], v[172:175], a[228:231], a[4:7]\n"
"v_exp_f32 v159, v159\n"
"v_mfma_f32_16x16x32_bf16 a[4:7], v[168:171], a[224:227], a[4:7]\n"
"ds_read_b64_tr_b16 a[236:237], v3 offset:8192\n"
"v_cvt_pk_bf16_f32 v191, v158, v159\n"
"v_lshrrev_b32 v236, 4, v13\n"
"v_mfma_f32_16x16x32_bf16 v[148:151], a[192:195], v[80:83], v[132:135]\n"
"v_exp_f32 v144, v144\n"
"v_mfma_f32_16x16x32_bf16 v[148:151], a[196:199], v[84:87], v[148:151]\n"
"ds_read_b64_tr_b16 a[238:239], v3 offset:12288\n"
"v_add_f32 v32, v32, v144\n"
"v_and_b32 v235, 15, v13\n"
"v_mfma_f32_16x16x32_bf16 v[148:151], a[200:203], v[88:91], v[148:151]\n"
"v_exp_f32 v145, v145\n"
"v_mfma_f32_16x16x32_bf16 v[148:151], a[204:207], v[92:95], v[148:151]\n"
"v_add_f32 v32, v32, v145\n"
"v_cvt_pk_bf16_f32 v192, v144, v145\n"
"v_mul_u32_u24 v234, 1280, v235\n"
"v_mfma_f32_16x16x32_bf16 a[8:11], v[176:179], a[224:227], a[8:11]\n"
"v_exp_f32 v146, v146\n"
"v_mfma_f32_16x16x32_bf16 a[8:11], v[180:183], a[228:231], a[8:11]\n"
"v_add_f32 v32, v32, v146\n"
"v_lshl_add_u32 v234, v236, 3, v234\n"
"v_mfma_f32_16x16x32_bf16 a[12:15], v[188:191], a[228:231], a[12:15]\n"
"v_exp_f32 v147, v147\n"
"v_mfma_f32_16x16x32_bf16 a[12:15], v[184:187], a[224:227], a[12:15]\n"
"ds_read_b128 a[208:211], v1 offset:4096\n"
"v_add_f32 v32, v32, v147\n"
"v_cvt_pk_bf16_f32 v193, v146, v147\n"
"v_mfma_f32_16x16x32_bf16 a[240:243], v[28:31], v[160:163], a[240:243]\n"
"v_mfma_f32_16x16x32_bf16 a[240:243], v[28:31], v[164:167], a[240:243]\n"
"v_mfma_f32_16x16x32_bf16 v[152:155], a[192:195], v[96:99], v[136:139]\n"
"s_lshl_b32 s60, s98, 2\n"
"s_add_u32 s60, s60, s90\n"
"v_exp_f32 v148, v148\n"
"v_mfma_f32_16x16x32_bf16 v[152:155], a[196:199], v[100:103], v[152:155]\n"
"ds_read_b128 a[212:215], v1 offset:4608\n"
"s_lshl_b32 s61, s60, 5\n"
"s_add_u32 s84, s52, s61\n"
"s_addc_u32 s85, s53, 0\n"
"v_add_f32 v33, v33, v148\n"
"v_mfma_f32_16x16x32_bf16 v[152:155], a[200:203], v[104:107], v[152:155]\n"
"s_waitcnt lgkmcnt(2)\n"
"v_exp_f32 v149, v149\n"
"v_mfma_f32_16x16x32_bf16 v[152:155], a[204:207], v[108:111], v[152:155]\n"
"ds_read_b64_tr_b16 a[224:225], v2 offset:512\n"
"s_mov_b32 s86, 0x20\n"
"s_mov_b32 s87, 0x20000\n"
"v_add_f32 v33, v33, v149\n"
"v_cvt_pk_bf16_f32 v200, v148, v149\n"
"v_mfma_f32_16x16x32_bf16 a[16:19], v[160:163], a[232:235], a[16:19]\n"
"v_exp_f32 v150, v150\n"
"v_lshlrev_b32 v235, 1, v235\n"
"v_mfma_f32_16x16x32_bf16 a[16:19], v[164:167], a[236:239], a[16:19]\n"
"ds_read_b64_tr_b16 a[226:227], v2 offset:4608\n"
"v_add_f32 v33, v33, v150\n"
"v_mfma_f32_16x16x32_bf16 a[20:23], v[172:175], a[236:239], a[20:23]\n"
"buffer_load_ushort v235, v235, s[84:87], 0 offen\n"
"v_exp_f32 v151, v151\n"
"v_mfma_f32_16x16x32_bf16 a[20:23], v[168:171], a[232:235], a[20:23]\n"
"ds_read_b128 a[216:219], v1 offset:5120\n"
"v_add_f32 v33, v33, v151\n"
"v_cvt_pk_bf16_f32 v201, v150, v151\n"
"v_mfma_f32_16x16x32_bf16 v[156:159], a[192:195], v[112:115], v[140:143]\n"
"v_exp_f32 v152, v152\n"
"v_mfma_f32_16x16x32_bf16 v[156:159], a[196:199], v[116:119], v[156:159]\n"
"ds_read_b128 a[220:223], v1 offset:5632\n"
"v_add_f32 v34, v34, v152\n"
"v_mfma_f32_16x16x32_bf16 v[156:159], a[200:203], v[120:123], v[156:159]\n"
"v_exp_f32 v153, v153\n"
"v_mfma_f32_16x16x32_bf16 v[156:159], a[204:207], v[124:127], v[156:159]\n"
"ds_read_b64_tr_b16 a[228:229], v2 offset:8704\n"
"s_mul_i32 s60, s60, 20480\n"
"s_mul_i32 s61, s99, 160\n"
"v_add_f32 v34, v34, v153\n"
"v_cvt_pk_bf16_f32 v208, v152, v153\n"
"v_mfma_f32_16x16x32_bf16 a[24:27], v[176:179], a[232:235], a[24:27]\n"
"s_add_u32 s60, s60, s61\n"
"s_add_u32 s72, s36, s60\n"
"s_addc_u32 s73, s37, 0\n"
"v_exp_f32 v154, v154\n"
"v_mfma_f32_16x16x32_bf16 a[24:27], v[180:183], a[236:239], a[24:27]\n"
"ds_read_b64_tr_b16 a[230:231], v2 offset:12800\n"
"s_mov_b32 s74, 0x4ba0\n"
"v_add_f32 v34, v34, v154\n"
"v_mfma_f32_16x16x32_bf16 a[28:31], v[188:191], a[236:239], a[28:31]\n"
"s_waitcnt lgkmcnt(2)\n"
"s_mov_b32 s75, 0x20000\n"
"v_exp_f32 v155, v155\n"
"v_mfma_f32_16x16x32_bf16 a[28:31], v[184:187], a[232:235], a[28:31]\n"
"ds_read_b64_tr_b16 a[232:233], v3 offset:512\n"
"v_add_f32 v34, v34, v155\n"
"v_cvt_pk_bf16_f32 v209, v154, v155\n"
"v_lshl_add_u32 v236, v236, 3, v234\n"
"v_mfma_f32_16x16x32_bf16 v[144:147], a[208:211], v[64:67], v[128:131]\n"
"v_exp_f32 v156, v156\n"
"v_mfma_f32_16x16x32_bf16 v[144:147], a[212:215], v[68:71], v[144:147]\n"
"ds_read_b64_tr_b16 a[234:235], v3 offset:4608\n"
"buffer_load_dwordx4 v[224:227], v236, s[72:75], 0 offen\n"
"v_add_f32 v35, v35, v156\n"
"v_mfma_f32_16x16x32_bf16 v[144:147], a[216:219], v[72:75], v[144:147]\n"
"s_waitcnt lgkmcnt(2)\n"
"v_exp_f32 v157, v157\n"
"v_mfma_f32_16x16x32_bf16 v[144:147], a[220:223], v[76:79], v[144:147]\n"
"v_add_f32 v35, v35, v157\n"
"v_cvt_pk_bf16_f32 v216, v156, v157\n"
"v_mfma_f32_16x16x32_bf16 a[32:35], v[160:163], a[224:227], a[32:35]\n"
"v_exp_f32 v158, v158\n"
"v_mfma_f32_16x16x32_bf16 a[32:35], v[164:167], a[228:231], a[32:35]\n"
"buffer_load_dwordx4 v[228:231], v236, s[72:75], 0 offen offset:64\n"
"v_add_f32 v35, v35, v158\n"
"v_mfma_f32_16x16x32_bf16 a[36:39], v[172:175], a[228:231], a[36:39]\n"
"v_exp_f32 v159, v159\n"
"v_mfma_f32_16x16x32_bf16 a[36:39], v[168:171], a[224:227], a[36:39]\n"
"ds_read_b64_tr_b16 a[236:237], v3 offset:8704\n"
"v_add_f32 v35, v35, v159\n"
"v_cvt_pk_bf16_f32 v217, v158, v159\n"
"v_mfma_f32_16x16x32_bf16 v[148:151], a[208:211], v[80:83], v[132:135]\n"
"v_exp_f32 v144, v144\n"
"v_mfma_f32_16x16x32_bf16 v[148:151], a[212:215], v[84:87], v[148:151]\n"
"ds_read_b64_tr_b16 a[238:239], v3 offset:12800\n"
"v_add_f32 v32, v32, v144\n"
"v_mfma_f32_16x16x32_bf16 v[148:151], a[216:219], v[88:91], v[148:151]\n"
"buffer_load_dwordx2 v[232:233], v234, s[72:75], 0 offen offset:128\n"
"v_exp_f32 v145, v145\n"
"v_mfma_f32_16x16x32_bf16 v[148:151], a[220:223], v[92:95], v[148:151]\n"
"v_add_f32 v32, v32, v145\n"
"v_cvt_pk_bf16_f32 v194, v144, v145\n"
"v_mfma_f32_16x16x32_bf16 a[40:43], v[176:179], a[224:227], a[40:43]\n"
"v_exp_f32 v146, v146\n"
"v_mfma_f32_16x16x32_bf16 a[40:43], v[180:183], a[228:231], a[40:43]\n"
"v_add_f32 v32, v32, v146\n"
"v_mfma_f32_16x16x32_bf16 a[44:47], v[188:191], a[228:231], a[44:47]\n"
"v_exp_f32 v147, v147\n"
"v_mfma_f32_16x16x32_bf16 a[44:47], v[184:187], a[224:227], a[44:47]\n"
"ds_read_b128 a[192:195], v1 offset:8192\n"
"v_add_f32 v32, v32, v147\n"
"v_cvt_pk_bf16_f32 v195, v146, v147\n"
"v_mfma_f32_16x16x32_bf16 a[244:247], v[28:31], v[168:171], a[244:247]\n"
"v_mfma_f32_16x16x32_bf16 a[244:247], v[28:31], v[172:175], a[244:247]\n"
"v_mfma_f32_16x16x32_bf16 v[152:155], a[208:211], v[96:99], v[136:139]\n"
"v_exp_f32 v148, v148\n"
"v_mfma_f32_16x16x32_bf16 v[152:155], a[212:215], v[100:103], v[152:155]\n"
"ds_read_b128 a[196:199], v1 offset:8704\n"
"v_add_f32 v33, v33, v148\n"
"v_mfma_f32_16x16x32_bf16 v[152:155], a[216:219], v[104:107], v[152:155]\n"
"s_waitcnt lgkmcnt(2)\n"
"v_exp_f32 v149, v149\n"
"v_mfma_f32_16x16x32_bf16 v[152:155], a[220:223], v[108:111], v[152:155]\n"
"ds_read_b64_tr_b16 a[224:225], v2 offset:1024\n"
"v_add_f32 v33, v33, v149\n"
"v_cvt_pk_bf16_f32 v202, v148, v149\n"
"v_mfma_f32_16x16x32_bf16 a[48:51], v[160:163], a[232:235], a[48:51]\n"
"v_exp_f32 v150, v150\n"
"v_mfma_f32_16x16x32_bf16 a[48:51], v[164:167], a[236:239], a[48:51]\n"
"ds_read_b64_tr_b16 a[226:227], v2 offset:5120\n"
"v_add_f32 v33, v33, v150\n"
"v_mfma_f32_16x16x32_bf16 a[52:55], v[172:175], a[236:239], a[52:55]\n"
"s_waitcnt vmcnt(4)\n"
"v_cmp_eq_u64 vcc, s[88:89], v[54:55]\n"
"s_mov_b32 s91, 0\n"
"s_nop 2\n"
"s_cmp_eq_u64 vcc, -1\n"
"s_cbranch_scc1 L_OK3%=\n"
"L_SP3%=:\n"
"s_sleep 2\n"
"buffer_load_dwordx2 v[54:55], v53, s[92:95], 0 offen sc1\n"
"s_waitcnt vmcnt(0)\n"
"v_cmp_eq_u64 vcc, s[88:89], v[54:55]\n"
"s_add_u32 s91, s91, 1\n"
"s_nop 3\n"
"s_cmp_eq_u64 vcc, -1\n"
"s_cbranch_scc1 L_OK3%=\n"
"s_cmp_lt_u32 s91, 0x40000\n"
"s_cbranch_scc1 L_SP3%=\n"
"L_OK3%=:\n"
"v_exp_f32 v151, v151\n"
"v_mfma_f32_16x16x32_bf16 a[52:55], v[168:171], a[232:235], a[52:55]\n"
"ds_read_b128 a[200:203], v1 offset:9216\n"
"v_add_f32 v33, v33, v151\n"
"v_cvt_pk_bf16_f32 v203, v150, v151\n"
"v_mfma_f32_16x16x32_bf16 v[156:159], a[208:211], v[112:115], v[140:143]\n"
"v_exp_f32 v152, v152\n"
"v_mfma_f32_16x16x32_bf16 v[156:159], a[212:215], v[116:119], v[156:159]\n"
"ds_read_b128 a[204:207], v1 offset:9728\n"
"v_add_f32 v34, v34, v152\n"
"v_mfma_f32_16x16x32_bf16 v[156:159], a[216:219], v[120:123], v[156:159]\n"
"s_mov_b32 s31, 0x0\n"
"s_add_u32 m0, s63, 0x1e000\n"
"s_nop 0\n"
"buffer_load_dwordx4 v44, s[16:19], s31 offen lds\n"
"v_exp_f32 v153, v153\n"
"v_mfma_f32_16x16x32_bf16 v[156:159], a[220:223], v[124:127], v[156:159]\n"
"ds_read_b64_tr_b16 a[228:229], v2 offset:9216\n"
"v_add_f32 v34, v34, v153\n"
"v_cvt_pk_bf16_f32 v210, v152, v153\n"
"v_mfma_f32_16x16x32_bf16 a[56:59], v[176:179], a[232:235], a[56:59]\n"
"buffer_load_dwordx4 v45, s[16:19], s31 offen offset:1024 lds\n"
"v_exp_f32 v154, v154\n"
"v_mfma_f32_16x16x32_bf16 a[56:59], v[180:183], a[236:239], a[56:59]\n"
"ds_read_b64_tr_b16 a[230:231], v2 offset:13312\n"
"v_add_f32 v34, v34, v154\n"
"v_mfma_f32_16x16x32_bf16 a[60:63], v[188:191], a[236:239], a[60:63]\n"
"s_waitcnt lgkmcnt(2)\n"
"v_exp_f32 v155, v155\n"
"v_mfma_f32_16x16x32_bf16 a[60:63], v[184:187], a[232:235], a[60:63]\n"
"ds_read_b64_tr_b16 a[232:233], v3 offset:1024\n"
"v_add_f32 v34, v34, v155\n"
"v_cvt_pk_bf16_f32 v211, v154, v155\n"
"v_mfma_f32_16x16x32_bf16 v[144:147], a[192:195], v[64:67], v[128:131]\n"
"v_exp_f32 v156, v156\n"
"v_mfma_f32_16x16x32_bf16 v[144:147], a[196:199], v[68:71], v[144:147]\n"
"ds_read_b64_tr_b16 a[234:235], v3 offset:5120\n"
"v_add_f32 v35, v35, v156\n"
"v_mfma_f32_16x16x32_bf16 v[144:147], a[200:203], v[72:75], v[144:147]\n"
"s_waitcnt lgkmcnt(2)\n"
"buffer_load_dwordx4 v46, s[16:19], s31 offen offset:2048 lds\n"
"v_exp_f32 v157, v157\n"
"v_mfma_f32_16x16x32_bf16 v[144:147], a[204:207], v[76:79], v[144:147]\n"
"v_add_f32 v35, v35, v157\n"
"v_cvt_pk_bf16_f32 v218, v156, v157\n"
"v_mfma_f32_16x16x32_bf16 a[64:67], v[160:163], a[224:227], a[64:67]\n"
"buffer_load_dwordx4 v47, s[16:19], s31 offen offset:3072 lds\n"
"v_exp_f32 v158, v158\n"
"v_mfma_f32_16x16x32_bf16 a[64:67], v[164:167], a[228:231], a[64:67]\n"
"v_add_f32 v35, v35, v158\n"
"v_mfma_f32_16x16x32_bf16 a[68:71], v[172:175], a[228:231], a[68:71]\n"
"v_exp_f32 v159, v159\n"
"v_mfma_f32_16x16x32_bf16 a[68:71], v[168:171], a[224:227], a[68:71]\n"
"ds_read_b64_tr_b16 a[236:237], v3 offset:9216\n"
"v_add_f32 v35, v35, v159\n"
"v_cvt_pk_bf16_f32 v219, v158, v159\n"
"v_mfma_f32_16x16x32_bf16 v[148:151], a[192:195], v[80:83], v[132:135]\n"
"v_exp_f32 v144, v144\n"
"v_mfma_f32_16x16x32_bf16 v[148:151], a[196:199], v[84:87], v[148:151]\n"
"ds_read_b64_tr_b16 a[238:239], v3 offset:13312\n"
"v_add_f32 v32, v32, v144\n"
"v_mfma_f32_16x16x32_bf16 v[148:151], a[200:203], v[88:91], v[148:151]\n"
"s_mov_b32 s35, 0x0\n"
"buffer_load_dwordx4 a[128:131], v12, s[20:23], s35 offen sc1\n"
"v_exp_f32 v145, v145\n"
"v_mfma_f32_16x16x32_bf16 v[148:151], a[204:207], v[92:95], v[148:151]\n"
"v_add_f32 v32, v32, v145\n"
"v_cvt_pk_bf16_f32 v196, v144, v145\n"
"v_mfma_f32_16x16x32_bf16 a[72:75], v[176:179], a[224:227], a[72:75]\n"
"buffer_load_dwordx4 a[132:135], v12, s[20:23], s35 offen offset:1024 sc1\n"
"v_exp_f32 v146, v146\n"
"v_mfma_f32_16x16x32_bf16 a[72:75], v[180:183], a[228:231], a[72:75]\n"
"v_add_f32 v32, v32, v146\n"
"v_mfma_f32_16x16x32_bf16 a[76:79], v[188:191], a[228:231], a[76:79]\n"
"v_exp_f32 v147, v147\n"
"v_mfma_f32_16x16x32_bf16 a[76:79], v[184:187], a[224:227], a[76:79]\n"
"ds_read_b128 a[208:211], v1 offset:12288\n"
"v_add_f32 v32, v32, v147\n"
"v_cvt_pk_bf16_f32 v197, v146, v147\n"
"v_mfma_f32_16x16x32_bf16 a[248:251], v[28:31], v[176:179], a[248:251]\n"
"v_mfma_f32_16x16x32_bf16 a[248:251], v[28:31], v[180:183], a[248:251]\n"
"v_mfma_f32_16x16x32_bf16 v[152:155], a[192:195], v[96:99], v[136:139]\n"
"v_exp_f32 v148, v148\n"
"v_mfma_f32_16x16x32_bf16 v[152:155], a[196:199], v[100:103], v[152:155]\n"
"ds_read_b128 a[212:215], v1 offset:12800\n"
"v_add_f32 v33, v33, v148\n"
"v_mfma_f32_16x16x32_bf16 v[152:155], a[200:203], v[104:107], v[152:155]\n"
"s_waitcnt lgkmcnt(2)\n"
"buffer_load_dwordx4 a[136:139], v12, s[20:23], s35 offen offset:2048 sc1\n"
"v_exp_f32 v149, v149\n"
"v_mfma_f32_16x16x32_bf16 v[152:155], a[204:207], v[108:111], v[152:155]\n"
"ds_read_b64_tr_b16 a[224:225], v2 offset:1536\n"
"v_add_f32 v33, v33, v149\n"
"v_cvt_pk_bf16_f32 v204, v148, v149\n"
"v_mfma_f32_16x16x32_bf16 a[80:83], v[160:163], a[232:235], a[80:83]\n"
"buffer_load_dwordx4 a[140:143], v12, s[20:23], s35 offen offset:3072 sc1\n"
"v_exp_f32 v150, v150\n"
"v_mfma_f32_16x16x32_bf16 a[80:83], v[164:167], a[236:239], a[80:83]\n"
"ds_read_b64_tr_b16 a[226:227], v2 offset:5632\n"
"v_add_f32 v33, v33, v150\n"
"v_mfma_f32_16x16x32_bf16 a[84:87], v[172:175], a[236:239], a[84:87]\n"
"v_exp_f32 v151, v151\n"
"v_mfma_f32_16x16x32_bf16 a[84:87], v[168:171], a[232:235], a[84:87]\n"
"ds_read_b128 a[216:219], v1 offset:13312\n"
"v_add_f32 v33, v33, v151\n"
"v_cvt_pk_bf16_f32 v205, v150, v151\n"
"v_mfma_f32_16x16x32_bf16 v[156:159], a[192:195], v[112:115], v[140:143]\n"
"v_exp_f32 v152, v152\n"
"v_mfma_f32_16x16x32_bf16 v[156:159], a[196:199], v[116:119], v[156:159]\n"
"ds_read_b128 a[220:223], v1 offset:13824\n"
"v_add_f32 v34, v34, v152\n"
"v_mfma_f32_16x16x32_bf16 v[156:159], a[200:203], v[120:123], v[156:159]\n"
"buffer_load_dwordx4 a[144:147], v52, s[20:23], s35 offen sc1\n"
"v_exp_f32 v153, v153\n"
"v_mfma_f32_16x16x32_bf16 v[156:159], a[204:207], v[124:127], v[156:159]\n"
"ds_read_b64_tr_b16 a[228:229], v2 offset:9728\n"
"v_add_f32 v34, v34, v153\n"
"v_cvt_pk_bf16_f32 v212, v152, v153\n"
"v_mfma_f32_16x16x32_bf16 a[88:91], v[176:179], a[232:235], a[88:91]\n"
"s_mov_b32 s31, 0x20000\n"
"s_add_u32 m0, s63, 0x23000\n"
"s_nop 0\n"
"buffer_load_dwordx4 v44, s[16:19], s31 offen lds\n"
"v_exp_f32 v154, v154\n"
"v_mfma_f32_16x16x32_bf16 a[88:91], v[180:183], a[236:239], a[88:91]\n"
"ds_read_b64_tr_b16 a[230:231], v2 offset:13824\n"
"v_add_f32 v34, v34, v154\n"
"v_mfma_f32_16x16x32_bf16 a[92:95], v[188:191], a[236:239], a[92:95]\n"
"s_waitcnt lgkmcnt(2)\n"
"v_exp_f32 v155, v155\n"
"v_mfma_f32_16x16x32_bf16 a[92:95], v[184:187], a[232:235], a[92:95]\n"
"ds_read_b64_tr_b16 a[232:233], v3 offset:1536\n"
"v_add_f32 v34, v34, v155\n"
"v_cvt_pk_bf16_f32 v213, v154, v155\n"
"v_mfma_f32_16x16x32_bf16 v[144:147], a[208:211], v[64:67], v[128:131]\n"
"v_exp_f32 v156, v156\n"
"v_mfma_f32_16x16x32_bf16 v[144:147], a[212:215], v[68:71], v[144:147]\n"
"ds_read_b64_tr_b16 a[234:235], v3 offset:5632\n"
"v_add_f32 v35, v35, v156\n"
"v_add_u32 v2, s78, v11\n"
"v_mfma_f32_16x16x32_bf16 v[144:147], a[216:219], v[72:75], v[144:147]\n"
"s_waitcnt lgkmcnt(2)\n"
"buffer_load_dwordx4 v45, s[16:19], s31 offen offset:1024 lds\n"
"v_exp_f32 v157, v157\n"
"v_mfma_f32_16x16x32_bf16 v[144:147], a[220:223], v[76:79], v[144:147]\n"
"v_add_f32 v35, v35, v157\n"
"v_cvt_pk_bf16_f32 v220, v156, v157\n"
"v_mfma_f32_16x16x32_bf16 a[96:99], v[160:163], a[224:227], a[96:99]\n"
"buffer_load_dwordx4 v46, s[16:19], s31 offen offset:2048 lds\n"
"v_exp_f32 v158, v158\n"
"v_mfma_f32_16x16x32_bf16 a[96:99], v[164:167], a[228:231], a[96:99]\n"
"v_add_f32 v35, v35, v158\n"
"v_mfma_f32_16x16x32_bf16 a[100:103], v[172:175], a[228:231], a[100:103]\n"
"v_exp_f32 v159, v159\n"
"v_mfma_f32_16x16x32_bf16 a[100:103], v[168:171], a[224:227], a[100:103]\n"
"ds_read_b64_tr_b16 a[236:237], v3 offset:9728\n"
"v_add_f32 v35, v35, v159\n"
"v_cvt_pk_bf16_f32 v221, v158, v159\n"
"v_mfma_f32_16x16x32_bf16 v[148:151], a[208:211], v[80:83], v[132:135]\n"
"v_exp_f32 v144, v144\n"
"v_mfma_f32_16x16x32_bf16 v[148:151], a[212:215], v[84:87], v[148:151]\n"
"ds_read_b64_tr_b16 a[238:239], v3 offset:13824\n"
"v_add_f32 v32, v32, v144\n"
"v_mfma_f32_16x16x32_bf16 v[148:151], a[216:219], v[88:91], v[148:151]\n"
"s_mov_b32 s81, s78\n"
"buffer_load_dwordx4 v47, s[16:19], s31 offen offset:3072 lds\n"
"v_exp_f32 v145, v145\n"
"v_mfma_f32_16x16x32_bf16 v[148:151], a[220:223], v[92:95], v[148:151]\n"
"v_add_f32 v32, v32, v145\n"
"v_cvt_pk_bf16_f32 v198, v144, v145\n"
"v_mfma_f32_16x16x32_bf16 a[104:107], v[176:179], a[224:227], a[104:107]\n"
"s_mov_b32 s78, s79\n"
"s_mov_b32 s35, 0x1400\n"
"buffer_load_dwordx4 a[148:151], v12, s[20:23], s35 offen sc1\n"
"v_exp_f32 v146, v146\n"
"v_mfma_f32_16x16x32_bf16 a[104:107], v[180:183], a[228:231], a[104:107]\n"
"v_add_f32 v32, v32, v146\n"
"v_xor_b32 v3, 32, v2\n"
"v_mfma_f32_16x16x32_bf16 a[108:111], v[188:191], a[228:231], a[108:111]\n"
"s_waitcnt vmcnt(21) lgkmcnt(0)\n"
"s_barrier\n"
"v_exp_f32 v147, v147\n"
"v_mfma_f32_16x16x32_bf16 a[108:111], v[184:187], a[224:227], a[108:111]\n"
"ds_read_b64_tr_b16 a[224:225], v2 offset:0\n"
"v_add_f32 v32, v32, v147\n"
"v_cvt_pk_bf16_f32 v199, v146, v147\n"
"v_mfma_f32_16x16x32_bf16 a[252:255], v[28:31], v[184:187], a[252:255]\n"
"v_mfma_f32_16x16x32_bf16 a[252:255], v[28:31], v[188:191], a[252:255]\n"
"v_mfma_f32_16x16x32_bf16 v[152:155], a[208:211], v[96:99], v[136:139]\n"
"v_exp_f32 v148, v148\n"
"v_mfma_f32_16x16x32_bf16 v[152:155], a[212:215], v[100:103], v[152:155]\n"
"ds_read_b64_tr_b16 a[226:227], v2 offset:4096\n"
"v_add_f32 v33, v33, v148\n"
"v_mfma_f32_16x16x32_bf16 v[152:155], a[216:219], v[104:107], v[152:155]\n"
"s_waitcnt lgkmcnt(2)\n"
"buffer_load_dwordx4 a[152:155], v12, s[20:23], s35 offen offset:1024 sc1\n"
"v_exp_f32 v149, v149\n"
"v_mfma_f32_16x16x32_bf16 v[152:155], a[220:223], v[108:111], v[152:155]\n"
"v_add_f32 v33, v33, v149\n"
"v_cvt_pk_bf16_f32 v206, v148, v149\n"
"v_mfma_f32_16x16x32_bf16 a[112:115], v[160:163], a[232:235], a[112:115]\n"
"buffer_load_dwordx4 a[156:159], v12, s[20:23], s35 offen offset:2048 sc1\n"
"v_exp_f32 v150, v150\n"
"v_mfma_f32_16x16x32_bf16 a[112:115], v[164:167], a[236:239], a[112:115]\n"
"v_add_f32 v33, v33, v150\n"
"v_mfma_f32_16x16x32_bf16 a[116:119], v[172:175], a[236:239], a[116:119]\n"
"s_mov_b32 s79, s80\n"
"v_exp_f32 v151, v151\n"
"v_mfma_f32_16x16x32_bf16 a[116:119], v[168:171], a[232:235], a[116:119]\n"
"ds_read_b64_tr_b16 a[228:229], v2 offset:8192\n"
"v_add_f32 v33, v33, v151\n"
"v_cvt_pk_bf16_f32 v207, v150, v151\n"
"v_mfma_f32_16x16x32_bf16 v[156:159], a[208:211], v[112:115], v[140:143]\n"
"v_exp_f32 v152, v152\n"
"v_mfma_f32_16x16x32_bf16 v[156:159], a[212:215], v[116:119], v[156:159]\n"
"ds_read_b64_tr_b16 a[230:231], v2 offset:12288\n"
"v_add_f32 v34, v34, v152\n"
"v_mfma_f32_16x16x32_bf16 v[156:159], a[216:219], v[120:123], v[156:159]\n"
"s_mov_b32 s80, s81\n"
"buffer_load_dwordx4 a[160:163], v12, s[20:23], s35 offen offset:3072 sc1\n"
"v_exp_f32 v153, v153\n"
"v_mfma_f32_16x16x32_bf16 v[156:159], a[220:223], v[124:127], v[156:159]\n"
"v_add_f32 v34, v34, v153\n"
"v_cvt_pk_bf16_f32 v214, v152, v153\n"
"v_mfma_f32_16x16x32_bf16 a[120:123], v[176:179], a[232:235], a[120:123]\n"
"buffer_load_dwordx4 a[164:167], v52, s[20:23], s35 offen sc1\n"
"v_exp_f32 v154, v154\n"
"v_mfma_f32_16x16x32_bf16 a[120:123], v[180:183], a[236:239], a[120:123]\n"
"v_add_f32 v34, v34, v154\n"
"v_mfma_f32_16x16x32_bf16 a[124:127], v[188:191], a[236:239], a[124:127]\n"
"v_exp_f32 v155, v155\n"
"v_mfma_f32_16x16x32_bf16 a[124:127], v[184:187], a[232:235], a[124:127]\n"
"ds_read_b64_tr_b16 a[232:233], v3 offset:0\n"
"s_mov_b32 s31, 0x40000\n"
"s_add_u32 m0, s63, 0x14000\n"
"s_nop 0\n"
"buffer_load_dwordx4 v44, s[16:19], s31 offen lds\n"
"v_add_f32 v34, v34, v155\n"
"v_cvt_pk_bf16_f32 v215, v154, v155\n"
"buffer_load_dwordx4 v45, s[16:19], s31 offen offset:1024 lds\n"
"v_exp_f32 v156, v156\n"
"ds_read_b64_tr_b16 a[234:235], v3 offset:4096\n"
"v_add_f32 v35, v35, v156\n"
"s_waitcnt lgkmcnt(2)\n"
"v_exp_f32 v157, v157\n"
"s_nop 0\n"
"v_add_f32 v35, v35, v157\n"
"v_cvt_pk_bf16_f32 v222, v156, v157\n"
"v_mfma_f32_16x16x32_bf16 a[0:3], v[192:195], a[224:227], a[0:3]\n"
"v_exp_f32 v158, v158\n"
"v_mfma_f32_16x16x32_bf16 a[0:3], v[196:199], a[228:231], a[0:3]\n"
"v_add_f32 v35, v35, v158\n"
"v_mfma_f32_16x16x32_bf16 a[4:7], v[204:207], a[228:231], a[4:7]\n"
"v_exp_f32 v159, v159\n"
"v_mfma_f32_16x16x32_bf16 a[4:7], v[200:203], a[224:227], a[4:7]\n"
"ds_read_b64_tr_b16 a[236:237], v3 offset:8192\n"
"buffer_load_dwordx4 v46, s[16:19], s31 offen offset:2048 lds\n"
"v_add_f32 v35, v35, v159\n"
"v_cvt_pk_bf16_f32 v223, v158, v159\n"
"buffer_load_dwordx4 v47, s[16:19], s31 offen offset:3072 lds\n"
"ds_read_b64_tr_b16 a[238:239], v3 offset:12288\n"
"v_accvgpr_read_b32 v36, a240\n"
"v_accvgpr_read_b32 v37, a244\n"
"v_accvgpr_read_b32 v38, a248\n"
"v_accvgpr_read_b32 v39, a252\n"
"v_mfma_f32_16x16x32_bf16 a[8:11], v[208:211], a[224:227], a[8:11]\n"
"v_add_f32 v32, v32, v36\n"
"v_add_f32 v33, v33, v37\n"
"v_add_f32 v34, v34, v38\n"
"v_add_f32 v35, v35, v39\n"
"v_mfma_f32_16x16x32_bf16 a[8:11], v[212:215], a[228:231], a[8:11]\n"
"v_add_f32 v36, v32, v33\n"
"v_add_f32 v37, v34, v35\n"
"v_mov_b32 v40, 1.0\n"
"v_add_f32 v36, v36, v37\n"
"v_mfma_f32_16x16x32_bf16 a[12:15], v[220:223], a[228:231], a[12:15]\n"
"s_mov_b32 s60, 0x57800000\n"
"v_cmp_nge_f32 vcc, s60, v36\n"
"s_lshl_b32 s60, s90, 2\n"
"s_add_u32 s60, s60, 106496\n"
"v_mfma_f32_16x16x4_f32 v[176:179], v32, v40, 0\n"
"v_mfma_f32_16x16x4_f32 v[180:183], v33, v40, 0\n"
"s_cmp_lg_u64 vcc, 0\n"
"s_cselect_b32 s61, 1, 0\n"
"v_mov_b32 v36, s60\n"
"v_mov_b32 v37, s61\n"
"v_mov_b32 v38, 0x1a000\n"
"ds_write_b32 v36, v37\n"
"v_mfma_f32_16x16x4_f32 v[184:187], v34, v40, 0\n"
"v_mfma_f32_16x16x4_f32 v[188:191], v35, v40, 0\n"
"v_mfma_f32_16x16x32_bf16 a[12:15], v[216:219], a[224:227], a[12:15]\n"
"ds_read_b64_tr_b16 a[224:225], v2 offset:512\n"
"s_mov_b32 s35, 0x2800\n"
"buffer_load_dwordx4 a[168:171], v12, s[20:23], s35 offen sc1\n"
"ds_read_b64_tr_b16 a[226:227], v2 offset:4608\n"
"s_waitcnt lgkmcnt(3)\n"
"v_and_b32 v37, 15, v13\n"
"v_lshrrev_b32 v36, 4, v13\n"
"v_lshlrev_b32 v37, 6, v37\n"
"v_lshl_add_u32 v39, v36, 4, v37\n"
"v_mfma_f32_16x16x32_bf16 a[16:19], v[192:195], a[232:235], a[16:19]\n"
"s_waitcnt lgkmcnt(0)\n"
"s_barrier\n"
"ds_read_b128 v[40:43], v38\n"
"v_rcp_f32 v176, v176\n"
"v_rcp_f32 v177, v177\n"
"v_rcp_f32 v178, v178\n"
"v_rcp_f32 v179, v179\n"
"v_mfma_f32_16x16x32_bf16 a[16:19], v[196:199], a[236:239], a[16:19]\n"
"v_rcp_f32 v180, v180\n"
"v_rcp_f32 v181, v181\n"
"v_rcp_f32 v182, v182\n"
"v_rcp_f32 v183, v183\n"
"v_rcp_f32 v184, v184\n"
"v_rcp_f32 v185, v185\n"
"v_rcp_f32 v186, v186\n"
"v_rcp_f32 v187, v187\n"
"v_mfma_f32_16x16x32_bf16 a[20:23], v[204:207], a[236:239], a[20:23]\n"
"s_waitcnt lgkmcnt(0)\n"
"v_or3_b32 v40, v40, v41, v42\n"
"v_or_b32 v40, v40, v43\n"
"s_nop 0\n"
"v_readfirstlane_b32 s60, v40\n"
"v_rcp_f32 v188, v188\n"
"v_rcp_f32 v189, v189\n"
"v_rcp_f32 v190, v190\n"
"v_rcp_f32 v191, v191\n"
"s_andn2_b32 s60, s60, s30\n"
"s_cbranch_scc1 L_FB%=\n"
"v_mfma_f32_16x16x32_bf16 a[20:23], v[200:203], a[232:235], a[20:23]\n"
"ds_read_b64_tr_b16 a[228:229], v2 offset:8704\n"
"buffer_load_dwordx4 a[172:175], v12, s[20:23], s35 offen offset:1024 sc1\n"
"ds_read_b64_tr_b16 a[230:231], v2 offset:12800\n"
"v_accvgpr_read_b32 v128, a0\n"
"v_accvgpr_read_b32 v129, a1\n"
"v_accvgpr_read_b32 v130, a2\n"
"v_accvgpr_read_b32 v131, a3\n"
"v_accvgpr_read_b32 v132, a16\n"
"v_accvgpr_read_b32 v133, a17\n"
"v_mfma_f32_16x16x32_bf16 a[24:27], v[208:211], a[232:235], a[24:27]\n"
"v_accvgpr_read_b32 v134, a18\n"
"v_accvgpr_read_b32 v135, a19\n"
"v_mul_f32 v128, v128, v176\n"
"v_mul_f32 v129, v129, v177\n"
"v_mul_f32 v130, v130, v178\n"
"v_mul_f32 v131, v131, v179\n"
"v_mfma_f32_16x16x32_bf16 a[24:27], v[212:215], a[236:239], a[24:27]\n"
"v_mul_f32 v132, v132, v176\n"
"v_mul_f32 v133, v133, v177\n"
"v_mul_f32 v134, v134, v178\n"
"v_mul_f32 v135, v135, v179\n"
"v_cvt_pk_bf16_f32 v136, v128, v129\n"
"v_cvt_pk_bf16_f32 v137, v130, v131\n"
"v_mfma_f32_16x16x32_bf16 a[28:31], v[220:223], a[236:239], a[28:31]\n"
"v_cvt_pk_bf16_f32 v138, v132, v133\n"
"v_cvt_pk_bf16_f32 v139, v134, v135\n"
"s_mov_b32 s62, 0x0\n"
"buffer_store_dwordx4 v[136:139], v39, s[24:27], s62 offen offset:0 sc1\n"
"v_mfma_f32_16x16x32_bf16 a[28:31], v[216:219], a[232:235], a[28:31]\n"
"ds_read_b64_tr_b16 a[232:233], v3 offset:512\n"
"buffer_load_dwordx4 a[176:179], v12, s[20:23], s35 offen offset:2048 sc1\n"
"ds_read_b64_tr_b16 a[234:235], v3 offset:4608\n"
"s_waitcnt lgkmcnt(2)\n"
"v_accvgpr_read_b32 v140, a4\n"
"v_accvgpr_read_b32 v141, a5\n"
"v_accvgpr_read_b32 v142, a6\n"
"v_accvgpr_read_b32 v143, a7\n"
"v_accvgpr_read_b32 v144, a20\n"
"v_accvgpr_read_b32 v145, a21\n"
"v_mfma_f32_16x16x32_bf16 a[32:35], v[192:195], a[224:227], a[32:35]\n"
"v_accvgpr_read_b32 v146, a22\n"
"v_accvgpr_read_b32 v147, a23\n"
"v_mul_f32 v140, v140, v180\n"
"v_mul_f32 v141, v141, v181\n"
"v_mul_f32 v142, v142, v182\n"
"v_mul_f32 v143, v143, v183\n"
"v_mfma_f32_16x16x32_bf16 a[32:35], v[196:199], a[228:231], a[32:35]\n"
"v_mul_f32 v144, v144, v180\n"
"v_mul_f32 v145, v145, v181\n"
"v_mul_f32 v146, v146, v182\n"
"v_mul_f32 v147, v147, v183\n"
"v_cvt_pk_bf16_f32 v148, v140, v141\n"
"v_cvt_pk_bf16_f32 v149, v142, v143\n"
"v_mfma_f32_16x16x32_bf16 a[36:39], v[204:207], a[228:231], a[36:39]\n"
"v_cvt_pk_bf16_f32 v150, v144, v145\n"
"v_cvt_pk_bf16_f32 v151, v146, v147\n"
"s_mov_b32 s62, 0x0\n"
"buffer_store_dwordx4 v[148:151], v39, s[24:27], s62 offen offset:1024 sc1\n"
"v_mfma_f32_16x16x32_bf16 a[36:39], v[200:203], a[224:227], a[36:39]\n"
"ds_read_b64_tr_b16 a[236:237], v3 offset:8704\n"
"buffer_load_dwordx4 a[180:183], v12, s[20:23], s35 offen offset:3072 sc1\n"
"ds_read_b64_tr_b16 a[238:239], v3 offset:12800\n"
"v_accvgpr_read_b32 v152, a8\n"
"v_accvgpr_read_b32 v153, a9\n"
"v_accvgpr_read_b32 v154, a10\n"
"v_accvgpr_read_b32 v155, a11\n"
"v_accvgpr_read_b32 v156, a24\n"
"v_accvgpr_read_b32 v157, a25\n"
"v_mfma_f32_16x16x32_bf16 a[40:43], v[208:211], a[224:227], a[40:43]\n"
"v_accvgpr_read_b32 v158, a26\n"
"v_accvgpr_read_b32 v159, a27\n"
"v_mul_f32 v152, v152, v184\n"
"v_mul_f32 v153, v153, v185\n"
"v_mul_f32 v154, v154, v186\n"
"v_mul_f32 v155, v155, v187\n"
"v_mfma_f32_16x16x32_bf16 a[40:43], v[212:215], a[228:231], a[40:43]\n"
"v_mul_f32 v156, v156, v184\n"
"v_mul_f32 v157, v157, v185\n"
"v_mul_f32 v158, v158, v186\n"
"v_mul_f32 v159, v159, v187\n"
"v_cvt_pk_bf16_f32 v160, v152, v153\n"
"v_cvt_pk_bf16_f32 v161, v154, v155\n"
"v_mfma_f32_16x16x32_bf16 a[44:47], v[220:223], a[228:231], a[44:47]\n"
"v_cvt_pk_bf16_f32 v162, v156, v157\n"
"v_cvt_pk_bf16_f32 v163, v158, v159\n"
"s_mov_b32 s62, 0x0\n"
"buffer_store_dwordx4 v[160:163], v39, s[24:27], s62 offen offset:2048 sc1\n"
"v_mfma_f32_16x16x32_bf16 a[44:47], v[216:219], a[224:227], a[44:47]\n"
"ds_read_b64_tr_b16 a[224:225], v2 offset:1024\n"
"buffer_load_dwordx4 a[184:187], v52, s[20:23], s35 offen sc1\n"
"ds_read_b64_tr_b16 a[226:227], v2 offset:5120\n"
"s_waitcnt lgkmcnt(2)\n"
"v_accvgpr_read_b32 v164, a12\n"
"v_accvgpr_read_b32 v165, a13\n"
"v_accvgpr_read_b32 v166, a14\n"
"v_accvgpr_read_b32 v167, a15\n"
"v_accvgpr_read_b32 v168, a28\n"
"v_accvgpr_read_b32 v169, a29\n"
"v_mfma_f32_16x16x32_bf16 a[48:51], v[192:195], a[232:235], a[48:51]\n"
"v_accvgpr_read_b32 v170, a30\n"
"v_accvgpr_read_b32 v171, a31\n"
"v_mul_f32 v164, v164, v188\n"
"v_mul_f32 v165, v165, v189\n"
"v_mul_f32 v166, v166, v190\n"
"v_mul_f32 v167, v167, v191\n"
"v_mfma_f32_16x16x32_bf16 a[48:51], v[196:199], a[236:239], a[48:51]\n"
"v_mul_f32 v168, v168, v188\n"
"v_mul_f32 v169, v169, v189\n"
"v_mul_f32 v170, v170, v190\n"
"v_mul_f32 v171, v171, v191\n"
"v_cvt_pk_bf16_f32 v172, v164, v165\n"
"v_cvt_pk_bf16_f32 v173, v166, v167\n"
"v_mfma_f32_16x16x32_bf16 a[52:55], v[204:207], a[236:239], a[52:55]\n"
"v_cvt_pk_bf16_f32 v174, v168, v169\n"
"v_cvt_pk_bf16_f32 v175, v170, v171\n"
"s_mov_b32 s62, 0x0\n"
"buffer_store_dwordx4 v[172:175], v39, s[24:27], s62 offen offset:3072 sc1\n"
"v_mfma_f32_16x16x32_bf16 a[52:55], v[200:203], a[232:235], a[52:55]\n"
"ds_read_b64_tr_b16 a[228:229], v2 offset:9216\n"
"s_mov_b32 s31, 0x60000\n"
"s_add_u32 m0, s63, 0x19000\n"
"s_nop 0\n"
"buffer_load_dwordx4 v44, s[16:19], s31 offen lds\n"
"ds_read_b64_tr_b16 a[230:231], v2 offset:13312\n"
"v_accvgpr_read_b32 v128, a32\n"
"v_accvgpr_read_b32 v129, a33\n"
"v_accvgpr_read_b32 v130, a34\n"
"v_accvgpr_read_b32 v131, a35\n"
"v_accvgpr_read_b32 v132, a48\n"
"v_accvgpr_read_b32 v133, a49\n"
"v_mfma_f32_16x16x32_bf16 a[56:59], v[208:211], a[232:235], a[56:59]\n"
"v_accvgpr_read_b32 v134, a50\n"
"v_accvgpr_read_b32 v135, a51\n"
"v_mul_f32 v128, v128, v176\n"
"v_mul_f32 v129, v129, v177\n"
"v_mul_f32 v130, v130, v178\n"
"v_mul_f32 v131, v131, v179\n"
"v_mfma_f32_16x16x32_bf16 a[56:59], v[212:215], a[236:239], a[56:59]\n"
"v_mul_f32 v132, v132, v176\n"
"v_mul_f32 v133, v133, v177\n"
"v_mul_f32 v134, v134, v178\n"
"v_mul_f32 v135, v135, v179\n"
"v_cvt_pk_bf16_f32 v136, v128, v129\n"
"v_cvt_pk_bf16_f32 v137, v130, v131\n"
"v_mfma_f32_16x16x32_bf16 a[60:63], v[220:223], a[236:239], a[60:63]\n"
"v_cvt_pk_bf16_f32 v138, v132, v133\n"
"v_cvt_pk_bf16_f32 v139, v134, v135\n"
"s_mov_b32 s62, 0x1000\n"
"buffer_store_dwordx4 v[136:139], v39, s[24:27], s62 offen offset:0 sc1\n"
"v_mfma_f32_16x16x32_bf16 a[60:63], v[216:219], a[232:235], a[60:63]\n"
"ds_read_b64_tr_b16 a[232:233], v3 offset:1024\n"
"buffer_load_dwordx4 v45, s[16:19], s31 offen offset:1024 lds\n"
"ds_read_b64_tr_b16 a[234:235], v3 offset:5120\n"
"s_waitcnt lgkmcnt(2)\n"
"v_accvgpr_read_b32 v140, a36\n"
"v_accvgpr_read_b32 v141, a37\n"
"v_accvgpr_read_b32 v142, a38\n"
"v_accvgpr_read_b32 v143, a39\n"
"v_accvgpr_read_b32 v144, a52\n"
"v_accvgpr_read_b32 v145, a53\n"
"v_mfma_f32_16x16x32_bf16 a[64:67], v[192:195], a[224:227], a[64:67]\n"
"v_accvgpr_read_b32 v146, a54\n"
"v_accvgpr_read_b32 v147, a55\n"
"v_mul_f32 v140, v140, v180\n"
"v_mul_f32 v141, v141, v181\n"
"v_mul_f32 v142, v142, v182\n"
"v_mul_f32 v143, v143, v183\n"
"v_mfma_f32_16x16x32_bf16 a[64:67], v[196:199], a[228:231], a[64:67]\n"
"v_mul_f32 v144, v144, v180\n"
"v_mul_f32 v145, v145, v181\n"
"v_mul_f32 v146, v146, v182\n"
"v_mul_f32 v147, v147, v183\n"
"v_cvt_pk_bf16_f32 v148, v140, v141\n"
"v_cvt_pk_bf16_f32 v149, v142, v143\n"
"v_mfma_f32_16x16x32_bf16 a[68:71], v[204:207], a[228:231], a[68:71]\n"
"v_cvt_pk_bf16_f32 v150, v144, v145\n"
"v_cvt_pk_bf16_f32 v151, v146, v147\n"
"s_mov_b32 s62, 0x1000\n"
"buffer_store_dwordx4 v[148:151], v39, s[24:27], s62 offen offset:1024 sc1\n"
"v_mfma_f32_16x16x32_bf16 a[68:71], v[200:203], a[224:227], a[68:71]\n"
"ds_read_b64_tr_b16 a[236:237], v3 offset:9216\n"
"buffer_load_dwordx4 v46, s[16:19], s31 offen offset:2048 lds\n"
"ds_read_b64_tr_b16 a[238:239], v3 offset:13312\n"
"v_accvgpr_read_b32 v152, a40\n"
"v_accvgpr_read_b32 v153, a41\n"
"v_accvgpr_read_b32 v154, a42\n"
"v_accvgpr_read_b32 v155, a43\n"
"v_accvgpr_read_b32 v156, a56\n"
"v_accvgpr_read_b32 v157, a57\n"
"v_mfma_f32_16x16x32_bf16 a[72:75], v[208:211], a[224:227], a[72:75]\n"
"v_accvgpr_read_b32 v158, a58\n"
"v_accvgpr_read_b32 v159, a59\n"
"v_mul_f32 v152, v152, v184\n"
"v_mul_f32 v153, v153, v185\n"
"v_mul_f32 v154, v154, v186\n"
"v_mul_f32 v155, v155, v187\n"
"v_mfma_f32_16x16x32_bf16 a[72:75], v[212:215], a[228:231], a[72:75]\n"
"v_mul_f32 v156, v156, v184\n"
"v_mul_f32 v157, v157, v185\n"
"v_mul_f32 v158, v158, v186\n"
"v_mul_f32 v159, v159, v187\n"
"v_cvt_pk_bf16_f32 v160, v152, v153\n"
"v_cvt_pk_bf16_f32 v161, v154, v155\n"
"v_mfma_f32_16x16x32_bf16 a[76:79], v[220:223], a[228:231], a[76:79]\n"
"v_cvt_pk_bf16_f32 v162, v156, v157\n"
"v_cvt_pk_bf16_f32 v163, v158, v159\n"
"s_mov_b32 s62, 0x1000\n"
"buffer_store_dwordx4 v[160:163], v39, s[24:27], s62 offen offset:2048 sc1\n"
"v_mfma_f32_16x16x32_bf16 a[76:79], v[216:219], a[224:227], a[76:79]\n"
"ds_read_b64_tr_b16 a[224:225], v2 offset:1536\n"
"buffer_load_dwordx4 v47, s[16:19], s31 offen offset:3072 lds\n"
"ds_read_b64_tr_b16 a[226:227], v2 offset:5632\n"
"s_waitcnt lgkmcnt(2)\n"
"v_accvgpr_read_b32 v164, a44\n"
"v_accvgpr_read_b32 v165, a45\n"
"v_accvgpr_read_b32 v166, a46\n"
"v_accvgpr_read_b32 v167, a47\n"
"v_accvgpr_read_b32 v168, a60\n"
"v_accvgpr_read_b32 v169, a61\n"
"v_mfma_f32_16x16x32_bf16 a[80:83], v[192:195], a[232:235], a[80:83]\n"
"v_accvgpr_read_b32 v170, a62\n"
"v_accvgpr_read_b32 v171, a63\n"
"v_mul_f32 v164, v164, v188\n"
"v_mul_f32 v165, v165, v189\n"
"v_mul_f32 v166, v166, v190\n"
"v_mul_f32 v167, v167, v191\n"
"v_mfma_f32_16x16x32_bf16 a[80:83], v[196:199], a[236:239], a[80:83]\n"
"v_mul_f32 v168, v168, v188\n"
"v_mul_f32 v169, v169, v189\n"
"v_mul_f32 v170, v170, v190\n"
"v_mul_f32 v171, v171, v191\n"
"v_cvt_pk_bf16_f32 v172, v164, v165\n"
"v_cvt_pk_bf16_f32 v173, v166, v167\n"
"v_mfma_f32_16x16x32_bf16 a[84:87], v[204:207], a[236:239], a[84:87]\n"
"v_cvt_pk_bf16_f32 v174, v168, v169\n"
"v_cvt_pk_bf16_f32 v175, v170, v171\n"
"s_mov_b32 s62, 0x1000\n"
"buffer_store_dwordx4 v[172:175], v39, s[24:27], s62 offen offset:3072 sc1\n"
"v_mfma_f32_16x16x32_bf16 a[84:87], v[200:203], a[232:235], a[84:87]\n"
"ds_read_b64_tr_b16 a[228:229], v2 offset:9728\n"
"s_mov_b32 s35, 0x3c00\n"
"buffer_load_dwordx4 a[188:191], v12, s[20:23], s35 offen sc1\n"
"ds_read_b64_tr_b16 a[230:231], v2 offset:13824\n"
"v_accvgpr_read_b32 v128, a64\n"
"v_accvgpr_read_b32 v129, a65\n"
"v_accvgpr_read_b32 v130, a66\n"
"v_accvgpr_read_b32 v131, a67\n"
"v_accvgpr_read_b32 v132, a80\n"
"v_accvgpr_read_b32 v133, a81\n"
"v_mfma_f32_16x16x32_bf16 a[88:91], v[208:211], a[232:235], a[88:91]\n"
"v_accvgpr_read_b32 v134, a82\n"
"v_accvgpr_read_b32 v135, a83\n"
"v_mul_f32 v128, v128, v176\n"
"v_mul_f32 v129, v129, v177\n"
"v_mul_f32 v130, v130, v178\n"
"v_mul_f32 v131, v131, v179\n"
"v_mfma_f32_16x16x32_bf16 a[88:91], v[212:215], a[236:239], a[88:91]\n"
"v_mul_f32 v132, v132, v176\n"
"v_mul_f32 v133, v133, v177\n"
"v_mul_f32 v134, v134, v178\n"
"v_mul_f32 v135, v135, v179\n"
"v_cvt_pk_bf16_f32 v136, v128, v129\n"
"v_cvt_pk_bf16_f32 v137, v130, v131\n"
"v_mfma_f32_16x16x32_bf16 a[92:95], v[220:223], a[236:239], a[92:95]\n"
"v_cvt_pk_bf16_f32 v138, v132, v133\n"
"v_cvt_pk_bf16_f32 v139, v134, v135\n"
"s_mov_b32 s62, 0x2000\n"
"buffer_store_dwordx4 v[136:139], v39, s[24:27], s62 offen offset:0 sc1\n"
"v_mfma_f32_16x16x32_bf16 a[92:95], v[216:219], a[232:235], a[92:95]\n"
"ds_read_b64_tr_b16 a[232:233], v3 offset:1536\n"
"buffer_load_dwordx4 a[192:195], v12, s[20:23], s35 offen offset:1024 sc1\n"
"ds_read_b64_tr_b16 a[234:235], v3 offset:5632\n"
"s_waitcnt lgkmcnt(2)\n"
"v_accvgpr_read_b32 v140, a68\n"
"v_accvgpr_read_b32 v141, a69\n"
"v_accvgpr_read_b32 v142, a70\n"
"v_accvgpr_read_b32 v143, a71\n"
"v_accvgpr_read_b32 v144, a84\n"
"v_accvgpr_read_b32 v145, a85\n"
"v_mfma_f32_16x16x32_bf16 a[96:99], v[192:195], a[224:227], a[96:99]\n"
"v_accvgpr_read_b32 v146, a86\n"
"v_accvgpr_read_b32 v147, a87\n"
"v_mul_f32 v140, v140, v180\n"
"v_mul_f32 v141, v141, v181\n"
"v_mul_f32 v142, v142, v182\n"
"v_mul_f32 v143, v143, v183\n"
"v_mfma_f32_16x16x32_bf16 a[96:99], v[196:199], a[228:231], a[96:99]\n"
"v_mul_f32 v144, v144, v180\n"
"v_mul_f32 v145, v145, v181\n"
"v_mul_f32 v146, v146, v182\n"
"v_mul_f32 v147, v147, v183\n"
"v_cvt_pk_bf16_f32 v148, v140, v141\n"
"v_cvt_pk_bf16_f32 v149, v142, v143\n"
"v_mfma_f32_16x16x32_bf16 a[100:103], v[204:207], a[228:231], a[100:103]\n"
"v_cvt_pk_bf16_f32 v150, v144, v145\n"
"v_cvt_pk_bf16_f32 v151, v146, v147\n"
"s_mov_b32 s62, 0x2000\n"
"buffer_store_dwordx4 v[148:151], v39, s[24:27], s62 offen offset:1024 sc1\n"
"v_mfma_f32_16x16x32_bf16 a[100:103], v[200:203], a[224:227], a[100:103]\n"
"ds_read_b64_tr_b16 a[236:237], v3 offset:9728\n"
"buffer_load_dwordx4 a[196:199], v12, s[20:23], s35 offen offset:2048 sc1\n"
"ds_read_b64_tr_b16 a[238:239], v3 offset:13824\n"
"v_accvgpr_read_b32 v152, a72\n"
"v_accvgpr_read_b32 v153, a73\n"
"v_accvgpr_read_b32 v154, a74\n"
"v_accvgpr_read_b32 v155, a75\n"
"v_accvgpr_read_b32 v156, a88\n"
"v_accvgpr_read_b32 v157, a89\n"
"v_mfma_f32_16x16x32_bf16 a[104:107], v[208:211], a[224:227], a[104:107]\n"
"v_accvgpr_read_b32 v158, a90\n"
"v_accvgpr_read_b32 v159, a91\n"
"v_mul_f32 v152, v152, v184\n"
"v_mul_f32 v153, v153, v185\n"
"v_mul_f32 v154, v154, v186\n"
"v_mul_f32 v155, v155, v187\n"
"v_mfma_f32_16x16x32_bf16 a[104:107], v[212:215], a[228:231], a[104:107]\n"
"v_mul_f32 v156, v156, v184\n"
"v_mul_f32 v157, v157, v185\n"
"v_mul_f32 v158, v158, v186\n"
"v_mul_f32 v159, v159, v187\n"
"v_cvt_pk_bf16_f32 v160, v152, v153\n"
"v_cvt_pk_bf16_f32 v161, v154, v155\n"
"v_mfma_f32_16x16x32_bf16 a[108:111], v[220:223], a[228:231], a[108:111]\n"
"v_cvt_pk_bf16_f32 v162, v156, v157\n"
"v_cvt_pk_bf16_f32 v163, v158, v159\n"
"s_mov_b32 s62, 0x2000\n"
"buffer_store_dwordx4 v[160:163], v39, s[24:27], s62 offen offset:2048 sc1\n"
"v_mfma_f32_16x16x32_bf16 a[108:111], v[216:219], a[224:227], a[108:111]\n"
"buffer_load_dwordx4 a[200:203], v12, s[20:23], s35 offen offset:3072 sc1\n"
"s_waitcnt lgkmcnt(0)\n"
"v_accvgpr_read_b32 v164, a76\n"
"v_accvgpr_read_b32 v165, a77\n"
"v_accvgpr_read_b32 v166, a78\n"
"v_accvgpr_read_b32 v167, a79\n"
"v_accvgpr_read_b32 v168, a92\n"
"v_accvgpr_read_b32 v169, a93\n"
"v_mfma_f32_16x16x32_bf16 a[112:115], v[192:195], a[232:235], a[112:115]\n"
"v_accvgpr_read_b32 v170, a94\n"
"v_accvgpr_read_b32 v171, a95\n"
"v_mul_f32 v164, v164, v188\n"
"v_mul_f32 v165, v165, v189\n"
"v_mul_f32 v166, v166, v190\n"
"v_mul_f32 v167, v167, v191\n"
"v_mfma_f32_16x16x32_bf16 a[112:115], v[196:199], a[236:239], a[112:115]\n"
"v_mul_f32 v168, v168, v188\n"
"v_mul_f32 v169, v169, v189\n"
"v_mul_f32 v170, v170, v190\n"
"v_mul_f32 v171, v171, v191\n"
"v_cvt_pk_bf16_f32 v172, v164, v165\n"
"v_cvt_pk_bf16_f32 v173, v166, v167\n"
"v_mfma_f32_16x16x32_bf16 a[116:119], v[204:207], a[236:239], a[116:119]\n"
"v_cvt_pk_bf16_f32 v174, v168, v169\n"
"v_cvt_pk_bf16_f32 v175, v170, v171\n"
"s_mov_b32 s62, 0x2000\n"
"buffer_store_dwordx4 v[172:175], v39, s[24:27], s62 offen offset:3072 sc1\n"
"v_mfma_f32_16x16x32_bf16 a[116:119], v[200:203], a[232:235], a[116:119]\n"
"buffer_load_dwordx4 a[204:207], v52, s[20:23], s35 offen sc1\n"
"v_accvgpr_read_b32 v128, a96\n"
"v_accvgpr_read_b32 v129, a97\n"
"v_accvgpr_read_b32 v130, a98\n"
"v_accvgpr_read_b32 v131, a99\n"
"v_accvgpr_read_b32 v132, a112\n"
"v_accvgpr_read_b32 v133, a113\n"
"v_mfma_f32_16x16x32_bf16 a[120:123], v[208:211], a[232:235], a[120:123]\n"
"v_accvgpr_read_b32 v134, a114\n"
"v_accvgpr_read_b32 v135, a115\n"
"v_mul_f32 v128, v128, v176\n"
"v_mul_f32 v129, v129, v177\n"
"v_mul_f32 v130, v130, v178\n"
"v_mul_f32 v131, v131, v179\n"
"v_mfma_f32_16x16x32_bf16 a[120:123], v[212:215], a[236:239], a[120:123]\n"
"v_mul_f32 v132, v132, v176\n"
"v_mul_f32 v133, v133, v177\n"
"v_mul_f32 v134, v134, v178\n"
"v_mul_f32 v135, v135, v179\n"
"v_cvt_pk_bf16_f32 v136, v128, v129\n"
"v_cvt_pk_bf16_f32 v137, v130, v131\n"
"v_mfma_f32_16x16x32_bf16 a[124:127], v[220:223], a[236:239], a[124:127]\n"
"v_cvt_pk_bf16_f32 v138, v132, v133\n"
"v_cvt_pk_bf16_f32 v139, v134, v135\n"
"s_mov_b32 s62, 0x3000\n"
"buffer_store_dwordx4 v[136:139], v39, s[24:27], s62 offen offset:0 sc1\n"
"v_mfma_f32_16x16x32_bf16 a[124:127], v[216:219], a[232:235], a[124:127]\n"
"v_accvgpr_read_b32 v140, a100\n"
"v_accvgpr_read_b32 v141, a101\n"
"v_accvgpr_read_b32 v142, a102\n"
"v_accvgpr_read_b32 v143, a103\n"
"v_accvgpr_read_b32 v144, a116\n"
"v_accvgpr_read_b32 v145, a117\n"
"v_accvgpr_read_b32 v146, a118\n"
"v_accvgpr_read_b32 v147, a119\n"
"v_pk_mul_f32 v[140:141], v[140:141], v[180:181]\n"
"v_pk_mul_f32 v[142:143], v[142:143], v[182:183]\n"
"v_pk_mul_f32 v[144:145], v[144:145], v[180:181]\n"
"v_pk_mul_f32 v[146:147], v[146:147], v[182:183]\n"
"v_cvt_pk_bf16_f32 v148, v140, v141\n"
"v_cvt_pk_bf16_f32 v149, v142, v143\n"
"v_cvt_pk_bf16_f32 v150, v144, v145\n"
"v_cvt_pk_bf16_f32 v151, v146, v147\n"
"s_mov_b32 s62, 0x3000\n"
"buffer_store_dwordx4 v[148:151], v39, s[24:27], s62 offen offset:1024 sc1\n"
"v_accvgpr_read_b32 v152, a104\n"
"v_accvgpr_read_b32 v153, a105\n"
"v_accvgpr_read_b32 v154, a106\n"
"v_accvgpr_read_b32 v155, a107\n"
"v_accvgpr_read_b32 v156, a120\n"
"v_accvgpr_read_b32 v157, a121\n"
"v_accvgpr_read_b32 v158, a122\n"
"v_accvgpr_read_b32 v159, a123\n"
"v_pk_mul_f32 v[152:153], v[152:153], v[184:185]\n"
"v_pk_mul_f32 v[154:155], v[154:155], v[186:187]\n"
"v_pk_mul_f32 v[156:157], v[156:157], v[184:185]\n"
"v_pk_mul_f32 v[158:159], v[158:159], v[186:187]\n"
"v_cvt_pk_bf16_f32 v160, v152, v153\n"
"v_cvt_pk_bf16_f32 v161, v154, v155\n"
"v_cvt_pk_bf16_f32 v162, v156, v157\n"
"v_cvt_pk_bf16_f32 v163, v158, v159\n"
"s_mov_b32 s62, 0x3000\n"
"buffer_store_dwordx4 v[160:163], v39, s[24:27], s62 offen offset:2048 sc1\n"
"v_accvgpr_read_b32 v164, a108\n"
"v_accvgpr_read_b32 v165, a109\n"
"v_accvgpr_read_b32 v166, a110\n"
"v_accvgpr_read_b32 v167, a111\n"
"v_accvgpr_read_b32 v168, a124\n"
"v_accvgpr_read_b32 v169, a125\n"
"v_accvgpr_read_b32 v170, a126\n"
"v_accvgpr_read_b32 v171, a127\n"
"v_pk_mul_f32 v[164:165], v[164:165], v[188:189]\n"
"v_pk_mul_f32 v[166:167], v[166:167], v[190:191]\n"
"v_pk_mul_f32 v[168:169], v[168:169], v[188:189]\n"
"v_pk_mul_f32 v[170:171], v[170:171], v[190:191]\n"
"v_cvt_pk_bf16_f32 v172, v164, v165\n"
"v_cvt_pk_bf16_f32 v173, v166, v167\n"
"v_cvt_pk_bf16_f32 v174, v168, v169\n"
"v_cvt_pk_bf16_f32 v175, v170, v171\n"
"s_mov_b32 s62, 0x3000\n"
"buffer_store_dwordx4 v[172:175], v39, s[24:27], s62 offen offset:3072 sc1\n"
"s_waitcnt lgkmcnt(0)\n"
"s_barrier\n"
"s_waitcnt vmcnt(34)\n"
"v_add_u32 v208, 81920, v48\n"
"v_add_u32 v209, 81920, v49\n"
"v_add_u32 v210, 81920, v50\n"
"v_add_u32 v211, 81920, v51\n"
"v_mov_b32 v92, v224\n"
"v_mov_b32 v93, v225\n"
"v_mov_b32 v94, v226\n"
"v_mov_b32 v95, v227\n"
"v_mov_b32 v96, v228\n"
"v_mov_b32 v97, v229\n"
"v_mov_b32 v98, v230\n"
"v_mov_b32 v99, v231\n"
"v_mov_b32 v100, v232\n"
"v_mov_b32 v101, v233\n"
"v_mov_b32 v89, v234\n"
"v_mov_b32 v90, v235\n"
"v_permlane16_swap_b32 v92, v94\n"
"v_permlane16_swap_b32 v93, v95\n"
"v_permlane16_swap_b32 v96, v98\n"
"v_permlane16_swap_b32 v97, v99\n"
"v_permlane32_swap_b32 v92, v94\n"
"v_permlane32_swap_b32 v93, v95\n"
"v_permlane32_swap_b32 v96, v98\n"
"v_permlane32_swap_b32 v97, v99\n"
"s_lshl_b32 s60, s34, 2\n"
"s_add_u32 s60, s60, s90\n"
"s_lshl_b32 s60, s60, 11\n"
"s_lshl_b32 s61, s33, 6\n"
"s_add_u32 s60, s60, s61\n"
"s_add_u32 s28, s60, 0x10000\n"
"ds_read_b64_tr_b16 v[16:17], v208 offset:40960\n"
"ds_read_b64_tr_b16 v[18:19], v208 offset:41472\n"
"ds_read_b64_tr_b16 v[20:21], v208 offset:61440\n"
"ds_read_b64_tr_b16 v[22:23], v208 offset:61952\n"
"s_mov_b32 s31, 0x80000\n"
"s_add_u32 m0, s63, 0xa000\n"
"s_nop 0\n"
"buffer_load_dwordx4 v44, s[16:19], s31 offen lds\n"
"buffer_load_dwordx4 v45, s[16:19], s31 offen offset:1024 lds\n"
"s_waitcnt lgkmcnt(0)\n"
"v_mfma_f32_16x16x32_bf16 v[128:131], a[128:131], v[16:19], 0\n"
"ds_read_b64_tr_b16 v[24:25], v209 offset:40960\n"
"s_and_b32 s58, %1, 7\n"
"s_lshr_b32 s58, s58, 1\n"
"s_lshr_b32 s59, s90, 1\n"
"v_mfma_f32_16x16x32_bf16 v[128:131], a[148:151], v[20:23], v[128:131]\n"
"ds_read_b64_tr_b16 v[26:27], v209 offset:41472\n"
"s_and_b32 s62, s90, 1\n"
"s_lshl_b32 s82, s90, 12\n"
"s_mul_i32 s60, s62, 10240\n"
"v_mfma_f32_16x16x32_bf16 v[132:135], a[152:155], v[20:23], 0\n"
"ds_read_b64_tr_b16 v[28:29], v209 offset:61440\n"
"s_add_u32 s60, s60, 16384\n"
"v_add_u32 v66, s60, v12\n"
"v_add_u32 v67, 81920, v66\n"
"v_mfma_f32_16x16x32_bf16 v[132:135], a[132:135], v[16:19], v[132:135]\n"
"ds_read_b64_tr_b16 v[30:31], v209 offset:61952\n"
"s_mul_i32 s61, s59, 5120\n"
"s_add_u32 s83, s60, s61\n"
"s_lshl_b32 s60, s59, 13\n"
"v_mfma_f32_16x16x32_bf16 v[136:139], a[136:139], v[16:19], 0\n"
"buffer_load_dwordx4 v46, s[16:19], s31 offen offset:2048 lds\n"
"v_bfe_u32 v224, v13, 4, 1\n"
"v_lshl_add_u32 v224, v224, 3, v12\n"
"v_add_u32 v64, s60, v224\n"
"v_mfma_f32_16x16x32_bf16 v[136:139], a[156:159], v[20:23], v[136:139]\n"
"buffer_load_dwordx4 v47, s[16:19], s31 offen offset:3072 lds\n"
"v_xor_b32 v65, 8, v64\n"
"v_add_u32 v78, 81920, v64\n"
"v_add_u32 v79, 81920, v65\n"
"v_mfma_f32_16x16x32_bf16 v[140:143], a[160:163], v[20:23], 0\n"
"s_mov_b32 s35, 0x5000\n"
"buffer_load_dwordx4 a[208:211], v12, s[20:23], s35 offen sc1\n"
"s_and_b32 s60, s98, 0xfffe\n"
"s_add_u32 s60, s60, s59\n"
"s_lshl_b32 s61, s60, 11\n"
"v_mfma_f32_16x16x32_bf16 v[140:143], a[140:143], v[16:19], v[140:143]\n"
"buffer_load_dwordx4 a[212:215], v12, s[20:23], s35 offen offset:1024 sc1\n"
"s_lshl_b32 s70, s58, 9\n"
"s_add_u32 s61, s61, s70\n"
"s_add_u32 s61, s61, 0x10000\n"
"v_mfma_f32_16x16x32_bf16 v[144:147], a[144:147], v[16:19], 0\n"
"buffer_load_dwordx4 a[216:219], v12, s[20:23], s35 offen offset:2048 sc1\n"
"v_and_b32 v224, 7, v13\n"
"v_lshlrev_b32 v224, 6, v224\n"
"v_add_u32 v102, s61, v224\n"
"v_mfma_f32_16x16x32_bf16 v[144:147], a[164:167], v[20:23], v[144:147]\n"
"s_waitcnt lgkmcnt(0)\n"
"s_lshl_b32 s60, s60, 19\n"
"s_lshl_b32 s61, s58, 5\n"
"s_add_u32 s61, s61, s62\n"
"v_mfma_f32_16x16x32_bf16 v[148:151], a[128:131], v[24:27], 0\n"
"ds_read_b64_tr_b16 v[16:17], v210 offset:40960\n"
"s_lshl_b32 s61, s61, 12\n"
"s_add_u32 s60, s60, s61\n"
"s_add_u32 s60, s60, 0x800000\n"
"v_mfma_f32_16x16x32_bf16 v[148:151], a[148:151], v[28:31], v[148:151]\n"
"ds_read_b64_tr_b16 v[18:19], v210 offset:41472\n"
"s_add_u32 s12, s56, s60\n"
"s_addc_u32 s13, s57, 0\n"
"s_mov_b32 s14, 0x1f000\n"
"s_mov_b32 s15, 0x20000\n"
"v_mfma_f32_16x16x32_bf16 v[152:155], a[152:155], v[28:31], 0\n"
"ds_read_b64_tr_b16 v[20:21], v210 offset:61440\n"
"s_and_b32 s60, s99, 0xfffe\n"
"s_add_u32 s60, s60, s62\n"
"s_lshl_b32 s61, s60, 10\n"
"v_mfma_f32_16x16x32_bf16 v[152:155], a[132:135], v[24:27], v[152:155]\n"
"ds_read_b64_tr_b16 v[22:23], v210 offset:61952\n"
"s_lshl_b32 s70, s58, 8\n"
"s_add_u32 s61, s61, s70\n"
"s_add_u32 s61, s61, 0x8000\n"
"v_mfma_f32_16x16x32_bf16 v[156:159], a[136:139], v[24:27], 0\n"
"buffer_load_dwordx4 a[220:223], v12, s[20:23], s35 offen offset:3072 sc1\n"
"v_lshlrev_b32 v224, 3, v13\n"
"v_add_u32 v224, s61, v224\n"
"v_cmp_gt_u32 vcc, 32, v13\n"
"v_mfma_f32_16x16x32_bf16 v[156:159], a[156:159], v[28:31], v[156:159]\n"
"s_waitcnt vmcnt(8)\n"
"v_lshrrev_b32 v54, 1, v12\n"
"s_mov_b64 exec, 0xff\n"
"buffer_store_dwordx2 v[56:57], v54, s[92:95], s28 offen sc1\n"
"s_mov_b64 exec, -1\n"
"s_nop 1\n"
"v_cndmask_b32 v102, v102, v224, vcc\n"
"s_mul_i32 s60, s60, 655360\n"
"v_mfma_f32_16x16x32_bf16 v[160:163], a[160:163], v[28:31], 0\n"
"buffer_load_dwordx4 a[224:227], v52, s[20:23], s35 offen sc1\n"
"s_lshl_b32 s61, s58, 5\n"
"s_add_u32 s61, s61, s59\n"
"s_mul_i32 s61, s61, 5120\n"
"v_mfma_f32_16x16x32_bf16 v[160:163], a[140:143], v[24:27], v[160:163]\n"
"s_mov_b32 s31, 0xa0000\n"
"s_add_u32 m0, s63, 0xf000\n"
"s_nop 0\n"
"buffer_load_dwordx4 v44, s[16:19], s31 offen lds\n"
"s_add_u32 s60, s60, s61\n"
"s_add_u32 s60, s60, 0x1900000\n"
"s_add_u32 s8, s56, s60\n"
"s_addc_u32 s9, s57, 0\n"
"v_mfma_f32_16x16x32_bf16 v[164:167], a[144:147], v[24:27], 0\n"
"buffer_load_dwordx4 v45, s[16:19], s31 offen offset:1024 lds\n"
"s_mov_b32 s10, 0x26c00\n"
"s_mov_b32 s11, 0x20000\n"
"s_lshr_b32 s60, s98, 1\n"
"v_mfma_f32_16x16x32_bf16 v[164:167], a[164:167], v[28:31], v[164:167]\n"
"s_waitcnt lgkmcnt(0)\n"
"s_lshl_b32 s60, s60, 2\n"
"s_lshr_b32 s61, s99, 1\n"
"s_add_u32 s60, s60, s61\n"
"v_mfma_f32_16x16x32_bf16 v[168:171], a[128:131], v[16:19], 0\n"
"ds_read_b64_tr_b16 v[24:25], v211 offset:40960\n"
"s_lshl_b32 s60, s60, 2\n"
"s_add_u32 s61, s60, s90\n"
"s_lshl_b32 s61, s61, 2\n"
"v_mfma_f32_16x16x32_bf16 v[168:171], a[148:151], v[20:23], v[168:171]\n"
"ds_read_b64_tr_b16 v[26:27], v211 offset:41472\n"
"s_add_u32 s61, s61, s58\n"
"s_lshl_b32 s70, s61, 6\n"
"s_add_u32 s70, s70, 0x20000\n"
"v_mfma_f32_16x16x32_bf16 v[172:175], a[152:155], v[20:23], 0\n"
"ds_read_b64_tr_b16 v[28:29], v211 offset:61440\n"
"s_mul_i32 s61, s61, 10240\n"
"s_add_u32 s61, s61, 0x1f00000\n"
"s_add_u32 s72, s56, s61\n"
"s_addc_u32 s73, s57, 0\n"
"v_mfma_f32_16x16x32_bf16 v[172:175], a[132:135], v[16:19], v[172:175]\n"
"ds_read_b64_tr_b16 v[30:31], v211 offset:61952\n"
"s_mov_b32 s74, 0x2800\n"
"s_mov_b32 s75, 0x20000\n"
"s_add_u32 s60, s60, s58\n"
"v_mfma_f32_16x16x32_bf16 v[176:179], a[136:139], v[16:19], 0\n"
"buffer_load_dwordx4 v46, s[16:19], s31 offen offset:2048 lds\n"
"s_lshl_b32 s61, s60, 8\n"
"s_add_u32 s61, s61, 0x20000\n"
"v_and_b32 v224, 3, v13\n"
"v_mfma_f32_16x16x32_bf16 v[176:179], a[156:159], v[20:23], v[176:179]\n"
"buffer_load_dwordx4 v47, s[16:19], s31 offen offset:3072 lds\n"
"v_lshlrev_b32 v224, 6, v224\n"
"v_add_u32 v69, s61, v224\n"
"s_mul_i32 s60, s60, 40960\n"
"v_mfma_f32_16x16x32_bf16 v[180:183], a[160:163], v[20:23], 0\n"
"s_mov_b32 s35, 0x6400\n"
"buffer_load_dwordx4 a[228:231], v12, s[20:23], s35 offen sc1\n"
"s_mul_i32 s61, s90, 2560\n"
"s_add_u32 s60, s60, s61\n"
"s_add_u32 s60, s60, 0x1f00000\n"
"v_mfma_f32_16x16x32_bf16 v[180:183], a[140:143], v[16:19], v[180:183]\n"
"buffer_load_dwordx4 a[232:235], v12, s[20:23], s35 offen offset:1024 sc1\n"
"s_add_u32 s84, s56, s60\n"
"s_addc_u32 s85, s57, 0\n"
"s_mov_b32 s86, 0x8200\n"
"s_mov_b32 s87, 0x20000\n"
"v_mfma_f32_16x16x32_bf16 v[184:187], a[144:147], v[16:19], 0\n"
"buffer_load_dwordx4 a[236:239], v12, s[20:23], s35 offen offset:2048 sc1\n"
"v_lshlrev_b32 v68, 3, v13\n"
"v_mfma_f32_16x16x32_bf16 v[184:187], a[164:167], v[20:23], v[184:187]\n"
"s_waitcnt lgkmcnt(0)\n"
"v_mfma_f32_16x16x32_bf16 v[188:191], a[128:131], v[24:27], 0\n"
"buffer_load_dwordx4 a[240:243], v12, s[20:23], s35 offen offset:3072 sc1\n"
"v_mfma_f32_16x16x32_bf16 v[188:191], a[148:151], v[28:31], v[188:191]\n"
"buffer_load_dwordx4 a[244:247], v52, s[20:23], s35 offen sc1\n"
"v_mfma_f32_16x16x32_bf16 v[192:195], a[152:155], v[28:31], 0\n"
"v_mfma_f32_16x16x32_bf16 v[192:195], a[132:135], v[24:27], v[192:195]\n"
"v_mfma_f32_16x16x32_bf16 v[196:199], a[136:139], v[24:27], 0\n"
"v_mfma_f32_16x16x32_bf16 v[196:199], a[156:159], v[28:31], v[196:199]\n"
"v_mfma_f32_16x16x32_bf16 v[200:203], a[160:163], v[28:31], 0\n"
"v_mfma_f32_16x16x32_bf16 v[200:203], a[140:143], v[24:27], v[200:203]\n"
"v_mfma_f32_16x16x32_bf16 v[204:207], a[144:147], v[24:27], 0\n"
"v_mfma_f32_16x16x32_bf16 v[204:207], a[164:167], v[28:31], v[204:207]\n"
"s_waitcnt vmcnt(23)\n"
"ds_read_b64_tr_b16 v[16:17], v208 offset:0\n"
"ds_read_b64_tr_b16 v[18:19], v208 offset:512\n"
"ds_read_b64_tr_b16 v[20:21], v208 offset:20480\n"
"ds_read_b64_tr_b16 v[22:23], v208 offset:20992\n"
"s_mov_b32 s31, 0xc0000\n"
"s_add_u32 m0, s63, 0x0\n"
"s_nop 0\n"
"buffer_load_dwordx4 v44, s[16:19], s31 offen lds\n"
"buffer_load_dwordx4 v45, s[16:19], s31 offen offset:1024 lds\n"
"s_waitcnt lgkmcnt(0)\n"
"v_mfma_f32_16x16x32_bf16 v[128:131], a[168:171], v[16:19], v[128:131]\n"
"ds_read_b64_tr_b16 v[24:25], v209 offset:0\n"
"v_mfma_f32_16x16x32_bf16 v[128:131], a[188:191], v[20:23], v[128:131]\n"
"ds_read_b64_tr_b16 v[26:27], v209 offset:512\n"
"v_mfma_f32_16x16x32_bf16 v[132:135], a[192:195], v[20:23], v[132:135]\n"
"ds_read_b64_tr_b16 v[28:29], v209 offset:20480\n"
"v_mfma_f32_16x16x32_bf16 v[132:135], a[172:175], v[16:19], v[132:135]\n"
"ds_read_b64_tr_b16 v[30:31], v209 offset:20992\n"
"v_mfma_f32_16x16x32_bf16 v[136:139], a[176:179], v[16:19], v[136:139]\n"
"buffer_load_dwordx4 v46, s[16:19], s31 offen offset:2048 lds\n"
"v_mfma_f32_16x16x32_bf16 v[136:139], a[196:199], v[20:23], v[136:139]\n"
"buffer_load_dwordx4 v47, s[16:19], s31 offen offset:3072 lds\n"
"v_mfma_f32_16x16x32_bf16 v[140:143], a[200:203], v[20:23], v[140:143]\n"
"s_mov_b32 s35, 0x7800\n"
"buffer_load_dwordx4 a[128:131], v12, s[20:23], s35 offen sc1\n"
"v_mfma_f32_16x16x32_bf16 v[140:143], a[180:183], v[16:19], v[140:143]\n"
"buffer_load_dwordx4 a[132:135], v12, s[20:23], s35 offen offset:1024 sc1\n"
"v_mfma_f32_16x16x32_bf16 v[144:147], a[184:187], v[16:19], v[144:147]\n"
"buffer_load_dwordx4 a[136:139], v12, s[20:23], s35 offen offset:2048 sc1\n"
"v_mfma_f32_16x16x32_bf16 v[144:147], a[204:207], v[20:23], v[144:147]\n"
"s_waitcnt lgkmcnt(0)\n"
"v_mfma_f32_16x16x32_bf16 v[148:151], a[168:171], v[24:27], v[148:151]\n"
"ds_read_b64_tr_b16 v[16:17], v210 offset:0\n"
"v_mfma_f32_16x16x32_bf16 v[148:151], a[188:191], v[28:31], v[148:151]\n"
"ds_read_b64_tr_b16 v[18:19], v210 offset:512\n"
"v_mfma_f32_16x16x32_bf16 v[152:155], a[192:195], v[28:31], v[152:155]\n"
"ds_read_b64_tr_b16 v[20:21], v210 offset:20480\n"
"v_mfma_f32_16x16x32_bf16 v[152:155], a[172:175], v[24:27], v[152:155]\n"
"ds_read_b64_tr_b16 v[22:23], v210 offset:20992\n"
"v_mfma_f32_16x16x32_bf16 v[156:159], a[176:179], v[24:27], v[156:159]\n"
"buffer_load_dwordx4 a[140:143], v12, s[20:23], s35 offen offset:3072 sc1\n"
"v_mfma_f32_16x16x32_bf16 v[156:159], a[196:199], v[28:31], v[156:159]\n"
"buffer_load_dwordx4 a[144:147], v52, s[20:23], s35 offen sc1\n"
"v_mfma_f32_16x16x32_bf16 v[160:163], a[200:203], v[28:31], v[160:163]\n"
"s_mov_b32 s31, 0xe0000\n"
"s_add_u32 m0, s63, 0x5000\n"
"s_nop 0\n"
"buffer_load_dwordx4 v44, s[16:19], s31 offen lds\n"
"v_mfma_f32_16x16x32_bf16 v[160:163], a[180:183], v[24:27], v[160:163]\n"
"buffer_load_dwordx4 v45, s[16:19], s31 offen offset:1024 lds\n"
"v_mfma_f32_16x16x32_bf16 v[164:167], a[184:187], v[24:27], v[164:167]\n"
"buffer_load_dwordx4 v46, s[16:19], s31 offen offset:2048 lds\n"
"v_mfma_f32_16x16x32_bf16 v[164:167], a[204:207], v[28:31], v[164:167]\n"
"s_waitcnt lgkmcnt(0)\n"
"v_mfma_f32_16x16x32_bf16 v[168:171], a[168:171], v[16:19], v[168:171]\n"
"ds_read_b64_tr_b16 v[24:25], v211 offset:0\n"
"v_mfma_f32_16x16x32_bf16 v[168:171], a[188:191], v[20:23], v[168:171]\n"
"ds_read_b64_tr_b16 v[26:27], v211 offset:512\n"
"v_mfma_f32_16x16x32_bf16 v[172:175], a[192:195], v[20:23], v[172:175]\n"
"ds_read_b64_tr_b16 v[28:29], v211 offset:20480\n"
"v_mfma_f32_16x16x32_bf16 v[172:175], a[172:175], v[16:19], v[172:175]\n"
"ds_read_b64_tr_b16 v[30:31], v211 offset:20992\n"
"v_mfma_f32_16x16x32_bf16 v[176:179], a[176:179], v[16:19], v[176:179]\n"
"buffer_load_dwordx4 v47, s[16:19], s31 offen offset:3072 lds\n"
"v_mfma_f32_16x16x32_bf16 v[176:179], a[196:199], v[20:23], v[176:179]\n"
"s_mov_b32 s35, 0x8c00\n"
"buffer_load_dwordx4 a[148:151], v12, s[20:23], s35 offen sc1\n"
"v_mfma_f32_16x16x32_bf16 v[180:183], a[200:203], v[20:23], v[180:183]\n"
"buffer_load_dwordx4 a[152:155], v12, s[20:23], s35 offen offset:1024 sc1\n"
"v_mfma_f32_16x16x32_bf16 v[180:183], a[180:183], v[16:19], v[180:183]\n"
"buffer_load_dwordx4 a[156:159], v12, s[20:23], s35 offen offset:2048 sc1\n"
"v_mfma_f32_16x16x32_bf16 v[184:187], a[184:187], v[16:19], v[184:187]\n"
"buffer_load_dwordx4 a[160:163], v12, s[20:23], s35 offen offset:3072 sc1\n"
"v_mfma_f32_16x16x32_bf16 v[184:187], a[204:207], v[20:23], v[184:187]\n"
"s_waitcnt lgkmcnt(0)\n"
"v_mfma_f32_16x16x32_bf16 v[188:191], a[168:171], v[24:27], v[188:191]\n"
"buffer_load_dwordx4 a[164:167], v52, s[20:23], s35 offen sc1\n"
"v_mfma_f32_16x16x32_bf16 v[188:191], a[188:191], v[28:31], v[188:191]\n"
"v_mfma_f32_16x16x32_bf16 v[192:195], a[192:195], v[28:31], v[192:195]\n"
"v_mfma_f32_16x16x32_bf16 v[192:195], a[172:175], v[24:27], v[192:195]\n"
"v_mfma_f32_16x16x32_bf16 v[196:199], a[176:179], v[24:27], v[196:199]\n"
"v_mfma_f32_16x16x32_bf16 v[196:199], a[196:199], v[28:31], v[196:199]\n"
"v_mfma_f32_16x16x32_bf16 v[200:203], a[200:203], v[28:31], v[200:203]\n"
"v_mfma_f32_16x16x32_bf16 v[200:203], a[180:183], v[24:27], v[200:203]\n"
"v_mfma_f32_16x16x32_bf16 v[204:207], a[184:187], v[24:27], v[204:207]\n"
"v_mfma_f32_16x16x32_bf16 v[204:207], a[204:207], v[28:31], v[204:207]\n"
"s_waitcnt vmcnt(18)\n"
"s_barrier\n"
"buffer_load_dwordx2 v[54:55], v102, s[92:95], 0 offen sc1\n"
"ds_read_b64_tr_b16 v[16:17], v48 offset:40960\n"
"ds_read_b64_tr_b16 v[18:19], v48 offset:41472\n"
"ds_read_b64_tr_b16 v[20:21], v48 offset:61440\n"
"ds_read_b64_tr_b16 v[22:23], v48 offset:61952\n"
"s_waitcnt vmcnt(0)\n"
"v_cmp_eq_u64 vcc, s[88:89], v[54:55]\n"
"s_mov_b32 s91, 0\n"
"s_nop 2\n"
"s_cmp_eq_u64 vcc, -1\n"
"s_cbranch_scc1 L_OK4%=\n"
"L_SP4%=:\n"
"s_sleep 2\n"
"buffer_load_dwordx2 v[54:55], v102, s[92:95], 0 offen sc1\n"
"s_waitcnt vmcnt(0)\n"
"v_cmp_eq_u64 vcc, s[88:89], v[54:55]\n"
"s_add_u32 s91, s91, 1\n"
"s_nop 3\n"
"s_cmp_eq_u64 vcc, -1\n"
"s_cbranch_scc1 L_OK4%=\n"
"s_cmp_lt_u32 s91, 0x40000\n"
"s_cbranch_scc1 L_SP4%=\n"
"L_OK4%=:\n"
"s_mov_b32 s35, 0x0\n"
"s_add_u32 m0, s82, 0x1e000\n"
"s_nop 0\n"
"buffer_load_dwordx4 v12, s[12:15], s35 offen sc1 lds\n"
"s_waitcnt lgkmcnt(0)\n"
"v_mfma_f32_16x16x32_bf16 v[128:131], a[208:211], v[16:19], v[128:131]\n"
"ds_read_b64_tr_b16 v[24:25], v49 offset:40960\n"
"v_mfma_f32_16x16x32_bf16 v[128:131], a[228:231], v[20:23], v[128:131]\n"
"ds_read_b64_tr_b16 v[26:27], v49 offset:41472\n"
"v_mfma_f32_16x16x32_bf16 v[132:135], a[232:235], v[20:23], v[132:135]\n"
"ds_read_b64_tr_b16 v[28:29], v49 offset:61440\n"
"v_mfma_f32_16x16x32_bf16 v[132:135], a[212:215], v[16:19], v[132:135]\n"
"ds_read_b64_tr_b16 v[30:31], v49 offset:61952\n"
"v_mfma_f32_16x16x32_bf16 v[136:139], a[216:219], v[16:19], v[136:139]\n"
"buffer_load_dwordx4 v12, s[12:15], s35 offen offset:1024 sc1 lds\n"
"v_mfma_f32_16x16x32_bf16 v[136:139], a[236:239], v[20:23], v[136:139]\n"
"buffer_load_dwordx4 v12, s[12:15], s35 offen offset:2048 sc1 lds\n"
"v_mfma_f32_16x16x32_bf16 v[140:143], a[240:243], v[20:23], v[140:143]\n"
"buffer_load_dwordx4 v12, s[12:15], s35 offen offset:3072 sc1 lds\n"
"v_mfma_f32_16x16x32_bf16 v[140:143], a[220:223], v[16:19], v[140:143]\n"
"s_mov_b32 s31, 0x0\n"
"s_add_u32 m0, s83, 0x1e000\n"
"s_nop 0\n"
"buffer_load_dwordx4 v12, s[8:11], s31 offen sc1 lds\n"
"v_mfma_f32_16x16x32_bf16 v[144:147], a[224:227], v[16:19], v[144:147]\n"
"buffer_load_dwordx4 v12, s[8:11], s31 offen offset:1024 sc1 lds\n"
"v_mfma_f32_16x16x32_bf16 v[144:147], a[244:247], v[20:23], v[144:147]\n"
"s_waitcnt lgkmcnt(0)\n"
"v_mfma_f32_16x16x32_bf16 v[148:151], a[208:211], v[24:27], v[148:151]\n"
"ds_read_b64_tr_b16 v[16:17], v50 offset:40960\n"
"v_mfma_f32_16x16x32_bf16 v[148:151], a[228:231], v[28:31], v[148:151]\n"
"ds_read_b64_tr_b16 v[18:19], v50 offset:41472\n"
"v_mfma_f32_16x16x32_bf16 v[152:155], a[232:235], v[28:31], v[152:155]\n"
"ds_read_b64_tr_b16 v[20:21], v50 offset:61440\n"
"v_mfma_f32_16x16x32_bf16 v[152:155], a[212:215], v[24:27], v[152:155]\n"
"ds_read_b64_tr_b16 v[22:23], v50 offset:61952\n"
"v_mfma_f32_16x16x32_bf16 v[156:159], a[216:219], v[24:27], v[156:159]\n"
"buffer_load_dwordx4 v12, s[8:11], s31 offen offset:2048 sc1 lds\n"
"v_mfma_f32_16x16x32_bf16 v[156:159], a[236:239], v[28:31], v[156:159]\n"
"buffer_load_dwordx4 v12, s[8:11], s31 offen offset:3072 sc1 lds\n"
"v_mfma_f32_16x16x32_bf16 v[160:163], a[240:243], v[28:31], v[160:163]\n"
"s_add_u32 m0, s83, 0x1f000\n"
"s_nop 0\n"
"buffer_load_dwordx4 v52, s[8:11], s31 offen sc1 lds\n"
"v_mfma_f32_16x16x32_bf16 v[160:163], a[220:223], v[24:27], v[160:163]\n"
"s_mov_b32 s35, 0x2000\n"
"s_add_u32 m0, s82, 0x14000\n"
"s_nop 0\n"
"buffer_load_dwordx4 v12, s[12:15], s35 offen sc1 lds\n"
"v_mfma_f32_16x16x32_bf16 v[164:167], a[224:227], v[24:27], v[164:167]\n"
"buffer_load_dwordx4 v12, s[12:15], s35 offen offset:1024 sc1 lds\n"
"v_mfma_f32_16x16x32_bf16 v[164:167], a[244:247], v[28:31], v[164:167]\n"
"s_waitcnt lgkmcnt(0)\n"
"v_mfma_f32_16x16x32_bf16 v[168:171], a[208:211], v[16:19], v[168:171]\n"
"ds_read_b64_tr_b16 v[24:25], v51 offset:40960\n"
"v_mfma_f32_16x16x32_bf16 v[168:171], a[228:231], v[20:23], v[168:171]\n"
"ds_read_b64_tr_b16 v[26:27], v51 offset:41472\n"
"v_mfma_f32_16x16x32_bf16 v[172:175], a[232:235], v[20:23], v[172:175]\n"
"ds_read_b64_tr_b16 v[28:29], v51 offset:61440\n"
"v_mfma_f32_16x16x32_bf16 v[172:175], a[212:215], v[16:19], v[172:175]\n"
"ds_read_b64_tr_b16 v[30:31], v51 offset:61952\n"
"v_mfma_f32_16x16x32_bf16 v[176:179], a[216:219], v[16:19], v[176:179]\n"
"buffer_load_dwordx4 v12, s[12:15], s35 offen offset:2048 sc1 lds\n"
"v_mfma_f32_16x16x32_bf16 v[176:179], a[236:239], v[20:23], v[176:179]\n"
"buffer_load_dwordx4 v12, s[12:15], s35 offen offset:3072 sc1 lds\n"
"v_mfma_f32_16x16x32_bf16 v[180:183], a[240:243], v[20:23], v[180:183]\n"
"s_mov_b32 s31, 0x2800\n"
"s_add_u32 m0, s83, 0x14000\n"
"s_nop 0\n"
"buffer_load_dwordx4 v12, s[8:11], s31 offen sc1 lds\n"
"v_mfma_f32_16x16x32_bf16 v[180:183], a[220:223], v[16:19], v[180:183]\n"
"buffer_load_dwordx4 v12, s[8:11], s31 offen offset:1024 sc1 lds\n"
"v_mfma_f32_16x16x32_bf16 v[184:187], a[224:227], v[16:19], v[184:187]\n"
"buffer_load_dwordx4 v12, s[8:11], s31 offen offset:2048 sc1 lds\n"
"v_mfma_f32_16x16x32_bf16 v[184:187], a[244:247], v[20:23], v[184:187]\n"
"s_waitcnt lgkmcnt(0)\n"
"v_mfma_f32_16x16x32_bf16 v[188:191], a[208:211], v[24:27], v[188:191]\n"
"buffer_load_dwordx4 v12, s[8:11], s31 offen offset:3072 sc1 lds\n"
"v_mfma_f32_16x16x32_bf16 v[188:191], a[228:231], v[28:31], v[188:191]\n"
"s_add_u32 m0, s83, 0x15000\n"
"s_nop 0\n"
"buffer_load_dwordx4 v52, s[8:11], s31 offen sc1 lds\n"
"v_mfma_f32_16x16x32_bf16 v[192:195], a[232:235], v[28:31], v[192:195]\n"
"v_mfma_f32_16x16x32_bf16 v[192:195], a[212:215], v[24:27], v[192:195]\n"
"v_mfma_f32_16x16x32_bf16 v[196:199], a[216:219], v[24:27], v[196:199]\n"
"v_mfma_f32_16x16x32_bf16 v[196:199], a[236:239], v[28:31], v[196:199]\n"
"v_mfma_f32_16x16x32_bf16 v[200:203], a[240:243], v[28:31], v[200:203]\n"
"v_mfma_f32_16x16x32_bf16 v[200:203], a[220:223], v[24:27], v[200:203]\n"
"v_mfma_f32_16x16x32_bf16 v[204:207], a[224:227], v[24:27], v[204:207]\n"
"v_mfma_f32_16x16x32_bf16 v[204:207], a[244:247], v[28:31], v[204:207]\n"
"s_waitcnt vmcnt(19)\n"
"s_barrier\n"
"ds_read_b64_tr_b16 v[16:17], v48 offset:0\n"
"ds_read_b64_tr_b16 v[18:19], v48 offset:512\n"
"ds_read_b64_tr_b16 v[20:21], v48 offset:20480\n"
"ds_read_b64_tr_b16 v[22:23], v48 offset:20992\n"
"s_mov_b32 s35, 0x4000\n"
"s_add_u32 m0, s82, 0xa000\n"
"s_nop 0\n"
"buffer_load_dwordx4 v12, s[12:15], s35 offen sc1 lds\n"
"buffer_load_dwordx4 v12, s[12:15], s35 offen offset:1024 sc1 lds\n"
"s_waitcnt lgkmcnt(0)\n"
"v_mfma_f32_16x16x32_bf16 v[128:131], a[128:131], v[16:19], v[128:131]\n"
"ds_read_b64_tr_b16 v[24:25], v49 offset:0\n"
"v_mfma_f32_16x16x32_bf16 v[128:131], a[148:151], v[20:23], v[128:131]\n"
"ds_read_b64_tr_b16 v[26:27], v49 offset:512\n"
"v_mfma_f32_16x16x32_bf16 v[132:135], a[152:155], v[20:23], v[132:135]\n"
"ds_read_b64_tr_b16 v[28:29], v49 offset:20480\n"
"v_mfma_f32_16x16x32_bf16 v[132:135], a[132:135], v[16:19], v[132:135]\n"
"ds_read_b64_tr_b16 v[30:31], v49 offset:20992\n"
"v_mfma_f32_16x16x32_bf16 v[136:139], a[136:139], v[16:19], v[136:139]\n"
"buffer_load_dwordx4 v12, s[12:15], s35 offen offset:2048 sc1 lds\n"
"v_mfma_f32_16x16x32_bf16 v[136:139], a[156:159], v[20:23], v[136:139]\n"
"buffer_load_dwordx4 v12, s[12:15], s35 offen offset:3072 sc1 lds\n"
"v_mfma_f32_16x16x32_bf16 v[140:143], a[160:163], v[20:23], v[140:143]\n"
"s_mov_b32 s31, 0x5000\n"
"s_add_u32 m0, s83, 0xa000\n"
"s_nop 0\n"
"buffer_load_dwordx4 v12, s[8:11], s31 offen sc1 lds\n"
"v_mfma_f32_16x16x32_bf16 v[140:143], a[140:143], v[16:19], v[140:143]\n"
"buffer_load_dwordx4 v12, s[8:11], s31 offen offset:1024 sc1 lds\n"
"v_mfma_f32_16x16x32_bf16 v[144:147], a[144:147], v[16:19], v[144:147]\n"
"buffer_load_dwordx4 v12, s[8:11], s31 offen offset:2048 sc1 lds\n"
"v_mfma_f32_16x16x32_bf16 v[144:147], a[164:167], v[20:23], v[144:147]\n"
"s_waitcnt lgkmcnt(0)\n"
"v_mfma_f32_16x16x32_bf16 v[148:151], a[128:131], v[24:27], v[148:151]\n"
"ds_read_b64_tr_b16 v[16:17], v50 offset:0\n"
"v_mfma_f32_16x16x32_bf16 v[148:151], a[148:151], v[28:31], v[148:151]\n"
"ds_read_b64_tr_b16 v[18:19], v50 offset:512\n"
"v_mfma_f32_16x16x32_bf16 v[152:155], a[152:155], v[28:31], v[152:155]\n"
"ds_read_b64_tr_b16 v[20:21], v50 offset:20480\n"
"v_mfma_f32_16x16x32_bf16 v[152:155], a[132:135], v[24:27], v[152:155]\n"
"ds_read_b64_tr_b16 v[22:23], v50 offset:20992\n"
"v_mfma_f32_16x16x32_bf16 v[156:159], a[136:139], v[24:27], v[156:159]\n"
"buffer_load_dwordx4 v12, s[8:11], s31 offen offset:3072 sc1 lds\n"
"v_mfma_f32_16x16x32_bf16 v[156:159], a[156:159], v[28:31], v[156:159]\n"
"s_add_u32 m0, s83, 0xb000\n"
"s_nop 0\n"
"buffer_load_dwordx4 v52, s[8:11], s31 offen sc1 lds\n"
"v_mfma_f32_16x16x32_bf16 v[160:163], a[160:163], v[28:31], v[160:163]\n"
"v_mfma_f32_16x16x32_bf16 v[160:163], a[140:143], v[24:27], v[160:163]\n"
"v_mfma_f32_16x16x32_bf16 v[164:167], a[144:147], v[24:27], v[164:167]\n"
"v_mfma_f32_16x16x32_bf16 v[164:167], a[164:167], v[28:31], v[164:167]\n"
"s_waitcnt lgkmcnt(0)\n"
"v_mfma_f32_16x16x32_bf16 v[168:171], a[128:131], v[16:19], v[168:171]\n"
"ds_read_b64_tr_b16 v[24:25], v51 offset:0\n"
"v_mfma_f32_16x16x32_bf16 v[168:171], a[148:151], v[20:23], v[168:171]\n"
"ds_read_b64_tr_b16 v[26:27], v51 offset:512\n"
"v_mfma_f32_16x16x32_bf16 v[172:175], a[152:155], v[20:23], v[172:175]\n"
"ds_read_b64_tr_b16 v[28:29], v51 offset:20480\n"
"v_mfma_f32_16x16x32_bf16 v[172:175], a[132:135], v[16:19], v[172:175]\n"
"ds_read_b64_tr_b16 v[30:31], v51 offset:20992\n"
"v_mfma_f32_16x16x32_bf16 v[176:179], a[136:139], v[16:19], v[176:179]\n"
"v_mfma_f32_16x16x32_bf16 v[176:179], a[156:159], v[20:23], v[176:179]\n"
"v_mfma_f32_16x16x32_bf16 v[180:183], a[160:163], v[20:23], v[180:183]\n"
"v_mfma_f32_16x16x32_bf16 v[180:183], a[140:143], v[16:19], v[180:183]\n"
"v_mfma_f32_16x16x32_bf16 v[184:187], a[144:147], v[16:19], v[184:187]\n"
"v_mfma_f32_16x16x32_bf16 v[184:187], a[164:167], v[20:23], v[184:187]\n"
"s_waitcnt vmcnt(18) lgkmcnt(0)\n"
"s_barrier\n"
"v_mfma_f32_16x16x32_bf16 v[188:191], a[128:131], v[24:27], v[188:191]\n"
"ds_read_b128 v[16:19], v67 offset:40960\n"
"ds_read_b64_tr_b16 a[208:209], v78 offset:40960\n"
"v_mfma_f32_16x16x32_bf16 v[188:191], a[148:151], v[28:31], v[188:191]\n"
"ds_read_b64_tr_b16 a[210:211], v79 offset:40960\n"
"ds_read_b128 v[20:23], v67 offset:46080\n"
"v_mfma_f32_16x16x32_bf16 v[192:195], a[152:155], v[28:31], v[192:195]\n"
"ds_read_b64_tr_b16 a[224:225], v78 offset:45056\n"
"ds_read_b64_tr_b16 a[226:227], v79 offset:45056\n"
"v_mfma_f32_16x16x32_bf16 v[192:195], a[132:135], v[24:27], v[192:195]\n"
"ds_read_b64_tr_b16 a[228:229], v78 offset:46080\n"
"ds_read_b64_tr_b16 a[230:231], v79 offset:46080\n"
"v_mfma_f32_16x16x32_bf16 v[196:199], a[136:139], v[24:27], v[196:199]\n"
"ds_read_b64_tr_b16 a[212:213], v78 offset:41984\n"
"ds_read_b64_tr_b16 a[214:215], v79 offset:41984\n"
"v_mfma_f32_16x16x32_bf16 v[196:199], a[156:159], v[28:31], v[196:199]\n"
"ds_read_b64_tr_b16 a[216:217], v78 offset:43008\n"
"ds_read_b64_tr_b16 a[218:219], v79 offset:43008\n"
"v_mfma_f32_16x16x32_bf16 v[200:203], a[160:163], v[28:31], v[200:203]\n"
"ds_read_b64_tr_b16 a[232:233], v78 offset:47104\n"
"v_mfma_f32_16x16x32_bf16 v[200:203], a[140:143], v[24:27], v[200:203]\n"
"ds_read_b64_tr_b16 a[234:235], v79 offset:47104\n"
"v_mfma_f32_16x16x32_bf16 v[204:207], a[144:147], v[24:27], v[204:207]\n"
"ds_read_b64_tr_b16 a[236:237], v78 offset:48128\n"
"v_mfma_f32_16x16x32_bf16 v[204:207], a[164:167], v[28:31], v[204:207]\n"
"ds_read_b64_tr_b16 a[238:239], v79 offset:48128\n"
"s_waitcnt lgkmcnt(6)\n"
"v_mfma_f32_16x16x32_bf16 a[0:3], v[16:19], a[208:211], 0\n"
"ds_read_b64_tr_b16 a[220:221], v78 offset:44032\n"
"v_mfma_f32_16x16x32_bf16 a[0:3], v[20:23], a[224:227], a[0:3]\n"
"ds_read_b64_tr_b16 a[222:223], v79 offset:44032\n"
"v_mfma_f32_16x16x32_bf16 a[20:23], v[20:23], a[228:231], 0\n"
"ds_read_b128 v[24:27], v67 offset:41984\n"
"v_mfma_f32_16x16x32_bf16 a[20:23], v[16:19], a[212:215], a[20:23]\n"
"s_waitcnt lgkmcnt(3)\n"
"ds_read_b128 v[28:31], v67 offset:47104\n"
"v_mfma_f32_16x16x32_bf16 a[40:43], v[16:19], a[216:219], 0\n"
"s_mov_b32 s35, 0x6000\n"
"s_add_u32 m0, s82, 0x0\n"
"s_nop 0\n"
"buffer_load_dwordx4 v12, s[12:15], s35 offen sc1 lds\n"
"v_mfma_f32_16x16x32_bf16 a[40:43], v[20:23], a[232:235], a[40:43]\n"
"s_waitcnt lgkmcnt(2)\n"
"buffer_load_dwordx4 v12, s[12:15], s35 offen offset:1024 sc1 lds\n"
"v_mfma_f32_16x16x32_bf16 a[60:63], v[20:23], a[236:239], 0\n"
"buffer_load_dwordx4 v12, s[12:15], s35 offen offset:2048 sc1 lds\n"
"v_mfma_f32_16x16x32_bf16 a[60:63], v[16:19], a[220:223], a[60:63]\n"
"s_waitcnt lgkmcnt(0)\n"
"v_mfma_f32_16x16x32_bf16 a[4:7], v[24:27], a[208:211], 0\n"
"ds_read_b128 v[70:73], v67 offset:43008\n"
"v_mfma_f32_16x16x32_bf16 a[4:7], v[28:31], a[224:227], a[4:7]\n"
"ds_read_b128 v[74:77], v67 offset:48128\n"
"v_mfma_f32_16x16x32_bf16 a[24:27], v[28:31], a[228:231], 0\n"
"buffer_load_dwordx4 v12, s[12:15], s35 offen offset:3072 sc1 lds\n"
"v_mfma_f32_16x16x32_bf16 a[24:27], v[24:27], a[212:215], a[24:27]\n"
"s_mov_b32 s31, 0x7800\n"
"s_add_u32 m0, s83, 0x0\n"
"s_nop 0\n"
"buffer_load_dwordx4 v12, s[8:11], s31 offen sc1 lds\n"
"v_mfma_f32_16x16x32_bf16 a[44:47], v[24:27], a[216:219], 0\n"
"buffer_load_dwordx4 v12, s[8:11], s31 offen offset:1024 sc1 lds\n"
"v_mfma_f32_16x16x32_bf16 a[44:47], v[28:31], a[232:235], a[44:47]\n"
"buffer_load_dwordx4 v12, s[8:11], s31 offen offset:2048 sc1 lds\n"
"v_mfma_f32_16x16x32_bf16 a[64:67], v[28:31], a[236:239], 0\n"
"buffer_load_dwordx4 v12, s[8:11], s31 offen offset:3072 sc1 lds\n"
"v_mfma_f32_16x16x32_bf16 a[64:67], v[24:27], a[220:223], a[64:67]\n"
"s_waitcnt lgkmcnt(0)\n"
"v_mfma_f32_16x16x32_bf16 a[8:11], v[70:73], a[208:211], 0\n"
"ds_read_b128 v[16:19], v67 offset:44032\n"
"v_mfma_f32_16x16x32_bf16 a[8:11], v[74:77], a[224:227], a[8:11]\n"
"ds_read_b128 v[20:23], v67 offset:49152\n"
"v_mfma_f32_16x16x32_bf16 a[28:31], v[74:77], a[228:231], 0\n"
"ds_read_b128 v[24:27], v67 offset:45056\n"
"v_mfma_f32_16x16x32_bf16 a[28:31], v[70:73], a[212:215], a[28:31]\n"
"ds_read_b128 v[28:31], v67 offset:50176\n"
"v_mfma_f32_16x16x32_bf16 a[48:51], v[70:73], a[216:219], 0\n"
"s_add_u32 m0, s83, 0x1000\n"
"s_nop 0\n"
"buffer_load_dwordx4 v52, s[8:11], s31 offen sc1 lds\n"
"v_mfma_f32_16x16x32_bf16 a[48:51], v[74:77], a[232:235], a[48:51]\n"
"v_mfma_f32_16x16x32_bf16 a[68:71], v[74:77], a[236:239], 0\n"
"v_mfma_f32_16x16x32_bf16 a[68:71], v[70:73], a[220:223], a[68:71]\n"
"s_waitcnt vmcnt(18) lgkmcnt(0)\n"
"s_barrier\n"
"v_mfma_f32_16x16x32_bf16 a[12:15], v[16:19], a[208:211], 0\n"
"ds_read_b128 v[70:73], v67 offset:0\n"
"v_mfma_f32_16x16x32_bf16 a[12:15], v[20:23], a[224:227], a[12:15]\n"
"ds_read_b64_tr_b16 a[80:81], v78 offset:0\n"
"v_mfma_f32_16x16x32_bf16 a[32:35], v[20:23], a[228:231], 0\n"
"ds_read_b64_tr_b16 a[82:83], v79 offset:0\n"
"v_mfma_f32_16x16x32_bf16 a[32:35], v[16:19], a[212:215], a[32:35]\n"
"ds_read_b128 v[74:77], v67 offset:5120\n"
"v_mfma_f32_16x16x32_bf16 a[52:55], v[16:19], a[216:219], 0\n"
"ds_read_b64_tr_b16 a[96:97], v78 offset:4096\n"
"v_mfma_f32_16x16x32_bf16 a[52:55], v[20:23], a[232:235], a[52:55]\n"
"ds_read_b64_tr_b16 a[98:99], v79 offset:4096\n"
"v_mfma_f32_16x16x32_bf16 a[72:75], v[20:23], a[236:239], 0\n"
"ds_read_b64_tr_b16 a[100:101], v78 offset:5120\n"
"v_mfma_f32_16x16x32_bf16 a[72:75], v[16:19], a[220:223], a[72:75]\n"
"ds_read_b64_tr_b16 a[102:103], v79 offset:5120\n"
"v_mfma_f32_16x16x32_bf16 a[16:19], v[24:27], a[208:211], 0\n"
"ds_read_b64_tr_b16 a[84:85], v78 offset:1024\n"
"v_mfma_f32_16x16x32_bf16 a[16:19], v[28:31], a[224:227], a[16:19]\n"
"ds_read_b64_tr_b16 a[86:87], v79 offset:1024\n"
"v_mfma_f32_16x16x32_bf16 a[36:39], v[28:31], a[228:231], 0\n"
"ds_read_b64_tr_b16 a[88:89], v78 offset:2048\n"
"v_mfma_f32_16x16x32_bf16 a[36:39], v[24:27], a[212:215], a[36:39]\n"
"ds_read_b64_tr_b16 a[90:91], v79 offset:2048\n"
"v_mfma_f32_16x16x32_bf16 a[56:59], v[24:27], a[216:219], 0\n"
"ds_read_b64_tr_b16 a[104:105], v78 offset:6144\n"
"v_mfma_f32_16x16x32_bf16 a[56:59], v[28:31], a[232:235], a[56:59]\n"
"ds_read_b64_tr_b16 a[106:107], v79 offset:6144\n"
"v_mfma_f32_16x16x32_bf16 a[76:79], v[28:31], a[236:239], 0\n"
"ds_read_b64_tr_b16 a[108:109], v78 offset:7168\n"
"v_mfma_f32_16x16x32_bf16 a[76:79], v[24:27], a[220:223], a[76:79]\n"
"ds_read_b64_tr_b16 a[110:111], v79 offset:7168\n"
"s_waitcnt lgkmcnt(6)\n"
"v_mfma_f32_16x16x32_bf16 a[0:3], v[70:73], a[80:83], a[0:3]\n"
"ds_read_b64_tr_b16 a[92:93], v78 offset:3072\n"
"v_mfma_f32_16x16x32_bf16 a[0:3], v[74:77], a[96:99], a[0:3]\n"
"ds_read_b64_tr_b16 a[94:95], v79 offset:3072\n"
"v_mfma_f32_16x16x32_bf16 a[20:23], v[74:77], a[100:103], a[20:23]\n"
"ds_read_b128 v[16:19], v67 offset:1024\n"
"v_mfma_f32_16x16x32_bf16 a[20:23], v[70:73], a[84:87], a[20:23]\n"
"s_waitcnt lgkmcnt(3)\n"
"ds_read_b128 v[20:23], v67 offset:6144\n"
"v_mfma_f32_16x16x32_bf16 a[40:43], v[70:73], a[88:91], a[40:43]\n"
"s_mov_b32 s35, 0x8000\n"
"s_add_u32 m0, s82, 0x1e000\n"
"s_nop 0\n"
"buffer_load_dwordx4 v12, s[12:15], s35 offen sc1 lds\n"
"v_mfma_f32_16x16x32_bf16 a[40:43], v[74:77], a[104:107], a[40:43]\n"
"s_waitcnt lgkmcnt(2)\n"
"buffer_load_dwordx4 v12, s[12:15], s35 offen offset:1024 sc1 lds\n"
"v_mfma_f32_16x16x32_bf16 a[60:63], v[74:77], a[108:111], a[60:63]\n"
"buffer_load_dwordx4 v12, s[12:15], s35 offen offset:2048 sc1 lds\n"
"v_mfma_f32_16x16x32_bf16 a[60:63], v[70:73], a[92:95], a[60:63]\n"
"s_waitcnt lgkmcnt(0)\n"
"v_mfma_f32_16x16x32_bf16 a[4:7], v[16:19], a[80:83], a[4:7]\n"
"ds_read_b128 v[24:27], v67 offset:2048\n"
"v_mfma_f32_16x16x32_bf16 a[4:7], v[20:23], a[96:99], a[4:7]\n"
"ds_read_b128 v[28:31], v67 offset:7168\n"
"v_mfma_f32_16x16x32_bf16 a[24:27], v[20:23], a[100:103], a[24:27]\n"
"buffer_load_dwordx4 v12, s[12:15], s35 offen offset:3072 sc1 lds\n"
"v_mfma_f32_16x16x32_bf16 a[24:27], v[16:19], a[84:87], a[24:27]\n"
"s_mov_b32 s31, 0xa000\n"
"s_add_u32 m0, s83, 0x1e000\n"
"s_nop 0\n"
"buffer_load_dwordx4 v12, s[8:11], s31 offen sc1 lds\n"
"v_mfma_f32_16x16x32_bf16 a[44:47], v[16:19], a[88:91], a[44:47]\n"
"buffer_load_dwordx4 v12, s[8:11], s31 offen offset:1024 sc1 lds\n"
"v_mfma_f32_16x16x32_bf16 a[44:47], v[20:23], a[104:107], a[44:47]\n"
"buffer_load_dwordx4 v12, s[8:11], s31 offen offset:2048 sc1 lds\n"
"v_mfma_f32_16x16x32_bf16 a[64:67], v[20:23], a[108:111], a[64:67]\n"
"buffer_load_dwordx4 v12, s[8:11], s31 offen offset:3072 sc1 lds\n"
"v_mfma_f32_16x16x32_bf16 a[64:67], v[16:19], a[92:95], a[64:67]\n"
"s_waitcnt lgkmcnt(0)\n"
"v_mfma_f32_16x16x32_bf16 a[8:11], v[24:27], a[80:83], a[8:11]\n"
"ds_read_b128 v[70:73], v67 offset:3072\n"
"v_mfma_f32_16x16x32_bf16 a[8:11], v[28:31], a[96:99], a[8:11]\n"
"ds_read_b128 v[74:77], v67 offset:8192\n"
"v_mfma_f32_16x16x32_bf16 a[28:31], v[28:31], a[100:103], a[28:31]\n"
"ds_read_b128 v[16:19], v67 offset:4096\n"
"v_mfma_f32_16x16x32_bf16 a[28:31], v[24:27], a[84:87], a[28:31]\n"
"ds_read_b128 v[20:23], v67 offset:9216\n"
"v_mfma_f32_16x16x32_bf16 a[48:51], v[24:27], a[88:91], a[48:51]\n"
"s_add_u32 m0, s83, 0x1f000\n"
"s_nop 0\n"
"buffer_load_dwordx4 v52, s[8:11], s31 offen sc1 lds\n"
"v_mfma_f32_16x16x32_bf16 a[48:51], v[28:31], a[104:107], a[48:51]\n"
"v_mfma_f32_16x16x32_bf16 a[68:71], v[28:31], a[108:111], a[68:71]\n"
"v_mfma_f32_16x16x32_bf16 a[68:71], v[24:27], a[92:95], a[68:71]\n"
"s_waitcnt vmcnt(18) lgkmcnt(0)\n"
"s_barrier\n"
"v_mfma_f32_16x16x32_bf16 a[12:15], v[70:73], a[80:83], a[12:15]\n"
"ds_read_b128 v[24:27], v66 offset:40960\n"
"v_mfma_f32_16x16x32_bf16 a[12:15], v[74:77], a[96:99], a[12:15]\n"
"ds_read_b64_tr_b16 a[208:209], v64 offset:40960\n"
"v_mfma_f32_16x16x32_bf16 a[32:35], v[74:77], a[100:103], a[32:35]\n"
"ds_read_b64_tr_b16 a[210:211], v65 offset:40960\n"
"v_mfma_f32_16x16x32_bf16 a[32:35], v[70:73], a[84:87], a[32:35]\n"
"ds_read_b128 v[28:31], v66 offset:46080\n"
"v_mfma_f32_16x16x32_bf16 a[52:55], v[70:73], a[88:91], a[52:55]\n"
"ds_read_b64_tr_b16 a[224:225], v64 offset:45056\n"
"v_mfma_f32_16x16x32_bf16 a[52:55], v[74:77], a[104:107], a[52:55]\n"
"ds_read_b64_tr_b16 a[226:227], v65 offset:45056\n"
"v_mfma_f32_16x16x32_bf16 a[72:75], v[74:77], a[108:111], a[72:75]\n"
"ds_read_b64_tr_b16 a[228:229], v64 offset:46080\n"
"v_mfma_f32_16x16x32_bf16 a[72:75], v[70:73], a[92:95], a[72:75]\n"
"ds_read_b64_tr_b16 a[230:231], v65 offset:46080\n"
"v_mfma_f32_16x16x32_bf16 a[16:19], v[16:19], a[80:83], a[16:19]\n"
"ds_read_b64_tr_b16 a[212:213], v64 offset:41984\n"
"v_mfma_f32_16x16x32_bf16 a[16:19], v[20:23], a[96:99], a[16:19]\n"
"ds_read_b64_tr_b16 a[214:215], v65 offset:41984\n"
"v_mfma_f32_16x16x32_bf16 a[36:39], v[20:23], a[100:103], a[36:39]\n"
"ds_read_b64_tr_b16 a[216:217], v64 offset:43008\n"
"v_mfma_f32_16x16x32_bf16 a[36:39], v[16:19], a[84:87], a[36:39]\n"
"ds_read_b64_tr_b16 a[218:219], v65 offset:43008\n"
"v_mfma_f32_16x16x32_bf16 a[56:59], v[16:19], a[88:91], a[56:59]\n"
"ds_read_b64_tr_b16 a[232:233], v64 offset:47104\n"
"v_mfma_f32_16x16x32_bf16 a[56:59], v[20:23], a[104:107], a[56:59]\n"
"ds_read_b64_tr_b16 a[234:235], v65 offset:47104\n"
"v_mfma_f32_16x16x32_bf16 a[76:79], v[20:23], a[108:111], a[76:79]\n"
"ds_read_b64_tr_b16 a[236:237], v64 offset:48128\n"
"v_mfma_f32_16x16x32_bf16 a[76:79], v[16:19], a[92:95], a[76:79]\n"
"ds_read_b64_tr_b16 a[238:239], v65 offset:48128\n"
"s_waitcnt lgkmcnt(6)\n"
"v_mfma_f32_16x16x32_bf16 a[0:3], v[24:27], a[208:211], a[0:3]\n"
"ds_read_b64_tr_b16 a[220:221], v64 offset:44032\n"
"v_mfma_f32_16x16x32_bf16 a[0:3], v[28:31], a[224:227], a[0:3]\n"
"ds_read_b64_tr_b16 a[222:223], v65 offset:44032\n"
"v_mfma_f32_16x16x32_bf16 a[20:23], v[28:31], a[228:231], a[20:23]\n"
"ds_read_b128 v[70:73], v66 offset:41984\n"
"v_mfma_f32_16x16x32_bf16 a[20:23], v[24:27], a[212:215], a[20:23]\n"
"s_waitcnt lgkmcnt(3)\n"
"ds_read_b128 v[74:77], v66 offset:47104\n"
"v_mfma_f32_16x16x32_bf16 a[40:43], v[24:27], a[216:219], a[40:43]\n"
"s_mov_b32 s35, 0xa000\n"
"s_add_u32 m0, s82, 0x14000\n"
"s_nop 0\n"
"buffer_load_dwordx4 v12, s[12:15], s35 offen sc1 lds\n"
"v_mfma_f32_16x16x32_bf16 a[40:43], v[28:31], a[232:235], a[40:43]\n"
"s_waitcnt lgkmcnt(2)\n"
"buffer_load_dwordx4 v12, s[12:15], s35 offen offset:1024 sc1 lds\n"
"v_mfma_f32_16x16x32_bf16 a[60:63], v[28:31], a[236:239], a[60:63]\n"
"buffer_load_dwordx4 v12, s[12:15], s35 offen offset:2048 sc1 lds\n"
"v_mfma_f32_16x16x32_bf16 a[60:63], v[24:27], a[220:223], a[60:63]\n"
"s_waitcnt lgkmcnt(0)\n"
"v_mfma_f32_16x16x32_bf16 a[4:7], v[70:73], a[208:211], a[4:7]\n"
"ds_read_b128 v[16:19], v66 offset:43008\n"
"v_mfma_f32_16x16x32_bf16 a[4:7], v[74:77], a[224:227], a[4:7]\n"
"ds_read_b128 v[20:23], v66 offset:48128\n"
"v_mfma_f32_16x16x32_bf16 a[24:27], v[74:77], a[228:231], a[24:27]\n"
"buffer_load_dwordx4 v12, s[12:15], s35 offen offset:3072 sc1 lds\n"
"v_mfma_f32_16x16x32_bf16 a[24:27], v[70:73], a[212:215], a[24:27]\n"
"s_mov_b32 s31, 0xc800\n"
"s_add_u32 m0, s83, 0x14000\n"
"s_nop 0\n"
"buffer_load_dwordx4 v12, s[8:11], s31 offen sc1 lds\n"
"v_mfma_f32_16x16x32_bf16 a[44:47], v[70:73], a[216:219], a[44:47]\n"
"buffer_load_dwordx4 v12, s[8:11], s31 offen offset:1024 sc1 lds\n"
"v_mfma_f32_16x16x32_bf16 a[44:47], v[74:77], a[232:235], a[44:47]\n"
"buffer_load_dwordx4 v12, s[8:11], s31 offen offset:2048 sc1 lds\n"
"v_mfma_f32_16x16x32_bf16 a[64:67], v[74:77], a[236:239], a[64:67]\n"
"buffer_load_dwordx4 v12, s[8:11], s31 offen offset:3072 sc1 lds\n"
"v_mfma_f32_16x16x32_bf16 a[64:67], v[70:73], a[220:223], a[64:67]\n"
"s_waitcnt lgkmcnt(0)\n"
"v_mfma_f32_16x16x32_bf16 a[8:11], v[16:19], a[208:211], a[8:11]\n"
"ds_read_b128 v[24:27], v66 offset:44032\n"
"v_mfma_f32_16x16x32_bf16 a[8:11], v[20:23], a[224:227], a[8:11]\n"
"ds_read_b128 v[28:31], v66 offset:49152\n"
"v_mfma_f32_16x16x32_bf16 a[28:31], v[20:23], a[228:231], a[28:31]\n"
"ds_read_b128 v[70:73], v66 offset:45056\n"
"v_mfma_f32_16x16x32_bf16 a[28:31], v[16:19], a[212:215], a[28:31]\n"
"ds_read_b128 v[74:77], v66 offset:50176\n"
"v_mfma_f32_16x16x32_bf16 a[48:51], v[16:19], a[216:219], a[48:51]\n"
"s_add_u32 m0, s83, 0x15000\n"
"s_nop 0\n"
"buffer_load_dwordx4 v52, s[8:11], s31 offen sc1 lds\n"
"v_mfma_f32_16x16x32_bf16 a[48:51], v[20:23], a[232:235], a[48:51]\n"
"v_mfma_f32_16x16x32_bf16 a[68:71], v[20:23], a[236:239], a[68:71]\n"
"v_mfma_f32_16x16x32_bf16 a[68:71], v[16:19], a[220:223], a[68:71]\n"
"s_waitcnt vmcnt(18) lgkmcnt(0)\n"
"s_barrier\n"
"v_mfma_f32_16x16x32_bf16 a[12:15], v[24:27], a[208:211], a[12:15]\n"
"ds_read_b128 v[16:19], v66 offset:0\n"
"v_mfma_f32_16x16x32_bf16 a[12:15], v[28:31], a[224:227], a[12:15]\n"
"ds_read_b64_tr_b16 a[80:81], v64 offset:0\n"
"v_mfma_f32_16x16x32_bf16 a[32:35], v[28:31], a[228:231], a[32:35]\n"
"ds_read_b64_tr_b16 a[82:83], v65 offset:0\n"
"v_mfma_f32_16x16x32_bf16 a[32:35], v[24:27], a[212:215], a[32:35]\n"
"ds_read_b128 v[20:23], v66 offset:5120\n"
"v_mfma_f32_16x16x32_bf16 a[52:55], v[24:27], a[216:219], a[52:55]\n"
"ds_read_b64_tr_b16 a[96:97], v64 offset:4096\n"
"v_mfma_f32_16x16x32_bf16 a[52:55], v[28:31], a[232:235], a[52:55]\n"
"ds_read_b64_tr_b16 a[98:99], v65 offset:4096\n"
"v_mfma_f32_16x16x32_bf16 a[72:75], v[28:31], a[236:239], a[72:75]\n"
"ds_read_b64_tr_b16 a[100:101], v64 offset:5120\n"
"v_mfma_f32_16x16x32_bf16 a[72:75], v[24:27], a[220:223], a[72:75]\n"
"ds_read_b64_tr_b16 a[102:103], v65 offset:5120\n"
"v_mfma_f32_16x16x32_bf16 a[16:19], v[70:73], a[208:211], a[16:19]\n"
"ds_read_b64_tr_b16 a[84:85], v64 offset:1024\n"
"v_mfma_f32_16x16x32_bf16 a[16:19], v[74:77], a[224:227], a[16:19]\n"
"ds_read_b64_tr_b16 a[86:87], v65 offset:1024\n"
"v_mfma_f32_16x16x32_bf16 a[36:39], v[74:77], a[228:231], a[36:39]\n"
"ds_read_b64_tr_b16 a[88:89], v64 offset:2048\n"
"v_mfma_f32_16x16x32_bf16 a[36:39], v[70:73], a[212:215], a[36:39]\n"
"ds_read_b64_tr_b16 a[90:91], v65 offset:2048\n"
"v_mfma_f32_16x16x32_bf16 a[56:59], v[70:73], a[216:219], a[56:59]\n"
"ds_read_b64_tr_b16 a[104:105], v64 offset:6144\n"
"v_mfma_f32_16x16x32_bf16 a[56:59], v[74:77], a[232:235], a[56:59]\n"
"ds_read_b64_tr_b16 a[106:107], v65 offset:6144\n"
"v_mfma_f32_16x16x32_bf16 a[76:79], v[74:77], a[236:239], a[76:79]\n"
"ds_read_b64_tr_b16 a[108:109], v64 offset:7168\n"
"v_mfma_f32_16x16x32_bf16 a[76:79], v[70:73], a[220:223], a[76:79]\n"
"ds_read_b64_tr_b16 a[110:111], v65 offset:7168\n"
"s_waitcnt lgkmcnt(6)\n"
"v_mfma_f32_16x16x32_bf16 a[0:3], v[16:19], a[80:83], a[0:3]\n"
"ds_read_b64_tr_b16 a[92:93], v64 offset:3072\n"
"v_mfma_f32_16x16x32_bf16 a[0:3], v[20:23], a[96:99], a[0:3]\n"
"ds_read_b64_tr_b16 a[94:95], v65 offset:3072\n"
"v_mfma_f32_16x16x32_bf16 a[20:23], v[20:23], a[100:103], a[20:23]\n"
"ds_read_b128 v[24:27], v66 offset:1024\n"
"v_mfma_f32_16x16x32_bf16 a[20:23], v[16:19], a[84:87], a[20:23]\n"
"s_waitcnt lgkmcnt(3)\n"
"ds_read_b128 v[28:31], v66 offset:6144\n"
"v_mfma_f32_16x16x32_bf16 a[40:43], v[16:19], a[88:91], a[40:43]\n"
"s_mov_b32 s35, 0xc000\n"
"s_add_u32 m0, s82, 0xa000\n"
"s_nop 0\n"
"buffer_load_dwordx4 v12, s[12:15], s35 offen sc1 lds\n"
"v_mfma_f32_16x16x32_bf16 a[40:43], v[20:23], a[104:107], a[40:43]\n"
"s_waitcnt lgkmcnt(2)\n"
"buffer_load_dwordx4 v12, s[12:15], s35 offen offset:1024 sc1 lds\n"
"v_mfma_f32_16x16x32_bf16 a[60:63], v[20:23], a[108:111], a[60:63]\n"
"buffer_load_dwordx4 v12, s[12:15], s35 offen offset:2048 sc1 lds\n"
"v_mfma_f32_16x16x32_bf16 a[60:63], v[16:19], a[92:95], a[60:63]\n"
"s_waitcnt lgkmcnt(0)\n"
"v_mfma_f32_16x16x32_bf16 a[4:7], v[24:27], a[80:83], a[4:7]\n"
"ds_read_b128 v[70:73], v66 offset:2048\n"
"v_mfma_f32_16x16x32_bf16 a[4:7], v[28:31], a[96:99], a[4:7]\n"
"ds_read_b128 v[74:77], v66 offset:7168\n"
"v_mfma_f32_16x16x32_bf16 a[24:27], v[28:31], a[100:103], a[24:27]\n"
"buffer_load_dwordx4 v12, s[12:15], s35 offen offset:3072 sc1 lds\n"
"v_mfma_f32_16x16x32_bf16 a[24:27], v[24:27], a[84:87], a[24:27]\n"
"s_mov_b32 s31, 0xf000\n"
"s_add_u32 m0, s83, 0xa000\n"
"s_nop 0\n"
"buffer_load_dwordx4 v12, s[8:11], s31 offen sc1 lds\n"
"v_mfma_f32_16x16x32_bf16 a[44:47], v[24:27], a[88:91], a[44:47]\n"
"buffer_load_dwordx4 v12, s[8:11], s31 offen offset:1024 sc1 lds\n"
"v_mfma_f32_16x16x32_bf16 a[44:47], v[28:31], a[104:107], a[44:47]\n"
"buffer_load_dwordx4 v12, s[8:11], s31 offen offset:2048 sc1 lds\n"
"v_mfma_f32_16x16x32_bf16 a[64:67], v[28:31], a[108:111], a[64:67]\n"
"buffer_load_dwordx4 v12, s[8:11], s31 offen offset:3072 sc1 lds\n"
"v_mfma_f32_16x16x32_bf16 a[64:67], v[24:27], a[92:95], a[64:67]\n"
"s_waitcnt lgkmcnt(0)\n"
"v_mfma_f32_16x16x32_bf16 a[8:11], v[70:73], a[80:83], a[8:11]\n"
"ds_read_b128 v[16:19], v66 offset:3072\n"
"v_mfma_f32_16x16x32_bf16 a[8:11], v[74:77], a[96:99], a[8:11]\n"
"ds_read_b128 v[20:23], v66 offset:8192\n"
"v_mfma_f32_16x16x32_bf16 a[28:31], v[74:77], a[100:103], a[28:31]\n"
"ds_read_b128 v[24:27], v66 offset:4096\n"
"v_mfma_f32_16x16x32_bf16 a[28:31], v[70:73], a[84:87], a[28:31]\n"
"ds_read_b128 v[28:31], v66 offset:9216\n"
"v_mfma_f32_16x16x32_bf16 a[48:51], v[70:73], a[88:91], a[48:51]\n"
"s_add_u32 m0, s83, 0xb000\n"
"s_nop 0\n"
"buffer_load_dwordx4 v52, s[8:11], s31 offen sc1 lds\n"
"v_mfma_f32_16x16x32_bf16 a[48:51], v[74:77], a[104:107], a[48:51]\n"
"v_mfma_f32_16x16x32_bf16 a[68:71], v[74:77], a[108:111], a[68:71]\n"
"v_mfma_f32_16x16x32_bf16 a[68:71], v[70:73], a[92:95], a[68:71]\n"
"s_waitcnt vmcnt(18) lgkmcnt(0)\n"
"s_barrier\n"
"v_mfma_f32_16x16x32_bf16 a[12:15], v[16:19], a[80:83], a[12:15]\n"
"ds_read_b128 v[70:73], v67 offset:40960\n"
"v_mfma_f32_16x16x32_bf16 a[12:15], v[20:23], a[96:99], a[12:15]\n"
"ds_read_b64_tr_b16 a[208:209], v78 offset:40960\n"
"v_mfma_f32_16x16x32_bf16 a[32:35], v[20:23], a[100:103], a[32:35]\n"
"ds_read_b64_tr_b16 a[210:211], v79 offset:40960\n"
"v_mfma_f32_16x16x32_bf16 a[32:35], v[16:19], a[84:87], a[32:35]\n"
"ds_read_b128 v[74:77], v67 offset:46080\n"
"v_mfma_f32_16x16x32_bf16 a[52:55], v[16:19], a[88:91], a[52:55]\n"
"ds_read_b64_tr_b16 a[224:225], v78 offset:45056\n"
"v_mfma_f32_16x16x32_bf16 a[52:55], v[20:23], a[104:107], a[52:55]\n"
"ds_read_b64_tr_b16 a[226:227], v79 offset:45056\n"
"v_mfma_f32_16x16x32_bf16 a[72:75], v[20:23], a[108:111], a[72:75]\n"
"ds_read_b64_tr_b16 a[228:229], v78 offset:46080\n"
"v_mfma_f32_16x16x32_bf16 a[72:75], v[16:19], a[92:95], a[72:75]\n"
"ds_read_b64_tr_b16 a[230:231], v79 offset:46080\n"
"v_mfma_f32_16x16x32_bf16 a[16:19], v[24:27], a[80:83], a[16:19]\n"
"ds_read_b64_tr_b16 a[212:213], v78 offset:41984\n"
"v_mfma_f32_16x16x32_bf16 a[16:19], v[28:31], a[96:99], a[16:19]\n"
"ds_read_b64_tr_b16 a[214:215], v79 offset:41984\n"
"v_mfma_f32_16x16x32_bf16 a[36:39], v[28:31], a[100:103], a[36:39]\n"
"ds_read_b64_tr_b16 a[216:217], v78 offset:43008\n"
"v_mfma_f32_16x16x32_bf16 a[36:39], v[24:27], a[84:87], a[36:39]\n"
"ds_read_b64_tr_b16 a[218:219], v79 offset:43008\n"
"v_mfma_f32_16x16x32_bf16 a[56:59], v[24:27], a[88:91], a[56:59]\n"
"ds_read_b64_tr_b16 a[232:233], v78 offset:47104\n"
"v_mfma_f32_16x16x32_bf16 a[56:59], v[28:31], a[104:107], a[56:59]\n"
"ds_read_b64_tr_b16 a[234:235], v79 offset:47104\n"
"v_mfma_f32_16x16x32_bf16 a[76:79], v[28:31], a[108:111], a[76:79]\n"
"ds_read_b64_tr_b16 a[236:237], v78 offset:48128\n"
"v_mfma_f32_16x16x32_bf16 a[76:79], v[24:27], a[92:95], a[76:79]\n"
"ds_read_b64_tr_b16 a[238:239], v79 offset:48128\n"
"s_waitcnt lgkmcnt(6)\n"
"v_mfma_f32_16x16x32_bf16 a[0:3], v[70:73], a[208:211], a[0:3]\n"
"ds_read_b64_tr_b16 a[220:221], v78 offset:44032\n"
"v_mfma_f32_16x16x32_bf16 a[0:3], v[74:77], a[224:227], a[0:3]\n"
"ds_read_b64_tr_b16 a[222:223], v79 offset:44032\n"
"v_mfma_f32_16x16x32_bf16 a[20:23], v[74:77], a[228:231], a[20:23]\n"
"ds_read_b128 v[16:19], v67 offset:41984\n"
"v_mfma_f32_16x16x32_bf16 a[20:23], v[70:73], a[212:215], a[20:23]\n"
"s_waitcnt lgkmcnt(3)\n"
"ds_read_b128 v[20:23], v67 offset:47104\n"
"v_mfma_f32_16x16x32_bf16 a[40:43], v[70:73], a[216:219], a[40:43]\n"
"s_mov_b32 s35, 0xe000\n"
"s_add_u32 m0, s82, 0x0\n"
"s_nop 0\n"
"buffer_load_dwordx4 v12, s[12:15], s35 offen sc1 lds\n"
"v_mfma_f32_16x16x32_bf16 a[40:43], v[74:77], a[232:235], a[40:43]\n"
"s_waitcnt lgkmcnt(2)\n"
"buffer_load_dwordx4 v12, s[12:15], s35 offen offset:1024 sc1 lds\n"
"v_mfma_f32_16x16x32_bf16 a[60:63], v[74:77], a[236:239], a[60:63]\n"
"buffer_load_dwordx4 v12, s[12:15], s35 offen offset:2048 sc1 lds\n"
"v_mfma_f32_16x16x32_bf16 a[60:63], v[70:73], a[220:223], a[60:63]\n"
"s_waitcnt lgkmcnt(0)\n"
"v_mfma_f32_16x16x32_bf16 a[4:7], v[16:19], a[208:211], a[4:7]\n"
"ds_read_b128 v[24:27], v67 offset:43008\n"
"v_mfma_f32_16x16x32_bf16 a[4:7], v[20:23], a[224:227], a[4:7]\n"
"ds_read_b128 v[28:31], v67 offset:48128\n"
"v_mfma_f32_16x16x32_bf16 a[24:27], v[20:23], a[228:231], a[24:27]\n"
"buffer_load_dwordx4 v12, s[12:15], s35 offen offset:3072 sc1 lds\n"
"v_mfma_f32_16x16x32_bf16 a[24:27], v[16:19], a[212:215], a[24:27]\n"
"s_mov_b32 s31, 0x11800\n"
"s_add_u32 m0, s83, 0x0\n"
"s_nop 0\n"
"buffer_load_dwordx4 v12, s[8:11], s31 offen sc1 lds\n"
"v_mfma_f32_16x16x32_bf16 a[44:47], v[16:19], a[216:219], a[44:47]\n"
"buffer_load_dwordx4 v12, s[8:11], s31 offen offset:1024 sc1 lds\n"
"v_mfma_f32_16x16x32_bf16 a[44:47], v[20:23], a[232:235], a[44:47]\n"
"buffer_load_dwordx4 v12, s[8:11], s31 offen offset:2048 sc1 lds\n"
"v_mfma_f32_16x16x32_bf16 a[64:67], v[20:23], a[236:239], a[64:67]\n"
"buffer_load_dwordx4 v12, s[8:11], s31 offen offset:3072 sc1 lds\n"
"v_mfma_f32_16x16x32_bf16 a[64:67], v[16:19], a[220:223], a[64:67]\n"
"s_waitcnt lgkmcnt(0)\n"
"v_mfma_f32_16x16x32_bf16 a[8:11], v[24:27], a[208:211], a[8:11]\n"
"ds_read_b128 v[70:73], v67 offset:44032\n"
"v_mfma_f32_16x16x32_bf16 a[8:11], v[28:31], a[224:227], a[8:11]\n"
"ds_read_b128 v[74:77], v67 offset:49152\n"
"v_mfma_f32_16x16x32_bf16 a[28:31], v[28:31], a[228:231], a[28:31]\n"
"ds_read_b128 v[16:19], v67 offset:45056\n"
"v_mfma_f32_16x16x32_bf16 a[28:31], v[24:27], a[212:215], a[28:31]\n"
"ds_read_b128 v[20:23], v67 offset:50176\n"
"v_mfma_f32_16x16x32_bf16 a[48:51], v[24:27], a[216:219], a[48:51]\n"
"s_add_u32 m0, s83, 0x1000\n"
"s_nop 0\n"
"buffer_load_dwordx4 v52, s[8:11], s31 offen sc1 lds\n"
"v_mfma_f32_16x16x32_bf16 a[48:51], v[28:31], a[232:235], a[48:51]\n"
"v_mfma_f32_16x16x32_bf16 a[68:71], v[28:31], a[236:239], a[68:71]\n"
"v_mfma_f32_16x16x32_bf16 a[68:71], v[24:27], a[220:223], a[68:71]\n"
"s_waitcnt vmcnt(18) lgkmcnt(0)\n"
"s_barrier\n"
"v_mfma_f32_16x16x32_bf16 a[12:15], v[70:73], a[208:211], a[12:15]\n"
"ds_read_b128 v[24:27], v67 offset:0\n"
"v_mfma_f32_16x16x32_bf16 a[12:15], v[74:77], a[224:227], a[12:15]\n"
"ds_read_b64_tr_b16 a[80:81], v78 offset:0\n"
"v_mfma_f32_16x16x32_bf16 a[32:35], v[74:77], a[228:231], a[32:35]\n"
"ds_read_b64_tr_b16 a[82:83], v79 offset:0\n"
"v_mfma_f32_16x16x32_bf16 a[32:35], v[70:73], a[212:215], a[32:35]\n"
"ds_read_b128 v[28:31], v67 offset:5120\n"
"v_mfma_f32_16x16x32_bf16 a[52:55], v[70:73], a[216:219], a[52:55]\n"
"ds_read_b64_tr_b16 a[96:97], v78 offset:4096\n"
"v_mfma_f32_16x16x32_bf16 a[52:55], v[74:77], a[232:235], a[52:55]\n"
"ds_read_b64_tr_b16 a[98:99], v79 offset:4096\n"
"v_mfma_f32_16x16x32_bf16 a[72:75], v[74:77], a[236:239], a[72:75]\n"
"ds_read_b64_tr_b16 a[100:101], v78 offset:5120\n"
"v_mfma_f32_16x16x32_bf16 a[72:75], v[70:73], a[220:223], a[72:75]\n"
"ds_read_b64_tr_b16 a[102:103], v79 offset:5120\n"
"v_mfma_f32_16x16x32_bf16 a[16:19], v[16:19], a[208:211], a[16:19]\n"
"ds_read_b64_tr_b16 a[84:85], v78 offset:1024\n"
"v_mfma_f32_16x16x32_bf16 a[16:19], v[20:23], a[224:227], a[16:19]\n"
"ds_read_b64_tr_b16 a[86:87], v79 offset:1024\n"
"v_mfma_f32_16x16x32_bf16 a[36:39], v[20:23], a[228:231], a[36:39]\n"
"ds_read_b64_tr_b16 a[88:89], v78 offset:2048\n"
"v_mfma_f32_16x16x32_bf16 a[36:39], v[16:19], a[212:215], a[36:39]\n"
"ds_read_b64_tr_b16 a[90:91], v79 offset:2048\n"
"v_mfma_f32_16x16x32_bf16 a[56:59], v[16:19], a[216:219], a[56:59]\n"
"ds_read_b64_tr_b16 a[104:105], v78 offset:6144\n"
"v_mfma_f32_16x16x32_bf16 a[56:59], v[20:23], a[232:235], a[56:59]\n"
"ds_read_b64_tr_b16 a[106:107], v79 offset:6144\n"
"v_mfma_f32_16x16x32_bf16 a[76:79], v[20:23], a[236:239], a[76:79]\n"
"ds_read_b64_tr_b16 a[108:109], v78 offset:7168\n"
"v_mfma_f32_16x16x32_bf16 a[76:79], v[16:19], a[220:223], a[76:79]\n"
"ds_read_b64_tr_b16 a[110:111], v79 offset:7168\n"
"s_waitcnt lgkmcnt(6)\n"
"v_mfma_f32_16x16x32_bf16 a[0:3], v[24:27], a[80:83], a[0:3]\n"
"ds_read_b64_tr_b16 a[92:93], v78 offset:3072\n"
"v_mfma_f32_16x16x32_bf16 a[0:3], v[28:31], a[96:99], a[0:3]\n"
"ds_read_b64_tr_b16 a[94:95], v79 offset:3072\n"
"v_mfma_f32_16x16x32_bf16 a[20:23], v[28:31], a[100:103], a[20:23]\n"
"ds_read_b128 v[70:73], v67 offset:1024\n"
"v_mfma_f32_16x16x32_bf16 a[20:23], v[24:27], a[84:87], a[20:23]\n"
"s_waitcnt lgkmcnt(3)\n"
"ds_read_b128 v[74:77], v67 offset:6144\n"
"v_mfma_f32_16x16x32_bf16 a[40:43], v[24:27], a[88:91], a[40:43]\n"
"s_mov_b32 s35, 0x10000\n"
"s_add_u32 m0, s82, 0x1e000\n"
"s_nop 0\n"
"buffer_load_dwordx4 v12, s[12:15], s35 offen sc1 lds\n"
"v_mfma_f32_16x16x32_bf16 a[40:43], v[28:31], a[104:107], a[40:43]\n"
"s_waitcnt lgkmcnt(2)\n"
"buffer_load_dwordx4 v12, s[12:15], s35 offen offset:1024 sc1 lds\n"
"v_mfma_f32_16x16x32_bf16 a[60:63], v[28:31], a[108:111], a[60:63]\n"
"buffer_load_dwordx4 v12, s[12:15], s35 offen offset:2048 sc1 lds\n"
"v_mfma_f32_16x16x32_bf16 a[60:63], v[24:27], a[92:95], a[60:63]\n"
"s_waitcnt lgkmcnt(0)\n"
"v_mfma_f32_16x16x32_bf16 a[4:7], v[70:73], a[80:83], a[4:7]\n"
"ds_read_b128 v[16:19], v67 offset:2048\n"
"v_mfma_f32_16x16x32_bf16 a[4:7], v[74:77], a[96:99], a[4:7]\n"
"ds_read_b128 v[20:23], v67 offset:7168\n"
"v_mfma_f32_16x16x32_bf16 a[24:27], v[74:77], a[100:103], a[24:27]\n"
"buffer_load_dwordx4 v12, s[12:15], s35 offen offset:3072 sc1 lds\n"
"v_mfma_f32_16x16x32_bf16 a[24:27], v[70:73], a[84:87], a[24:27]\n"
"s_mov_b32 s31, 0x14000\n"
"s_add_u32 m0, s83, 0x1e000\n"
"s_nop 0\n"
"buffer_load_dwordx4 v12, s[8:11], s31 offen sc1 lds\n"
"v_mfma_f32_16x16x32_bf16 a[44:47], v[70:73], a[88:91], a[44:47]\n"
"buffer_load_dwordx4 v12, s[8:11], s31 offen offset:1024 sc1 lds\n"
"v_mfma_f32_16x16x32_bf16 a[44:47], v[74:77], a[104:107], a[44:47]\n"
"buffer_load_dwordx4 v12, s[8:11], s31 offen offset:2048 sc1 lds\n"
"v_mfma_f32_16x16x32_bf16 a[64:67], v[74:77], a[108:111], a[64:67]\n"
"buffer_load_dwordx4 v12, s[8:11], s31 offen offset:3072 sc1 lds\n"
"v_mfma_f32_16x16x32_bf16 a[64:67], v[70:73], a[92:95], a[64:67]\n"
"s_waitcnt lgkmcnt(0)\n"
"v_mfma_f32_16x16x32_bf16 a[8:11], v[16:19], a[80:83], a[8:11]\n"
"ds_read_b128 v[24:27], v67 offset:3072\n"
"v_mfma_f32_16x16x32_bf16 a[8:11], v[20:23], a[96:99], a[8:11]\n"
"ds_read_b128 v[28:31], v67 offset:8192\n"
"v_mfma_f32_16x16x32_bf16 a[28:31], v[20:23], a[100:103], a[28:31]\n"
"ds_read_b128 v[70:73], v67 offset:4096\n"
"v_mfma_f32_16x16x32_bf16 a[28:31], v[16:19], a[84:87], a[28:31]\n"
"ds_read_b128 v[74:77], v67 offset:9216\n"
"v_mfma_f32_16x16x32_bf16 a[48:51], v[16:19], a[88:91], a[48:51]\n"
"s_add_u32 m0, s83, 0x1f000\n"
"s_nop 0\n"
"buffer_load_dwordx4 v52, s[8:11], s31 offen sc1 lds\n"
"v_mfma_f32_16x16x32_bf16 a[48:51], v[20:23], a[104:107], a[48:51]\n"
"v_mfma_f32_16x16x32_bf16 a[68:71], v[20:23], a[108:111], a[68:71]\n"
"v_mfma_f32_16x16x32_bf16 a[68:71], v[16:19], a[92:95], a[68:71]\n"
"s_waitcnt vmcnt(18) lgkmcnt(0)\n"
"s_barrier\n"
"v_mfma_f32_16x16x32_bf16 a[12:15], v[24:27], a[80:83], a[12:15]\n"
"ds_read_b128 v[16:19], v66 offset:40960\n"
"v_mfma_f32_16x16x32_bf16 a[12:15], v[28:31], a[96:99], a[12:15]\n"
"ds_read_b64_tr_b16 a[208:209], v64 offset:40960\n"
"v_mfma_f32_16x16x32_bf16 a[32:35], v[28:31], a[100:103], a[32:35]\n"
"ds_read_b64_tr_b16 a[210:211], v65 offset:40960\n"
"v_mfma_f32_16x16x32_bf16 a[32:35], v[24:27], a[84:87], a[32:35]\n"
"ds_read_b128 v[20:23], v66 offset:46080\n"
"v_mfma_f32_16x16x32_bf16 a[52:55], v[24:27], a[88:91], a[52:55]\n"
"ds_read_b64_tr_b16 a[224:225], v64 offset:45056\n"
"v_mfma_f32_16x16x32_bf16 a[52:55], v[28:31], a[104:107], a[52:55]\n"
"ds_read_b64_tr_b16 a[226:227], v65 offset:45056\n"
"v_mfma_f32_16x16x32_bf16 a[72:75], v[28:31], a[108:111], a[72:75]\n"
"ds_read_b64_tr_b16 a[228:229], v64 offset:46080\n"
"v_mfma_f32_16x16x32_bf16 a[72:75], v[24:27], a[92:95], a[72:75]\n"
"ds_read_b64_tr_b16 a[230:231], v65 offset:46080\n"
"v_mfma_f32_16x16x32_bf16 a[16:19], v[70:73], a[80:83], a[16:19]\n"
"ds_read_b64_tr_b16 a[212:213], v64 offset:41984\n"
"v_mfma_f32_16x16x32_bf16 a[16:19], v[74:77], a[96:99], a[16:19]\n"
"ds_read_b64_tr_b16 a[214:215], v65 offset:41984\n"
"v_mfma_f32_16x16x32_bf16 a[36:39], v[74:77], a[100:103], a[36:39]\n"
"ds_read_b64_tr_b16 a[216:217], v64 offset:43008\n"
"v_mfma_f32_16x16x32_bf16 a[36:39], v[70:73], a[84:87], a[36:39]\n"
"ds_read_b64_tr_b16 a[218:219], v65 offset:43008\n"
"v_mfma_f32_16x16x32_bf16 a[56:59], v[70:73], a[88:91], a[56:59]\n"
"ds_read_b64_tr_b16 a[232:233], v64 offset:47104\n"
"v_mfma_f32_16x16x32_bf16 a[56:59], v[74:77], a[104:107], a[56:59]\n"
"ds_read_b64_tr_b16 a[234:235], v65 offset:47104\n"
"v_mfma_f32_16x16x32_bf16 a[76:79], v[74:77], a[108:111], a[76:79]\n"
"ds_read_b64_tr_b16 a[236:237], v64 offset:48128\n"
"v_mfma_f32_16x16x32_bf16 a[76:79], v[70:73], a[92:95], a[76:79]\n"
"ds_read_b64_tr_b16 a[238:239], v65 offset:48128\n"
"s_waitcnt lgkmcnt(6)\n"
"v_mfma_f32_16x16x32_bf16 a[0:3], v[16:19], a[208:211], a[0:3]\n"
"ds_read_b64_tr_b16 a[220:221], v64 offset:44032\n"
"v_add_u32 v221, 0, v12\n"
"s_mul_i32 s60, s90, 10240\n"
"v_mfma_f32_16x16x32_bf16 a[0:3], v[20:23], a[224:227], a[0:3]\n"
"ds_read_b64_tr_b16 a[222:223], v65 offset:44032\n"
"v_add_u32 v82, s60, v221\n"
"s_add_u32 s60, s63, 81920\n"
"v_mfma_f32_16x16x32_bf16 a[20:23], v[20:23], a[228:231], a[20:23]\n"
"ds_read_b128 v[24:27], v66 offset:41984\n"
"v_add_u32 v104, s60, v221\n"
"s_sub_u32 s60, s90, 1\n"
"v_mfma_f32_16x16x32_bf16 a[20:23], v[16:19], a[212:215], a[20:23]\n"
"s_waitcnt lgkmcnt(3)\n"
"ds_read_b128 v[28:31], v66 offset:47104\n"
"s_and_b32 s60, s60, 3\n"
"s_mul_i32 s60, s60, 10240\n"
"v_mfma_f32_16x16x32_bf16 a[40:43], v[16:19], a[216:219], a[40:43]\n"
"s_mov_b32 s35, 0x12000\n"
"s_add_u32 m0, s82, 0x14000\n"
"s_nop 0\n"
"buffer_load_dwordx4 v12, s[12:15], s35 offen sc1 lds\n"
"s_add_u32 s60, s60, 0\n"
"v_add_u32 v85, s60, v221\n"
"v_mfma_f32_16x16x32_bf16 a[40:43], v[20:23], a[232:235], a[40:43]\n"
"s_waitcnt lgkmcnt(2)\n"
"buffer_load_dwordx4 v12, s[12:15], s35 offen offset:1024 sc1 lds\n"
"s_sub_u32 s60, s90, 2\n"
"s_and_b32 s60, s60, 3\n"
"v_mfma_f32_16x16x32_bf16 a[60:63], v[20:23], a[236:239], a[60:63]\n"
"buffer_load_dwordx4 v12, s[12:15], s35 offen offset:2048 sc1 lds\n"
"s_mul_i32 s60, s60, 10240\n"
"s_add_u32 s60, s60, 5120\n"
"v_mfma_f32_16x16x32_bf16 a[60:63], v[16:19], a[220:223], a[60:63]\n"
"s_waitcnt lgkmcnt(0)\n"
"v_add_u32 v86, s60, v221\n"
"s_sub_u32 s60, s90, 3\n"
"v_mfma_f32_16x16x32_bf16 a[4:7], v[24:27], a[208:211], a[4:7]\n"
"ds_read_b128 v[70:73], v66 offset:43008\n"
"s_and_b32 s60, s60, 3\n"
"s_mul_i32 s60, s60, 5120\n"
"v_mfma_f32_16x16x32_bf16 a[4:7], v[28:31], a[224:227], a[4:7]\n"
"ds_read_b128 v[74:77], v66 offset:48128\n"
"s_add_u32 s61, s60, 81920\n"
"v_add_u32 v106, s61, v221\n"
"v_mfma_f32_16x16x32_bf16 a[24:27], v[28:31], a[228:231], a[24:27]\n"
"buffer_load_dwordx4 v12, s[12:15], s35 offen offset:3072 sc1 lds\n"
"s_lshl_b32 s60, s98, 2\n"
"s_add_u32 s60, s60, s90\n"
"v_mfma_f32_16x16x32_bf16 a[24:27], v[24:27], a[212:215], a[24:27]\n"
"s_mov_b32 s31, 0x16800\n"
"s_add_u32 m0, s83, 0x14000\n"
"s_nop 0\n"
"buffer_load_dwordx4 v12, s[8:11], s31 offen sc1 lds\n"
"s_mul_i32 s60, s60, 20480\n"
"s_mul_i32 s61, s99, 160\n"
"v_mfma_f32_16x16x32_bf16 a[44:47], v[24:27], a[216:219], a[44:47]\n"
"buffer_load_dwordx4 v12, s[8:11], s31 offen offset:1024 sc1 lds\n"
"s_add_u32 s60, s60, s61\n"
"s_add_u32 s24, s54, s60\n"
"s_addc_u32 s25, s55, 0\n"
"v_mfma_f32_16x16x32_bf16 a[44:47], v[28:31], a[232:235], a[44:47]\n"
"buffer_load_dwordx4 v12, s[8:11], s31 offen offset:2048 sc1 lds\n"
"s_mov_b32 s26, 0x4ba0\n"
"s_mov_b32 s27, 0x20000\n"
"v_mfma_f32_16x16x32_bf16 a[64:67], v[28:31], a[236:239], a[64:67]\n"
"buffer_load_dwordx4 v12, s[8:11], s31 offen offset:3072 sc1 lds\n"
"v_mfma_f32_16x16x32_bf16 a[64:67], v[24:27], a[220:223], a[64:67]\n"
"s_waitcnt lgkmcnt(0)\n"
"v_mfma_f32_16x16x32_bf16 a[8:11], v[70:73], a[208:211], a[8:11]\n"
"ds_read_b128 v[16:19], v66 offset:44032\n"
"v_mfma_f32_16x16x32_bf16 a[8:11], v[74:77], a[224:227], a[8:11]\n"
"ds_read_b128 v[20:23], v66 offset:49152\n"
"v_mfma_f32_16x16x32_bf16 a[28:31], v[74:77], a[228:231], a[28:31]\n"
"ds_read_b128 v[24:27], v66 offset:45056\n"
"v_mfma_f32_16x16x32_bf16 a[28:31], v[70:73], a[212:215], a[28:31]\n"
"ds_read_b128 v[28:31], v66 offset:50176\n"
"v_mfma_f32_16x16x32_bf16 a[48:51], v[70:73], a[216:219], a[48:51]\n"
"s_add_u32 m0, s83, 0x15000\n"
"s_nop 0\n"
"buffer_load_dwordx4 v52, s[8:11], s31 offen sc1 lds\n"
"v_mfma_f32_16x16x32_bf16 a[48:51], v[74:77], a[232:235], a[48:51]\n"
"v_mfma_f32_16x16x32_bf16 a[68:71], v[74:77], a[236:239], a[68:71]\n"
"v_mfma_f32_16x16x32_bf16 a[68:71], v[70:73], a[220:223], a[68:71]\n"
"s_waitcnt vmcnt(18) lgkmcnt(0)\n"
"s_barrier\n"
"v_mfma_f32_16x16x32_bf16 a[12:15], v[16:19], a[208:211], a[12:15]\n"
"ds_read_b128 v[70:73], v66 offset:0\n"
"v_mfma_f32_16x16x32_bf16 a[12:15], v[20:23], a[224:227], a[12:15]\n"
"ds_read_b64_tr_b16 a[80:81], v64 offset:0\n"
"v_mfma_f32_16x16x32_bf16 a[32:35], v[20:23], a[228:231], a[32:35]\n"
"ds_read_b64_tr_b16 a[82:83], v65 offset:0\n"
"v_mfma_f32_16x16x32_bf16 a[32:35], v[16:19], a[212:215], a[32:35]\n"
"ds_read_b128 v[74:77], v66 offset:5120\n"
"v_mfma_f32_16x16x32_bf16 a[52:55], v[16:19], a[216:219], a[52:55]\n"
"ds_read_b64_tr_b16 a[96:97], v64 offset:4096\n"
"v_mfma_f32_16x16x32_bf16 a[52:55], v[20:23], a[232:235], a[52:55]\n"
"ds_read_b64_tr_b16 a[98:99], v65 offset:4096\n"
"v_mfma_f32_16x16x32_bf16 a[72:75], v[20:23], a[236:239], a[72:75]\n"
"ds_read_b64_tr_b16 a[100:101], v64 offset:5120\n"
"v_mfma_f32_16x16x32_bf16 a[72:75], v[16:19], a[220:223], a[72:75]\n"
"ds_read_b64_tr_b16 a[102:103], v65 offset:5120\n"
"v_mfma_f32_16x16x32_bf16 a[16:19], v[24:27], a[208:211], a[16:19]\n"
"ds_read_b64_tr_b16 a[84:85], v64 offset:1024\n"
"v_mfma_f32_16x16x32_bf16 a[16:19], v[28:31], a[224:227], a[16:19]\n"
"ds_read_b64_tr_b16 a[86:87], v65 offset:1024\n"
"v_mfma_f32_16x16x32_bf16 a[36:39], v[28:31], a[228:231], a[36:39]\n"
"ds_read_b64_tr_b16 a[88:89], v64 offset:2048\n"
"v_mfma_f32_16x16x32_bf16 a[36:39], v[24:27], a[212:215], a[36:39]\n"
"ds_read_b64_tr_b16 a[90:91], v65 offset:2048\n"
"v_mfma_f32_16x16x32_bf16 a[56:59], v[24:27], a[216:219], a[56:59]\n"
"ds_read_b64_tr_b16 a[104:105], v64 offset:6144\n"
"v_mfma_f32_16x16x32_bf16 a[56:59], v[28:31], a[232:235], a[56:59]\n"
"ds_read_b64_tr_b16 a[106:107], v65 offset:6144\n"
"v_mfma_f32_16x16x32_bf16 a[76:79], v[28:31], a[236:239], a[76:79]\n"
"ds_read_b64_tr_b16 a[108:109], v64 offset:7168\n"
"v_mfma_f32_16x16x32_bf16 a[76:79], v[24:27], a[220:223], a[76:79]\n"
"ds_read_b64_tr_b16 a[110:111], v65 offset:7168\n"
"s_waitcnt lgkmcnt(6)\n"
"v_mfma_f32_16x16x32_bf16 a[0:3], v[70:73], a[80:83], a[0:3]\n"
"ds_read_b64_tr_b16 a[92:93], v64 offset:3072\n"
"v_mfma_f32_16x16x32_bf16 a[0:3], v[74:77], a[96:99], a[0:3]\n"
"ds_read_b64_tr_b16 a[94:95], v65 offset:3072\n"
"v_mfma_f32_16x16x32_bf16 a[20:23], v[74:77], a[100:103], a[20:23]\n"
"ds_read_b128 v[16:19], v66 offset:1024\n"
"v_mfma_f32_16x16x32_bf16 a[20:23], v[70:73], a[84:87], a[20:23]\n"
"s_waitcnt lgkmcnt(3)\n"
"ds_read_b128 v[20:23], v66 offset:6144\n"
"v_mfma_f32_16x16x32_bf16 a[40:43], v[70:73], a[88:91], a[40:43]\n"
"s_mov_b32 s35, 0x14000\n"
"s_add_u32 m0, s82, 0xa000\n"
"s_nop 0\n"
"buffer_load_dwordx4 v12, s[12:15], s35 offen sc1 lds\n"
"v_mfma_f32_16x16x32_bf16 a[40:43], v[74:77], a[104:107], a[40:43]\n"
"s_waitcnt lgkmcnt(2)\n"
"buffer_load_dwordx4 v12, s[12:15], s35 offen offset:1024 sc1 lds\n"
"v_mfma_f32_16x16x32_bf16 a[60:63], v[74:77], a[108:111], a[60:63]\n"
"buffer_load_dwordx4 v12, s[12:15], s35 offen offset:2048 sc1 lds\n"
"v_mfma_f32_16x16x32_bf16 a[60:63], v[70:73], a[92:95], a[60:63]\n"
"s_waitcnt lgkmcnt(0)\n"
"v_mfma_f32_16x16x32_bf16 a[4:7], v[16:19], a[80:83], a[4:7]\n"
"ds_read_b128 v[24:27], v66 offset:2048\n"
"v_mfma_f32_16x16x32_bf16 a[4:7], v[20:23], a[96:99], a[4:7]\n"
"ds_read_b128 v[28:31], v66 offset:7168\n"
"v_mfma_f32_16x16x32_bf16 a[24:27], v[20:23], a[100:103], a[24:27]\n"
"buffer_load_dwordx4 v12, s[12:15], s35 offen offset:3072 sc1 lds\n"
"v_mfma_f32_16x16x32_bf16 a[24:27], v[16:19], a[84:87], a[24:27]\n"
"s_mov_b32 s31, 0x19000\n"
"s_add_u32 m0, s83, 0xa000\n"
"s_nop 0\n"
"buffer_load_dwordx4 v12, s[8:11], s31 offen sc1 lds\n"
"v_mfma_f32_16x16x32_bf16 a[44:47], v[16:19], a[88:91], a[44:47]\n"
"buffer_load_dwordx4 v12, s[8:11], s31 offen offset:1024 sc1 lds\n"
"v_mfma_f32_16x16x32_bf16 a[44:47], v[20:23], a[104:107], a[44:47]\n"
"buffer_load_dwordx4 v12, s[8:11], s31 offen offset:2048 sc1 lds\n"
"v_mfma_f32_16x16x32_bf16 a[64:67], v[20:23], a[108:111], a[64:67]\n"
"buffer_load_dwordx4 v12, s[8:11], s31 offen offset:3072 sc1 lds\n"
"v_mfma_f32_16x16x32_bf16 a[64:67], v[16:19], a[92:95], a[64:67]\n"
"s_waitcnt lgkmcnt(0)\n"
"v_mfma_f32_16x16x32_bf16 a[8:11], v[24:27], a[80:83], a[8:11]\n"
"ds_read_b128 v[70:73], v66 offset:3072\n"
"v_mfma_f32_16x16x32_bf16 a[8:11], v[28:31], a[96:99], a[8:11]\n"
"ds_read_b128 v[74:77], v66 offset:8192\n"
"v_mfma_f32_16x16x32_bf16 a[28:31], v[28:31], a[100:103], a[28:31]\n"
"ds_read_b128 v[16:19], v66 offset:4096\n"
"v_mfma_f32_16x16x32_bf16 a[28:31], v[24:27], a[84:87], a[28:31]\n"
"ds_read_b128 v[20:23], v66 offset:9216\n"
"v_mfma_f32_16x16x32_bf16 a[48:51], v[24:27], a[88:91], a[48:51]\n"
"s_add_u32 m0, s83, 0xb000\n"
"s_nop 0\n"
"buffer_load_dwordx4 v52, s[8:11], s31 offen sc1 lds\n"
"v_mfma_f32_16x16x32_bf16 a[48:51], v[28:31], a[104:107], a[48:51]\n"
"v_mfma_f32_16x16x32_bf16 a[68:71], v[28:31], a[108:111], a[68:71]\n"
"v_mfma_f32_16x16x32_bf16 a[68:71], v[24:27], a[92:95], a[68:71]\n"
"s_waitcnt vmcnt(18) lgkmcnt(0)\n"
"s_barrier\n"
"v_mfma_f32_16x16x32_bf16 a[12:15], v[70:73], a[80:83], a[12:15]\n"
"ds_read_b128 v[24:27], v67 offset:40960\n"
"v_mfma_f32_16x16x32_bf16 a[12:15], v[74:77], a[96:99], a[12:15]\n"
"ds_read_b64_tr_b16 a[208:209], v78 offset:40960\n"
"v_mfma_f32_16x16x32_bf16 a[32:35], v[74:77], a[100:103], a[32:35]\n"
"ds_read_b64_tr_b16 a[210:211], v79 offset:40960\n"
"v_mfma_f32_16x16x32_bf16 a[32:35], v[70:73], a[84:87], a[32:35]\n"
"ds_read_b128 v[28:31], v67 offset:46080\n"
"v_mfma_f32_16x16x32_bf16 a[52:55], v[70:73], a[88:91], a[52:55]\n"
"ds_read_b64_tr_b16 a[224:225], v78 offset:45056\n"
"v_mfma_f32_16x16x32_bf16 a[52:55], v[74:77], a[104:107], a[52:55]\n"
"ds_read_b64_tr_b16 a[226:227], v79 offset:45056\n"
"v_mfma_f32_16x16x32_bf16 a[72:75], v[74:77], a[108:111], a[72:75]\n"
"ds_read_b64_tr_b16 a[228:229], v78 offset:46080\n"
"v_mfma_f32_16x16x32_bf16 a[72:75], v[70:73], a[92:95], a[72:75]\n"
"ds_read_b64_tr_b16 a[230:231], v79 offset:46080\n"
"v_mfma_f32_16x16x32_bf16 a[16:19], v[16:19], a[80:83], a[16:19]\n"
"ds_read_b64_tr_b16 a[212:213], v78 offset:41984\n"
"v_mfma_f32_16x16x32_bf16 a[16:19], v[20:23], a[96:99], a[16:19]\n"
"ds_read_b64_tr_b16 a[214:215], v79 offset:41984\n"
"v_mfma_f32_16x16x32_bf16 a[36:39], v[20:23], a[100:103], a[36:39]\n"
"ds_read_b64_tr_b16 a[216:217], v78 offset:43008\n"
"v_mfma_f32_16x16x32_bf16 a[36:39], v[16:19], a[84:87], a[36:39]\n"
"ds_read_b64_tr_b16 a[218:219], v79 offset:43008\n"
"v_mfma_f32_16x16x32_bf16 a[56:59], v[16:19], a[88:91], a[56:59]\n"
"ds_read_b64_tr_b16 a[232:233], v78 offset:47104\n"
"v_mfma_f32_16x16x32_bf16 a[56:59], v[20:23], a[104:107], a[56:59]\n"
"ds_read_b64_tr_b16 a[234:235], v79 offset:47104\n"
"v_mfma_f32_16x16x32_bf16 a[76:79], v[20:23], a[108:111], a[76:79]\n"
"ds_read_b64_tr_b16 a[236:237], v78 offset:48128\n"
"v_mfma_f32_16x16x32_bf16 a[76:79], v[16:19], a[92:95], a[76:79]\n"
"ds_read_b64_tr_b16 a[238:239], v79 offset:48128\n"
"s_waitcnt lgkmcnt(6)\n"
"v_mfma_f32_16x16x32_bf16 a[0:3], v[24:27], a[208:211], a[0:3]\n"
"ds_read_b64_tr_b16 a[220:221], v78 offset:44032\n"
"v_mfma_f32_16x16x32_bf16 a[0:3], v[28:31], a[224:227], a[0:3]\n"
"ds_read_b64_tr_b16 a[222:223], v79 offset:44032\n"
"v_mfma_f32_16x16x32_bf16 a[20:23], v[28:31], a[228:231], a[20:23]\n"
"ds_read_b128 v[70:73], v67 offset:41984\n"
"v_mfma_f32_16x16x32_bf16 a[20:23], v[24:27], a[212:215], a[20:23]\n"
"s_waitcnt lgkmcnt(3)\n"
"ds_read_b128 v[74:77], v67 offset:47104\n"
"v_mfma_f32_16x16x32_bf16 a[40:43], v[24:27], a[216:219], a[40:43]\n"
"s_mov_b32 s35, 0x16000\n"
"s_add_u32 m0, s82, 0x0\n"
"s_nop 0\n"
"buffer_load_dwordx4 v12, s[12:15], s35 offen sc1 lds\n"
"v_mfma_f32_16x16x32_bf16 a[40:43], v[28:31], a[232:235], a[40:43]\n"
"s_waitcnt lgkmcnt(2)\n"
"buffer_load_dwordx4 v12, s[12:15], s35 offen offset:1024 sc1 lds\n"
"v_mfma_f32_16x16x32_bf16 a[60:63], v[28:31], a[236:239], a[60:63]\n"
"buffer_load_dwordx4 v12, s[12:15], s35 offen offset:2048 sc1 lds\n"
"v_mfma_f32_16x16x32_bf16 a[60:63], v[24:27], a[220:223], a[60:63]\n"
"s_waitcnt lgkmcnt(0)\n"
"v_mfma_f32_16x16x32_bf16 a[4:7], v[70:73], a[208:211], a[4:7]\n"
"ds_read_b128 v[16:19], v67 offset:43008\n"
"v_mfma_f32_16x16x32_bf16 a[4:7], v[74:77], a[224:227], a[4:7]\n"
"ds_read_b128 v[20:23], v67 offset:48128\n"
"v_mfma_f32_16x16x32_bf16 a[24:27], v[74:77], a[228:231], a[24:27]\n"
"buffer_load_dwordx4 v12, s[12:15], s35 offen offset:3072 sc1 lds\n"
"v_mfma_f32_16x16x32_bf16 a[24:27], v[70:73], a[212:215], a[24:27]\n"
"s_mov_b32 s31, 0x1b800\n"
"s_add_u32 m0, s83, 0x0\n"
"s_nop 0\n"
"buffer_load_dwordx4 v12, s[8:11], s31 offen sc1 lds\n"
"v_mfma_f32_16x16x32_bf16 a[44:47], v[70:73], a[216:219], a[44:47]\n"
"buffer_load_dwordx4 v12, s[8:11], s31 offen offset:1024 sc1 lds\n"
"v_mfma_f32_16x16x32_bf16 a[44:47], v[74:77], a[232:235], a[44:47]\n"
"buffer_load_dwordx4 v12, s[8:11], s31 offen offset:2048 sc1 lds\n"
"v_mfma_f32_16x16x32_bf16 a[64:67], v[74:77], a[236:239], a[64:67]\n"
"buffer_load_dwordx4 v12, s[8:11], s31 offen offset:3072 sc1 lds\n"
"v_mfma_f32_16x16x32_bf16 a[64:67], v[70:73], a[220:223], a[64:67]\n"
"s_waitcnt lgkmcnt(0)\n"
"v_mfma_f32_16x16x32_bf16 a[8:11], v[16:19], a[208:211], a[8:11]\n"
"ds_read_b128 v[24:27], v67 offset:44032\n"
"v_mfma_f32_16x16x32_bf16 a[8:11], v[20:23], a[224:227], a[8:11]\n"
"ds_read_b128 v[28:31], v67 offset:49152\n"
"v_mfma_f32_16x16x32_bf16 a[28:31], v[20:23], a[228:231], a[28:31]\n"
"ds_read_b128 v[70:73], v67 offset:45056\n"
"v_mfma_f32_16x16x32_bf16 a[28:31], v[16:19], a[212:215], a[28:31]\n"
"ds_read_b128 v[74:77], v67 offset:50176\n"
"v_mfma_f32_16x16x32_bf16 a[48:51], v[16:19], a[216:219], a[48:51]\n"
"s_add_u32 m0, s83, 0x1000\n"
"s_nop 0\n"
"buffer_load_dwordx4 v52, s[8:11], s31 offen sc1 lds\n"
"v_mfma_f32_16x16x32_bf16 a[48:51], v[20:23], a[232:235], a[48:51]\n"
"v_mfma_f32_16x16x32_bf16 a[68:71], v[20:23], a[236:239], a[68:71]\n"
"v_mfma_f32_16x16x32_bf16 a[68:71], v[16:19], a[220:223], a[68:71]\n"
"s_waitcnt vmcnt(18) lgkmcnt(0)\n"
"s_barrier\n"
"v_mfma_f32_16x16x32_bf16 a[12:15], v[24:27], a[208:211], a[12:15]\n"
"ds_read_b128 v[16:19], v67 offset:0\n"
"v_mfma_f32_16x16x32_bf16 a[12:15], v[28:31], a[224:227], a[12:15]\n"
"ds_read_b64_tr_b16 a[80:81], v78 offset:0\n"
"v_mfma_f32_16x16x32_bf16 a[32:35], v[28:31], a[228:231], a[32:35]\n"
"ds_read_b64_tr_b16 a[82:83], v79 offset:0\n"
"v_mfma_f32_16x16x32_bf16 a[32:35], v[24:27], a[212:215], a[32:35]\n"
"ds_read_b128 v[20:23], v67 offset:5120\n"
"v_mfma_f32_16x16x32_bf16 a[52:55], v[24:27], a[216:219], a[52:55]\n"
"ds_read_b64_tr_b16 a[96:97], v78 offset:4096\n"
"v_mfma_f32_16x16x32_bf16 a[52:55], v[28:31], a[232:235], a[52:55]\n"
"ds_read_b64_tr_b16 a[98:99], v79 offset:4096\n"
"v_mfma_f32_16x16x32_bf16 a[72:75], v[28:31], a[236:239], a[72:75]\n"
"ds_read_b64_tr_b16 a[100:101], v78 offset:5120\n"
"v_mfma_f32_16x16x32_bf16 a[72:75], v[24:27], a[220:223], a[72:75]\n"
"ds_read_b64_tr_b16 a[102:103], v79 offset:5120\n"
"v_mfma_f32_16x16x32_bf16 a[16:19], v[70:73], a[208:211], a[16:19]\n"
"ds_read_b64_tr_b16 a[84:85], v78 offset:1024\n"
"v_mfma_f32_16x16x32_bf16 a[16:19], v[74:77], a[224:227], a[16:19]\n"
"ds_read_b64_tr_b16 a[86:87], v79 offset:1024\n"
"v_mfma_f32_16x16x32_bf16 a[36:39], v[74:77], a[228:231], a[36:39]\n"
"ds_read_b64_tr_b16 a[88:89], v78 offset:2048\n"
"v_mfma_f32_16x16x32_bf16 a[36:39], v[70:73], a[212:215], a[36:39]\n"
"ds_read_b64_tr_b16 a[90:91], v79 offset:2048\n"
"v_mfma_f32_16x16x32_bf16 a[56:59], v[70:73], a[216:219], a[56:59]\n"
"ds_read_b64_tr_b16 a[104:105], v78 offset:6144\n"
"v_mfma_f32_16x16x32_bf16 a[56:59], v[74:77], a[232:235], a[56:59]\n"
"ds_read_b64_tr_b16 a[106:107], v79 offset:6144\n"
"v_mfma_f32_16x16x32_bf16 a[76:79], v[74:77], a[236:239], a[76:79]\n"
"ds_read_b64_tr_b16 a[108:109], v78 offset:7168\n"
"v_mfma_f32_16x16x32_bf16 a[76:79], v[70:73], a[220:223], a[76:79]\n"
"ds_read_b64_tr_b16 a[110:111], v79 offset:7168\n"
"s_waitcnt lgkmcnt(6)\n"
"v_mfma_f32_16x16x32_bf16 a[0:3], v[16:19], a[80:83], a[0:3]\n"
"ds_read_b64_tr_b16 a[92:93], v78 offset:3072\n"
"v_mfma_f32_16x16x32_bf16 a[0:3], v[20:23], a[96:99], a[0:3]\n"
"ds_read_b64_tr_b16 a[94:95], v79 offset:3072\n"
"v_mfma_f32_16x16x32_bf16 a[20:23], v[20:23], a[100:103], a[20:23]\n"
"ds_read_b128 v[24:27], v67 offset:1024\n"
"v_mfma_f32_16x16x32_bf16 a[20:23], v[16:19], a[84:87], a[20:23]\n"
"s_waitcnt lgkmcnt(3)\n"
"ds_read_b128 v[28:31], v67 offset:6144\n"
"v_mfma_f32_16x16x32_bf16 a[40:43], v[16:19], a[88:91], a[40:43]\n"
"s_mov_b32 s35, 0x18000\n"
"s_add_u32 m0, s82, 0x1e000\n"
"s_nop 0\n"
"buffer_load_dwordx4 v12, s[12:15], s35 offen sc1 lds\n"
"v_mfma_f32_16x16x32_bf16 a[40:43], v[20:23], a[104:107], a[40:43]\n"
"s_waitcnt lgkmcnt(2)\n"
"buffer_load_dwordx4 v12, s[12:15], s35 offen offset:1024 sc1 lds\n"
"v_mfma_f32_16x16x32_bf16 a[60:63], v[20:23], a[108:111], a[60:63]\n"
"buffer_load_dwordx4 v12, s[12:15], s35 offen offset:2048 sc1 lds\n"
"v_mfma_f32_16x16x32_bf16 a[60:63], v[16:19], a[92:95], a[60:63]\n"
"s_waitcnt lgkmcnt(0)\n"
"v_mfma_f32_16x16x32_bf16 a[4:7], v[24:27], a[80:83], a[4:7]\n"
"ds_read_b128 v[70:73], v67 offset:2048\n"
"v_mfma_f32_16x16x32_bf16 a[4:7], v[28:31], a[96:99], a[4:7]\n"
"ds_read_b128 v[74:77], v67 offset:7168\n"
"v_mfma_f32_16x16x32_bf16 a[24:27], v[28:31], a[100:103], a[24:27]\n"
"buffer_load_dwordx4 v12, s[12:15], s35 offen offset:3072 sc1 lds\n"
"v_mfma_f32_16x16x32_bf16 a[24:27], v[24:27], a[84:87], a[24:27]\n"
"s_mov_b32 s31, 0x1e000\n"
"s_add_u32 m0, s83, 0x1e000\n"
"s_nop 0\n"
"buffer_load_dwordx4 v12, s[8:11], s31 offen sc1 lds\n"
"v_mfma_f32_16x16x32_bf16 a[44:47], v[24:27], a[88:91], a[44:47]\n"
"buffer_load_dwordx4 v12, s[8:11], s31 offen offset:1024 sc1 lds\n"
"v_mfma_f32_16x16x32_bf16 a[44:47], v[28:31], a[104:107], a[44:47]\n"
"buffer_load_dwordx4 v12, s[8:11], s31 offen offset:2048 sc1 lds\n"
"v_mfma_f32_16x16x32_bf16 a[64:67], v[28:31], a[108:111], a[64:67]\n"
"buffer_load_dwordx4 v12, s[8:11], s31 offen offset:3072 sc1 lds\n"
"v_mfma_f32_16x16x32_bf16 a[64:67], v[24:27], a[92:95], a[64:67]\n"
"s_waitcnt lgkmcnt(0)\n"
"v_mfma_f32_16x16x32_bf16 a[8:11], v[70:73], a[80:83], a[8:11]\n"
"ds_read_b128 v[16:19], v67 offset:3072\n"
"v_mfma_f32_16x16x32_bf16 a[8:11], v[74:77], a[96:99], a[8:11]\n"
"ds_read_b128 v[20:23], v67 offset:8192\n"
"v_mfma_f32_16x16x32_bf16 a[28:31], v[74:77], a[100:103], a[28:31]\n"
"ds_read_b128 v[24:27], v67 offset:4096\n"
"v_mfma_f32_16x16x32_bf16 a[28:31], v[70:73], a[84:87], a[28:31]\n"
"ds_read_b128 v[28:31], v67 offset:9216\n"
"v_mfma_f32_16x16x32_bf16 a[48:51], v[70:73], a[88:91], a[48:51]\n"
"s_add_u32 m0, s83, 0x1f000\n"
"s_nop 0\n"
"buffer_load_dwordx4 v52, s[8:11], s31 offen sc1 lds\n"
"v_mfma_f32_16x16x32_bf16 a[48:51], v[74:77], a[104:107], a[48:51]\n"
"v_mfma_f32_16x16x32_bf16 a[68:71], v[74:77], a[108:111], a[68:71]\n"
"v_mfma_f32_16x16x32_bf16 a[68:71], v[70:73], a[92:95], a[68:71]\n"
"s_waitcnt vmcnt(18) lgkmcnt(0)\n"
"s_barrier\n"
"v_mfma_f32_16x16x32_bf16 a[12:15], v[16:19], a[80:83], a[12:15]\n"
"ds_read_b128 v[70:73], v66 offset:40960\n"
"v_mfma_f32_16x16x32_bf16 a[12:15], v[20:23], a[96:99], a[12:15]\n"
"ds_read_b64_tr_b16 a[208:209], v64 offset:40960\n"
"v_mfma_f32_16x16x32_bf16 a[32:35], v[20:23], a[100:103], a[32:35]\n"
"ds_read_b64_tr_b16 a[210:211], v65 offset:40960\n"
"v_mfma_f32_16x16x32_bf16 a[32:35], v[16:19], a[84:87], a[32:35]\n"
"ds_read_b128 v[74:77], v66 offset:46080\n"
"v_mfma_f32_16x16x32_bf16 a[52:55], v[16:19], a[88:91], a[52:55]\n"
"ds_read_b64_tr_b16 a[224:225], v64 offset:45056\n"
"v_mfma_f32_16x16x32_bf16 a[52:55], v[20:23], a[104:107], a[52:55]\n"
"ds_read_b64_tr_b16 a[226:227], v65 offset:45056\n"
"v_mfma_f32_16x16x32_bf16 a[72:75], v[20:23], a[108:111], a[72:75]\n"
"ds_read_b64_tr_b16 a[228:229], v64 offset:46080\n"
"v_mfma_f32_16x16x32_bf16 a[72:75], v[16:19], a[92:95], a[72:75]\n"
"ds_read_b64_tr_b16 a[230:231], v65 offset:46080\n"
"v_mfma_f32_16x16x32_bf16 a[16:19], v[24:27], a[80:83], a[16:19]\n"
"ds_read_b64_tr_b16 a[212:213], v64 offset:41984\n"
"v_mfma_f32_16x16x32_bf16 a[16:19], v[28:31], a[96:99], a[16:19]\n"
"ds_read_b64_tr_b16 a[214:215], v65 offset:41984\n"
"v_mfma_f32_16x16x32_bf16 a[36:39], v[28:31], a[100:103], a[36:39]\n"
"ds_read_b64_tr_b16 a[216:217], v64 offset:43008\n"
"v_mfma_f32_16x16x32_bf16 a[36:39], v[24:27], a[84:87], a[36:39]\n"
"ds_read_b64_tr_b16 a[218:219], v65 offset:43008\n"
"v_mfma_f32_16x16x32_bf16 a[56:59], v[24:27], a[88:91], a[56:59]\n"
"ds_read_b64_tr_b16 a[232:233], v64 offset:47104\n"
"v_mfma_f32_16x16x32_bf16 a[56:59], v[28:31], a[104:107], a[56:59]\n"
"ds_read_b64_tr_b16 a[234:235], v65 offset:47104\n"
"v_mfma_f32_16x16x32_bf16 a[76:79], v[28:31], a[108:111], a[76:79]\n"
"ds_read_b64_tr_b16 a[236:237], v64 offset:48128\n"
"v_mfma_f32_16x16x32_bf16 a[76:79], v[24:27], a[92:95], a[76:79]\n"
"ds_read_b64_tr_b16 a[238:239], v65 offset:48128\n"
"s_waitcnt lgkmcnt(6)\n"
"v_mfma_f32_16x16x32_bf16 a[0:3], v[70:73], a[208:211], a[0:3]\n"
"ds_read_b64_tr_b16 a[220:221], v64 offset:44032\n"
"v_mfma_f32_16x16x32_bf16 a[0:3], v[74:77], a[224:227], a[0:3]\n"
"ds_read_b64_tr_b16 a[222:223], v65 offset:44032\n"
"v_mfma_f32_16x16x32_bf16 a[20:23], v[74:77], a[228:231], a[20:23]\n"
"ds_read_b128 v[16:19], v66 offset:41984\n"
"v_mfma_f32_16x16x32_bf16 a[20:23], v[70:73], a[212:215], a[20:23]\n"
"s_waitcnt lgkmcnt(3)\n"
"ds_read_b128 v[20:23], v66 offset:47104\n"
"v_mfma_f32_16x16x32_bf16 a[40:43], v[70:73], a[216:219], a[40:43]\n"
"s_mov_b32 s35, 0x1a000\n"
"s_add_u32 m0, s82, 0x14000\n"
"s_nop 0\n"
"buffer_load_dwordx4 v12, s[12:15], s35 offen sc1 lds\n"
"v_mfma_f32_16x16x32_bf16 a[40:43], v[74:77], a[232:235], a[40:43]\n"
"s_waitcnt lgkmcnt(2)\n"
"buffer_load_dwordx4 v12, s[12:15], s35 offen offset:1024 sc1 lds\n"
"v_mfma_f32_16x16x32_bf16 a[60:63], v[74:77], a[236:239], a[60:63]\n"
"buffer_load_dwordx4 v12, s[12:15], s35 offen offset:2048 sc1 lds\n"
"v_mfma_f32_16x16x32_bf16 a[60:63], v[70:73], a[220:223], a[60:63]\n"
"s_waitcnt lgkmcnt(0)\n"
"v_mfma_f32_16x16x32_bf16 a[4:7], v[16:19], a[208:211], a[4:7]\n"
"ds_read_b128 v[24:27], v66 offset:43008\n"
"v_mfma_f32_16x16x32_bf16 a[4:7], v[20:23], a[224:227], a[4:7]\n"
"ds_read_b128 v[28:31], v66 offset:48128\n"
"v_mfma_f32_16x16x32_bf16 a[24:27], v[20:23], a[228:231], a[24:27]\n"
"buffer_load_dwordx4 v12, s[12:15], s35 offen offset:3072 sc1 lds\n"
"v_mfma_f32_16x16x32_bf16 a[24:27], v[16:19], a[212:215], a[24:27]\n"
"s_mov_b32 s31, 0x20800\n"
"s_add_u32 m0, s83, 0x14000\n"
"s_nop 0\n"
"buffer_load_dwordx4 v12, s[8:11], s31 offen sc1 lds\n"
"v_mfma_f32_16x16x32_bf16 a[44:47], v[16:19], a[216:219], a[44:47]\n"
"buffer_load_dwordx4 v12, s[8:11], s31 offen offset:1024 sc1 lds\n"
"v_mfma_f32_16x16x32_bf16 a[44:47], v[20:23], a[232:235], a[44:47]\n"
"buffer_load_dwordx4 v12, s[8:11], s31 offen offset:2048 sc1 lds\n"
"v_mfma_f32_16x16x32_bf16 a[64:67], v[20:23], a[236:239], a[64:67]\n"
"buffer_load_dwordx4 v12, s[8:11], s31 offen offset:3072 sc1 lds\n"
"v_mfma_f32_16x16x32_bf16 a[64:67], v[16:19], a[220:223], a[64:67]\n"
"s_waitcnt lgkmcnt(0)\n"
"v_mfma_f32_16x16x32_bf16 a[8:11], v[24:27], a[208:211], a[8:11]\n"
"ds_read_b128 v[70:73], v66 offset:44032\n"
"v_mfma_f32_16x16x32_bf16 a[8:11], v[28:31], a[224:227], a[8:11]\n"
"ds_read_b128 v[74:77], v66 offset:49152\n"
"v_mfma_f32_16x16x32_bf16 a[28:31], v[28:31], a[228:231], a[28:31]\n"
"ds_read_b128 v[16:19], v66 offset:45056\n"
"v_mfma_f32_16x16x32_bf16 a[28:31], v[24:27], a[212:215], a[28:31]\n"
"ds_read_b128 v[20:23], v66 offset:50176\n"
"v_mfma_f32_16x16x32_bf16 a[48:51], v[24:27], a[216:219], a[48:51]\n"
"s_add_u32 m0, s83, 0x15000\n"
"s_nop 0\n"
"buffer_load_dwordx4 v52, s[8:11], s31 offen sc1 lds\n"
"v_mfma_f32_16x16x32_bf16 a[48:51], v[28:31], a[232:235], a[48:51]\n"
"v_mfma_f32_16x16x32_bf16 a[68:71], v[28:31], a[236:239], a[68:71]\n"
"v_mfma_f32_16x16x32_bf16 a[68:71], v[24:27], a[220:223], a[68:71]\n"
"s_waitcnt vmcnt(18) lgkmcnt(0)\n"
"s_barrier\n"
"v_mfma_f32_16x16x32_bf16 a[12:15], v[70:73], a[208:211], a[12:15]\n"
"ds_read_b128 v[24:27], v66 offset:0\n"
"v_mfma_f32_16x16x32_bf16 a[12:15], v[74:77], a[224:227], a[12:15]\n"
"ds_read_b64_tr_b16 a[80:81], v64 offset:0\n"
"v_mfma_f32_16x16x32_bf16 a[32:35], v[74:77], a[228:231], a[32:35]\n"
"ds_read_b64_tr_b16 a[82:83], v65 offset:0\n"
"v_mfma_f32_16x16x32_bf16 a[32:35], v[70:73], a[212:215], a[32:35]\n"
"ds_read_b128 v[28:31], v66 offset:5120\n"
"v_mfma_f32_16x16x32_bf16 a[52:55], v[70:73], a[216:219], a[52:55]\n"
"ds_read_b64_tr_b16 a[96:97], v64 offset:4096\n"
"v_mfma_f32_16x16x32_bf16 a[52:55], v[74:77], a[232:235], a[52:55]\n"
"ds_read_b64_tr_b16 a[98:99], v65 offset:4096\n"
"v_mfma_f32_16x16x32_bf16 a[72:75], v[74:77], a[236:239], a[72:75]\n"
"ds_read_b64_tr_b16 a[100:101], v64 offset:5120\n"
"v_mfma_f32_16x16x32_bf16 a[72:75], v[70:73], a[220:223], a[72:75]\n"
"ds_read_b64_tr_b16 a[102:103], v65 offset:5120\n"
"v_mfma_f32_16x16x32_bf16 a[16:19], v[16:19], a[208:211], a[16:19]\n"
"ds_read_b64_tr_b16 a[84:85], v64 offset:1024\n"
"v_mfma_f32_16x16x32_bf16 a[16:19], v[20:23], a[224:227], a[16:19]\n"
"ds_read_b64_tr_b16 a[86:87], v65 offset:1024\n"
"v_mfma_f32_16x16x32_bf16 a[36:39], v[20:23], a[228:231], a[36:39]\n"
"ds_read_b64_tr_b16 a[88:89], v64 offset:2048\n"
"v_mfma_f32_16x16x32_bf16 a[36:39], v[16:19], a[212:215], a[36:39]\n"
"ds_read_b64_tr_b16 a[90:91], v65 offset:2048\n"
"v_mfma_f32_16x16x32_bf16 a[56:59], v[16:19], a[216:219], a[56:59]\n"
"ds_read_b64_tr_b16 a[104:105], v64 offset:6144\n"
"v_mfma_f32_16x16x32_bf16 a[56:59], v[20:23], a[232:235], a[56:59]\n"
"ds_read_b64_tr_b16 a[106:107], v65 offset:6144\n"
"v_mfma_f32_16x16x32_bf16 a[76:79], v[20:23], a[236:239], a[76:79]\n"
"ds_read_b64_tr_b16 a[108:109], v64 offset:7168\n"
"v_mfma_f32_16x16x32_bf16 a[76:79], v[16:19], a[220:223], a[76:79]\n"
"ds_read_b64_tr_b16 a[110:111], v65 offset:7168\n"
"s_waitcnt lgkmcnt(6)\n"
"v_mfma_f32_16x16x32_bf16 a[0:3], v[24:27], a[80:83], a[0:3]\n"
"ds_read_b64_tr_b16 a[92:93], v64 offset:3072\n"
"v_mfma_f32_16x16x32_bf16 a[0:3], v[28:31], a[96:99], a[0:3]\n"
"ds_read_b64_tr_b16 a[94:95], v65 offset:3072\n"
"v_mfma_f32_16x16x32_bf16 a[20:23], v[28:31], a[100:103], a[20:23]\n"
"ds_read_b128 v[70:73], v66 offset:1024\n"
"v_mfma_f32_16x16x32_bf16 a[20:23], v[24:27], a[84:87], a[20:23]\n"
"s_waitcnt lgkmcnt(3)\n"
"ds_read_b128 v[74:77], v66 offset:6144\n"
"v_mfma_f32_16x16x32_bf16 a[40:43], v[24:27], a[88:91], a[40:43]\n"
"s_mov_b32 s35, 0x1c000\n"
"s_add_u32 m0, s82, 0xa000\n"
"s_nop 0\n"
"buffer_load_dwordx4 v12, s[12:15], s35 offen sc1 lds\n"
"v_mfma_f32_16x16x32_bf16 a[40:43], v[28:31], a[104:107], a[40:43]\n"
"s_waitcnt lgkmcnt(2)\n"
"buffer_load_dwordx4 v12, s[12:15], s35 offen offset:1024 sc1 lds\n"
"v_mfma_f32_16x16x32_bf16 a[60:63], v[28:31], a[108:111], a[60:63]\n"
"buffer_load_dwordx4 v12, s[12:15], s35 offen offset:2048 sc1 lds\n"
"v_mfma_f32_16x16x32_bf16 a[60:63], v[24:27], a[92:95], a[60:63]\n"
"s_waitcnt lgkmcnt(0)\n"
"v_mfma_f32_16x16x32_bf16 a[4:7], v[70:73], a[80:83], a[4:7]\n"
"ds_read_b128 v[16:19], v66 offset:2048\n"
"v_mfma_f32_16x16x32_bf16 a[4:7], v[74:77], a[96:99], a[4:7]\n"
"ds_read_b128 v[20:23], v66 offset:7168\n"
"v_mfma_f32_16x16x32_bf16 a[24:27], v[74:77], a[100:103], a[24:27]\n"
"buffer_load_dwordx4 v12, s[12:15], s35 offen offset:3072 sc1 lds\n"
"v_mfma_f32_16x16x32_bf16 a[24:27], v[70:73], a[84:87], a[24:27]\n"
"s_mov_b32 s31, 0x23000\n"
"s_add_u32 m0, s83, 0xa000\n"
"s_nop 0\n"
"buffer_load_dwordx4 v12, s[8:11], s31 offen sc1 lds\n"
"v_mfma_f32_16x16x32_bf16 a[44:47], v[70:73], a[88:91], a[44:47]\n"
"buffer_load_dwordx4 v12, s[8:11], s31 offen offset:1024 sc1 lds\n"
"v_mfma_f32_16x16x32_bf16 a[44:47], v[74:77], a[104:107], a[44:47]\n"
"buffer_load_dwordx4 v12, s[8:11], s31 offen offset:2048 sc1 lds\n"
"v_mfma_f32_16x16x32_bf16 a[64:67], v[74:77], a[108:111], a[64:67]\n"
"buffer_load_dwordx4 v12, s[8:11], s31 offen offset:3072 sc1 lds\n"
"v_mfma_f32_16x16x32_bf16 a[64:67], v[70:73], a[92:95], a[64:67]\n"
"s_waitcnt lgkmcnt(0)\n"
"v_mfma_f32_16x16x32_bf16 a[8:11], v[16:19], a[80:83], a[8:11]\n"
"ds_read_b128 v[24:27], v66 offset:3072\n"
"v_mfma_f32_16x16x32_bf16 a[8:11], v[20:23], a[96:99], a[8:11]\n"
"ds_read_b128 v[28:31], v66 offset:8192\n"
"v_mfma_f32_16x16x32_bf16 a[28:31], v[20:23], a[100:103], a[28:31]\n"
"ds_read_b128 v[70:73], v66 offset:4096\n"
"v_mfma_f32_16x16x32_bf16 a[28:31], v[16:19], a[84:87], a[28:31]\n"
"ds_read_b128 v[74:77], v66 offset:9216\n"
"v_mfma_f32_16x16x32_bf16 a[48:51], v[16:19], a[88:91], a[48:51]\n"
"s_add_u32 m0, s83, 0xb000\n"
"s_nop 0\n"
"buffer_load_dwordx4 v52, s[8:11], s31 offen sc1 lds\n"
"v_mfma_f32_16x16x32_bf16 a[48:51], v[20:23], a[104:107], a[48:51]\n"
"v_mfma_f32_16x16x32_bf16 a[68:71], v[20:23], a[108:111], a[68:71]\n"
"v_mfma_f32_16x16x32_bf16 a[68:71], v[16:19], a[92:95], a[68:71]\n"
"s_waitcnt vmcnt(18) lgkmcnt(0)\n"
"s_barrier\n"
"v_mfma_f32_16x16x32_bf16 a[12:15], v[24:27], a[80:83], a[12:15]\n"
"ds_read_b128 v[16:19], v67 offset:40960\n"
"v_mfma_f32_16x16x32_bf16 a[12:15], v[28:31], a[96:99], a[12:15]\n"
"ds_read_b64_tr_b16 a[208:209], v78 offset:40960\n"
"v_mfma_f32_16x16x32_bf16 a[32:35], v[28:31], a[100:103], a[32:35]\n"
"ds_read_b64_tr_b16 a[210:211], v79 offset:40960\n"
"v_mfma_f32_16x16x32_bf16 a[32:35], v[24:27], a[84:87], a[32:35]\n"
"ds_read_b128 v[20:23], v67 offset:46080\n"
"v_mfma_f32_16x16x32_bf16 a[52:55], v[24:27], a[88:91], a[52:55]\n"
"ds_read_b64_tr_b16 a[224:225], v78 offset:45056\n"
"v_mfma_f32_16x16x32_bf16 a[52:55], v[28:31], a[104:107], a[52:55]\n"
"ds_read_b64_tr_b16 a[226:227], v79 offset:45056\n"
"v_mfma_f32_16x16x32_bf16 a[72:75], v[28:31], a[108:111], a[72:75]\n"
"ds_read_b64_tr_b16 a[228:229], v78 offset:46080\n"
"v_mfma_f32_16x16x32_bf16 a[72:75], v[24:27], a[92:95], a[72:75]\n"
"ds_read_b64_tr_b16 a[230:231], v79 offset:46080\n"
"v_mfma_f32_16x16x32_bf16 a[16:19], v[70:73], a[80:83], a[16:19]\n"
"ds_read_b64_tr_b16 a[212:213], v78 offset:41984\n"
"v_mfma_f32_16x16x32_bf16 a[16:19], v[74:77], a[96:99], a[16:19]\n"
"ds_read_b64_tr_b16 a[214:215], v79 offset:41984\n"
"v_mfma_f32_16x16x32_bf16 a[36:39], v[74:77], a[100:103], a[36:39]\n"
"ds_read_b64_tr_b16 a[216:217], v78 offset:43008\n"
"v_mfma_f32_16x16x32_bf16 a[36:39], v[70:73], a[84:87], a[36:39]\n"
"ds_read_b64_tr_b16 a[218:219], v79 offset:43008\n"
"v_mfma_f32_16x16x32_bf16 a[56:59], v[70:73], a[88:91], a[56:59]\n"
"ds_read_b64_tr_b16 a[232:233], v78 offset:47104\n"
"v_mfma_f32_16x16x32_bf16 a[56:59], v[74:77], a[104:107], a[56:59]\n"
"ds_read_b64_tr_b16 a[234:235], v79 offset:47104\n"
"v_mfma_f32_16x16x32_bf16 a[76:79], v[74:77], a[108:111], a[76:79]\n"
"ds_read_b64_tr_b16 a[236:237], v78 offset:48128\n"
"v_mfma_f32_16x16x32_bf16 a[76:79], v[70:73], a[92:95], a[76:79]\n"
"ds_read_b64_tr_b16 a[238:239], v79 offset:48128\n"
"s_waitcnt lgkmcnt(6)\n"
"v_mfma_f32_16x16x32_bf16 a[0:3], v[16:19], a[208:211], a[0:3]\n"
"ds_read_b64_tr_b16 a[220:221], v78 offset:44032\n"
"v_mfma_f32_16x16x32_bf16 a[0:3], v[20:23], a[224:227], a[0:3]\n"
"ds_read_b64_tr_b16 a[222:223], v79 offset:44032\n"
"v_mfma_f32_16x16x32_bf16 a[20:23], v[20:23], a[228:231], a[20:23]\n"
"ds_read_b128 v[24:27], v67 offset:41984\n"
"v_mfma_f32_16x16x32_bf16 a[20:23], v[16:19], a[212:215], a[20:23]\n"
"s_waitcnt lgkmcnt(3)\n"
"ds_read_b128 v[28:31], v67 offset:47104\n"
"v_mfma_f32_16x16x32_bf16 a[40:43], v[16:19], a[216:219], a[40:43]\n"
"s_mov_b32 s35, 0x1e000\n"
"s_add_u32 m0, s82, 0x0\n"
"s_nop 0\n"
"buffer_load_dwordx4 v12, s[12:15], s35 offen sc1 lds\n"
"v_mfma_f32_16x16x32_bf16 a[40:43], v[20:23], a[232:235], a[40:43]\n"
"s_waitcnt lgkmcnt(2)\n"
"buffer_load_dwordx4 v12, s[12:15], s35 offen offset:1024 sc1 lds\n"
"v_mfma_f32_16x16x32_bf16 a[60:63], v[20:23], a[236:239], a[60:63]\n"
"buffer_load_dwordx4 v12, s[12:15], s35 offen offset:2048 sc1 lds\n"
"v_mfma_f32_16x16x32_bf16 a[60:63], v[16:19], a[220:223], a[60:63]\n"
"s_waitcnt lgkmcnt(0)\n"
"v_mfma_f32_16x16x32_bf16 a[4:7], v[24:27], a[208:211], a[4:7]\n"
"ds_read_b128 v[70:73], v67 offset:43008\n"
"v_mfma_f32_16x16x32_bf16 a[4:7], v[28:31], a[224:227], a[4:7]\n"
"ds_read_b128 v[74:77], v67 offset:48128\n"
"v_mfma_f32_16x16x32_bf16 a[24:27], v[28:31], a[228:231], a[24:27]\n"
"buffer_load_dwordx4 v12, s[12:15], s35 offen offset:3072 sc1 lds\n"
"v_mfma_f32_16x16x32_bf16 a[24:27], v[24:27], a[212:215], a[24:27]\n"
"s_mov_b32 s31, 0x25800\n"
"s_add_u32 m0, s83, 0x0\n"
"s_nop 0\n"
"buffer_load_dwordx4 v12, s[8:11], s31 offen sc1 lds\n"
"v_mfma_f32_16x16x32_bf16 a[44:47], v[24:27], a[216:219], a[44:47]\n"
"buffer_load_dwordx4 v12, s[8:11], s31 offen offset:1024 sc1 lds\n"
"v_mfma_f32_16x16x32_bf16 a[44:47], v[28:31], a[232:235], a[44:47]\n"
"buffer_load_dwordx4 v12, s[8:11], s31 offen offset:2048 sc1 lds\n"
"v_mfma_f32_16x16x32_bf16 a[64:67], v[28:31], a[236:239], a[64:67]\n"
"buffer_load_dwordx4 v12, s[8:11], s31 offen offset:3072 sc1 lds\n"
"v_mfma_f32_16x16x32_bf16 a[64:67], v[24:27], a[220:223], a[64:67]\n"
"s_waitcnt lgkmcnt(0)\n"
"v_mfma_f32_16x16x32_bf16 a[8:11], v[70:73], a[208:211], a[8:11]\n"
"ds_read_b128 v[16:19], v67 offset:44032\n"
"v_mfma_f32_16x16x32_bf16 a[8:11], v[74:77], a[224:227], a[8:11]\n"
"ds_read_b128 v[20:23], v67 offset:49152\n"
"v_mfma_f32_16x16x32_bf16 a[28:31], v[74:77], a[228:231], a[28:31]\n"
"ds_read_b128 v[24:27], v67 offset:45056\n"
"v_mfma_f32_16x16x32_bf16 a[28:31], v[70:73], a[212:215], a[28:31]\n"
"ds_read_b128 v[28:31], v67 offset:50176\n"
"v_mfma_f32_16x16x32_bf16 a[48:51], v[70:73], a[216:219], a[48:51]\n"
"s_add_u32 m0, s83, 0x1000\n"
"s_nop 0\n"
"buffer_load_dwordx4 v52, s[8:11], s31 offen sc1 lds\n"
"v_mfma_f32_16x16x32_bf16 a[48:51], v[74:77], a[232:235], a[48:51]\n"
"v_mfma_f32_16x16x32_bf16 a[68:71], v[74:77], a[236:239], a[68:71]\n"
"v_mfma_f32_16x16x32_bf16 a[68:71], v[70:73], a[220:223], a[68:71]\n"
"s_waitcnt vmcnt(18) lgkmcnt(0)\n"
"s_barrier\n"
"v_mfma_f32_16x16x32_bf16 a[12:15], v[16:19], a[208:211], a[12:15]\n"
"ds_read_b128 v[70:73], v67 offset:0\n"
"v_mfma_f32_16x16x32_bf16 a[12:15], v[20:23], a[224:227], a[12:15]\n"
"ds_read_b64_tr_b16 a[80:81], v78 offset:0\n"
"v_mfma_f32_16x16x32_bf16 a[32:35], v[20:23], a[228:231], a[32:35]\n"
"ds_read_b64_tr_b16 a[82:83], v79 offset:0\n"
"v_mfma_f32_16x16x32_bf16 a[32:35], v[16:19], a[212:215], a[32:35]\n"
"ds_read_b128 v[74:77], v67 offset:5120\n"
"v_mfma_f32_16x16x32_bf16 a[52:55], v[16:19], a[216:219], a[52:55]\n"
"ds_read_b64_tr_b16 a[96:97], v78 offset:4096\n"
"v_mfma_f32_16x16x32_bf16 a[52:55], v[20:23], a[232:235], a[52:55]\n"
"ds_read_b64_tr_b16 a[98:99], v79 offset:4096\n"
"v_mfma_f32_16x16x32_bf16 a[72:75], v[20:23], a[236:239], a[72:75]\n"
"ds_read_b64_tr_b16 a[100:101], v78 offset:5120\n"
"v_mfma_f32_16x16x32_bf16 a[72:75], v[16:19], a[220:223], a[72:75]\n"
"ds_read_b64_tr_b16 a[102:103], v79 offset:5120\n"
"v_mfma_f32_16x16x32_bf16 a[16:19], v[24:27], a[208:211], a[16:19]\n"
"ds_read_b64_tr_b16 a[84:85], v78 offset:1024\n"
"v_mfma_f32_16x16x32_bf16 a[16:19], v[28:31], a[224:227], a[16:19]\n"
"ds_read_b64_tr_b16 a[86:87], v79 offset:1024\n"
"v_mfma_f32_16x16x32_bf16 a[36:39], v[28:31], a[228:231], a[36:39]\n"
"ds_read_b64_tr_b16 a[88:89], v78 offset:2048\n"
"v_mfma_f32_16x16x32_bf16 a[36:39], v[24:27], a[212:215], a[36:39]\n"
"ds_read_b64_tr_b16 a[90:91], v79 offset:2048\n"
"v_mfma_f32_16x16x32_bf16 a[56:59], v[24:27], a[216:219], a[56:59]\n"
"ds_read_b64_tr_b16 a[104:105], v78 offset:6144\n"
"v_mfma_f32_16x16x32_bf16 a[56:59], v[28:31], a[232:235], a[56:59]\n"
"ds_read_b64_tr_b16 a[106:107], v79 offset:6144\n"
"v_mfma_f32_16x16x32_bf16 a[76:79], v[28:31], a[236:239], a[76:79]\n"
"ds_read_b64_tr_b16 a[108:109], v78 offset:7168\n"
"v_mfma_f32_16x16x32_bf16 a[76:79], v[24:27], a[220:223], a[76:79]\n"
"ds_read_b64_tr_b16 a[110:111], v79 offset:7168\n"
"s_waitcnt lgkmcnt(6)\n"
"v_mfma_f32_16x16x32_bf16 a[0:3], v[70:73], a[80:83], a[0:3]\n"
"ds_read_b64_tr_b16 a[92:93], v78 offset:3072\n"
"v_mfma_f32_16x16x32_bf16 a[0:3], v[74:77], a[96:99], a[0:3]\n"
"ds_read_b64_tr_b16 a[94:95], v79 offset:3072\n"
"v_mfma_f32_16x16x32_bf16 a[20:23], v[74:77], a[100:103], a[20:23]\n"
"ds_read_b128 v[16:19], v67 offset:1024\n"
"v_mfma_f32_16x16x32_bf16 a[20:23], v[70:73], a[84:87], a[20:23]\n"
"s_waitcnt lgkmcnt(3)\n"
"ds_read_b128 v[20:23], v67 offset:6144\n"
"v_mfma_f32_16x16x32_bf16 a[40:43], v[70:73], a[88:91], a[40:43]\n"
"v_mfma_f32_16x16x32_bf16 a[40:43], v[74:77], a[104:107], a[40:43]\n"
"s_waitcnt lgkmcnt(2)\n"
"v_mfma_f32_16x16x32_bf16 a[60:63], v[74:77], a[108:111], a[60:63]\n"
"v_mfma_f32_16x16x32_bf16 a[60:63], v[70:73], a[92:95], a[60:63]\n"
"s_waitcnt lgkmcnt(0)\n"
"v_mfma_f32_16x16x32_bf16 a[4:7], v[16:19], a[80:83], a[4:7]\n"
"ds_read_b128 v[24:27], v67 offset:2048\n"
"v_mfma_f32_16x16x32_bf16 a[4:7], v[20:23], a[96:99], a[4:7]\n"
"ds_read_b128 v[28:31], v67 offset:7168\n"
"v_mfma_f32_16x16x32_bf16 a[24:27], v[20:23], a[100:103], a[24:27]\n"
"v_mfma_f32_16x16x32_bf16 a[24:27], v[16:19], a[84:87], a[24:27]\n"
"v_mfma_f32_16x16x32_bf16 a[44:47], v[16:19], a[88:91], a[44:47]\n"
"v_mfma_f32_16x16x32_bf16 a[44:47], v[20:23], a[104:107], a[44:47]\n"
"v_mfma_f32_16x16x32_bf16 a[64:67], v[20:23], a[108:111], a[64:67]\n"
"v_mfma_f32_16x16x32_bf16 a[64:67], v[16:19], a[92:95], a[64:67]\n"
"s_waitcnt lgkmcnt(0)\n"
"v_mfma_f32_16x16x32_bf16 a[8:11], v[24:27], a[80:83], a[8:11]\n"
"ds_read_b128 v[70:73], v67 offset:3072\n"
"v_mfma_f32_16x16x32_bf16 a[8:11], v[28:31], a[96:99], a[8:11]\n"
"ds_read_b128 v[74:77], v67 offset:8192\n"
"v_mfma_f32_16x16x32_bf16 a[28:31], v[28:31], a[100:103], a[28:31]\n"
"ds_read_b128 v[16:19], v67 offset:4096\n"
"v_mfma_f32_16x16x32_bf16 a[28:31], v[24:27], a[84:87], a[28:31]\n"
"ds_read_b128 v[20:23], v67 offset:9216\n"
"v_mfma_f32_16x16x32_bf16 a[48:51], v[24:27], a[88:91], a[48:51]\n"
"v_mfma_f32_16x16x32_bf16 a[48:51], v[28:31], a[104:107], a[48:51]\n"
"v_mfma_f32_16x16x32_bf16 a[68:71], v[28:31], a[108:111], a[68:71]\n"
"v_mfma_f32_16x16x32_bf16 a[68:71], v[24:27], a[92:95], a[68:71]\n"
"s_waitcnt vmcnt(9) lgkmcnt(0)\n"
"s_barrier\n"
"v_mfma_f32_16x16x32_bf16 a[12:15], v[70:73], a[80:83], a[12:15]\n"
"ds_read_b128 v[24:27], v66 offset:40960\n"
"v_mfma_f32_16x16x32_bf16 a[12:15], v[74:77], a[96:99], a[12:15]\n"
"ds_read_b64_tr_b16 a[208:209], v64 offset:40960\n"
"v_mfma_f32_16x16x32_bf16 a[32:35], v[74:77], a[100:103], a[32:35]\n"
"ds_read_b64_tr_b16 a[210:211], v65 offset:40960\n"
"v_mfma_f32_16x16x32_bf16 a[32:35], v[70:73], a[84:87], a[32:35]\n"
"ds_read_b128 v[28:31], v66 offset:46080\n"
"v_mfma_f32_16x16x32_bf16 a[52:55], v[70:73], a[88:91], a[52:55]\n"
"ds_read_b64_tr_b16 a[224:225], v64 offset:45056\n"
"v_mfma_f32_16x16x32_bf16 a[52:55], v[74:77], a[104:107], a[52:55]\n"
"ds_read_b64_tr_b16 a[226:227], v65 offset:45056\n"
"v_mfma_f32_16x16x32_bf16 a[72:75], v[74:77], a[108:111], a[72:75]\n"
"ds_read_b64_tr_b16 a[228:229], v64 offset:46080\n"
"v_mfma_f32_16x16x32_bf16 a[72:75], v[70:73], a[92:95], a[72:75]\n"
"ds_read_b64_tr_b16 a[230:231], v65 offset:46080\n"
"v_mfma_f32_16x16x32_bf16 a[16:19], v[16:19], a[80:83], a[16:19]\n"
"ds_read_b64_tr_b16 a[212:213], v64 offset:41984\n"
"v_mfma_f32_16x16x32_bf16 a[16:19], v[20:23], a[96:99], a[16:19]\n"
"ds_read_b64_tr_b16 a[214:215], v65 offset:41984\n"
"v_mfma_f32_16x16x32_bf16 a[36:39], v[20:23], a[100:103], a[36:39]\n"
"ds_read_b64_tr_b16 a[216:217], v64 offset:43008\n"
"v_mfma_f32_16x16x32_bf16 a[36:39], v[16:19], a[84:87], a[36:39]\n"
"ds_read_b64_tr_b16 a[218:219], v65 offset:43008\n"
"v_mfma_f32_16x16x32_bf16 a[56:59], v[16:19], a[88:91], a[56:59]\n"
"ds_read_b64_tr_b16 a[232:233], v64 offset:47104\n"
"v_mfma_f32_16x16x32_bf16 a[56:59], v[20:23], a[104:107], a[56:59]\n"
"ds_read_b64_tr_b16 a[234:235], v65 offset:47104\n"
"v_mfma_f32_16x16x32_bf16 a[76:79], v[20:23], a[108:111], a[76:79]\n"
"ds_read_b64_tr_b16 a[236:237], v64 offset:48128\n"
"v_mfma_f32_16x16x32_bf16 a[76:79], v[16:19], a[92:95], a[76:79]\n"
"ds_read_b64_tr_b16 a[238:239], v65 offset:48128\n"
"s_waitcnt lgkmcnt(6)\n"
"v_mfma_f32_16x16x32_bf16 a[0:3], v[24:27], a[208:211], a[0:3]\n"
"ds_read_b64_tr_b16 a[220:221], v64 offset:44032\n"
"v_mfma_f32_16x16x32_bf16 a[0:3], v[28:31], a[224:227], a[0:3]\n"
"ds_read_b64_tr_b16 a[222:223], v65 offset:44032\n"
"v_mfma_f32_16x16x32_bf16 a[20:23], v[28:31], a[228:231], a[20:23]\n"
"ds_read_b128 v[70:73], v66 offset:41984\n"
"v_mfma_f32_16x16x32_bf16 a[20:23], v[24:27], a[212:215], a[20:23]\n"
"s_waitcnt lgkmcnt(3)\n"
"ds_read_b128 v[74:77], v66 offset:47104\n"
"v_mfma_f32_16x16x32_bf16 a[40:43], v[24:27], a[216:219], a[40:43]\n"
"s_mov_b32 s31, 0x100000\n"
"s_add_u32 m0, s63, 0x1e000\n"
"s_nop 0\n"
"buffer_load_dwordx4 v44, s[16:19], s31 offen lds\n"
"v_mfma_f32_16x16x32_bf16 a[40:43], v[28:31], a[232:235], a[40:43]\n"
"s_waitcnt lgkmcnt(2)\n"
"buffer_load_dwordx4 v45, s[16:19], s31 offen offset:1024 lds\n"
"v_mfma_f32_16x16x32_bf16 a[60:63], v[28:31], a[236:239], a[60:63]\n"
"buffer_load_dwordx4 v46, s[16:19], s31 offen offset:2048 lds\n"
"v_mfma_f32_16x16x32_bf16 a[60:63], v[24:27], a[220:223], a[60:63]\n"
"s_waitcnt lgkmcnt(0)\n"
"v_mfma_f32_16x16x32_bf16 a[4:7], v[70:73], a[208:211], a[4:7]\n"
"ds_read_b128 v[16:19], v66 offset:43008\n"
"v_mfma_f32_16x16x32_bf16 a[4:7], v[74:77], a[224:227], a[4:7]\n"
"ds_read_b128 v[20:23], v66 offset:48128\n"
"v_mfma_f32_16x16x32_bf16 a[24:27], v[74:77], a[228:231], a[24:27]\n"
"buffer_load_dwordx4 v47, s[16:19], s31 offen offset:3072 lds\n"
"v_mfma_f32_16x16x32_bf16 a[24:27], v[70:73], a[212:215], a[24:27]\n"
"s_mov_b32 s35, 0xa000\n"
"buffer_load_dwordx4 a[168:171], v12, s[20:23], s35 offen sc1\n"
"v_mfma_f32_16x16x32_bf16 a[44:47], v[70:73], a[216:219], a[44:47]\n"
"buffer_load_dwordx4 a[172:175], v12, s[20:23], s35 offen offset:1024 sc1\n"
"v_mfma_f32_16x16x32_bf16 a[44:47], v[74:77], a[232:235], a[44:47]\n"
"buffer_load_dwordx4 a[176:179], v12, s[20:23], s35 offen offset:2048 sc1\n"
"v_mfma_f32_16x16x32_bf16 a[64:67], v[74:77], a[236:239], a[64:67]\n"
"buffer_load_dwordx4 a[180:183], v12, s[20:23], s35 offen offset:3072 sc1\n"
"v_mfma_f32_16x16x32_bf16 a[64:67], v[70:73], a[220:223], a[64:67]\n"
"s_waitcnt lgkmcnt(0)\n"
"v_mfma_f32_16x16x32_bf16 a[8:11], v[16:19], a[208:211], a[8:11]\n"
"ds_read_b128 v[24:27], v66 offset:44032\n"
"v_mfma_f32_16x16x32_bf16 a[8:11], v[20:23], a[224:227], a[8:11]\n"
"ds_read_b128 v[28:31], v66 offset:49152\n"
"v_mfma_f32_16x16x32_bf16 a[28:31], v[20:23], a[228:231], a[28:31]\n"
"ds_read_b128 v[70:73], v66 offset:45056\n"
"v_mfma_f32_16x16x32_bf16 a[28:31], v[16:19], a[212:215], a[28:31]\n"
"ds_read_b128 v[74:77], v66 offset:50176\n"
"v_mfma_f32_16x16x32_bf16 a[48:51], v[16:19], a[216:219], a[48:51]\n"
"buffer_load_dwordx4 a[184:187], v52, s[20:23], s35 offen sc1\n"
"v_mfma_f32_16x16x32_bf16 a[48:51], v[20:23], a[232:235], a[48:51]\n"
"s_mov_b32 s31, 0x120000\n"
"s_add_u32 m0, s63, 0x23000\n"
"s_nop 0\n"
"buffer_load_dwordx4 v44, s[16:19], s31 offen lds\n"
"v_mfma_f32_16x16x32_bf16 a[68:71], v[20:23], a[236:239], a[68:71]\n"
"buffer_load_dwordx4 v45, s[16:19], s31 offen offset:1024 lds\n"
"v_mfma_f32_16x16x32_bf16 a[68:71], v[16:19], a[220:223], a[68:71]\n"
"buffer_load_dwordx4 v46, s[16:19], s31 offen offset:2048 lds\n"
"buffer_load_dwordx4 v47, s[16:19], s31 offen offset:3072 lds\n"
"s_mov_b32 s35, 0xb400\n"
"buffer_load_dwordx4 a[188:191], v12, s[20:23], s35 offen sc1\n"
"buffer_load_dwordx4 a[192:195], v12, s[20:23], s35 offen offset:1024 sc1\n"
"buffer_load_dwordx4 a[196:199], v12, s[20:23], s35 offen offset:2048 sc1\n"
"buffer_load_dwordx4 a[200:203], v12, s[20:23], s35 offen offset:3072 sc1\n"
"buffer_load_dwordx4 a[204:207], v52, s[20:23], s35 offen sc1\n"
"s_waitcnt vmcnt(18) lgkmcnt(0)\n"
"s_barrier\n"
"v_mfma_f32_16x16x32_bf16 a[12:15], v[24:27], a[208:211], a[12:15]\n"
"ds_read_b128 v[16:19], v66 offset:0\n"
"v_mfma_f32_16x16x32_bf16 a[12:15], v[28:31], a[224:227], a[12:15]\n"
"ds_read_b64_tr_b16 a[80:81], v64 offset:0\n"
"v_mfma_f32_16x16x32_bf16 a[32:35], v[28:31], a[228:231], a[32:35]\n"
"ds_read_b64_tr_b16 a[82:83], v65 offset:0\n"
"v_mfma_f32_16x16x32_bf16 a[32:35], v[24:27], a[212:215], a[32:35]\n"
"ds_read_b128 v[20:23], v66 offset:5120\n"
"v_mfma_f32_16x16x32_bf16 a[52:55], v[24:27], a[216:219], a[52:55]\n"
"ds_read_b64_tr_b16 a[96:97], v64 offset:4096\n"
"v_mfma_f32_16x16x32_bf16 a[52:55], v[28:31], a[232:235], a[52:55]\n"
"ds_read_b64_tr_b16 a[98:99], v65 offset:4096\n"
"v_mfma_f32_16x16x32_bf16 a[72:75], v[28:31], a[236:239], a[72:75]\n"
"ds_read_b64_tr_b16 a[100:101], v64 offset:5120\n"
"v_mfma_f32_16x16x32_bf16 a[72:75], v[24:27], a[220:223], a[72:75]\n"
"ds_read_b64_tr_b16 a[102:103], v65 offset:5120\n"
"v_mfma_f32_16x16x32_bf16 a[16:19], v[70:73], a[208:211], a[16:19]\n"
"ds_read_b64_tr_b16 a[84:85], v64 offset:1024\n"
"v_mfma_f32_16x16x32_bf16 a[16:19], v[74:77], a[224:227], a[16:19]\n"
"ds_read_b64_tr_b16 a[86:87], v65 offset:1024\n"
"v_mfma_f32_16x16x32_bf16 a[36:39], v[74:77], a[228:231], a[36:39]\n"
"ds_read_b64_tr_b16 a[88:89], v64 offset:2048\n"
"v_mfma_f32_16x16x32_bf16 a[36:39], v[70:73], a[212:215], a[36:39]\n"
"ds_read_b64_tr_b16 a[90:91], v65 offset:2048\n"
"v_mfma_f32_16x16x32_bf16 a[56:59], v[70:73], a[216:219], a[56:59]\n"
"ds_read_b64_tr_b16 a[104:105], v64 offset:6144\n"
"v_mfma_f32_16x16x32_bf16 a[56:59], v[74:77], a[232:235], a[56:59]\n"
"ds_read_b64_tr_b16 a[106:107], v65 offset:6144\n"
"v_mfma_f32_16x16x32_bf16 a[76:79], v[74:77], a[236:239], a[76:79]\n"
"ds_read_b64_tr_b16 a[108:109], v64 offset:7168\n"
"v_mfma_f32_16x16x32_bf16 a[76:79], v[70:73], a[220:223], a[76:79]\n"
"ds_read_b64_tr_b16 a[110:111], v65 offset:7168\n"
"s_waitcnt lgkmcnt(6)\n"
"v_mfma_f32_16x16x32_bf16 a[0:3], v[16:19], a[80:83], a[0:3]\n"
"ds_read_b64_tr_b16 a[92:93], v64 offset:3072\n"
"v_mfma_f32_16x16x32_bf16 a[0:3], v[20:23], a[96:99], a[0:3]\n"
"ds_read_b64_tr_b16 a[94:95], v65 offset:3072\n"
"v_mfma_f32_16x16x32_bf16 a[20:23], v[20:23], a[100:103], a[20:23]\n"
"ds_read_b128 v[24:27], v66 offset:1024\n"
"v_mfma_f32_16x16x32_bf16 a[20:23], v[16:19], a[84:87], a[20:23]\n"
"s_waitcnt lgkmcnt(3)\n"
"ds_read_b128 v[28:31], v66 offset:6144\n"
"v_mfma_f32_16x16x32_bf16 a[40:43], v[16:19], a[88:91], a[40:43]\n"
"s_mov_b32 s31, 0x140000\n"
"s_add_u32 m0, s63, 0x14000\n"
"s_nop 0\n"
"buffer_load_dwordx4 v44, s[16:19], s31 offen lds\n"
"v_mfma_f32_16x16x32_bf16 a[40:43], v[20:23], a[104:107], a[40:43]\n"
"s_waitcnt lgkmcnt(2)\n"
"buffer_load_dwordx4 v45, s[16:19], s31 offen offset:1024 lds\n"
"v_mfma_f32_16x16x32_bf16 a[60:63], v[20:23], a[108:111], a[60:63]\n"
"buffer_load_dwordx4 v46, s[16:19], s31 offen offset:2048 lds\n"
"v_mfma_f32_16x16x32_bf16 a[60:63], v[16:19], a[92:95], a[60:63]\n"
"s_waitcnt lgkmcnt(0)\n"
"v_mfma_f32_16x16x32_bf16 a[4:7], v[24:27], a[80:83], a[4:7]\n"
"ds_read_b128 v[70:73], v66 offset:2048\n"
"v_mfma_f32_16x16x32_bf16 a[4:7], v[28:31], a[96:99], a[4:7]\n"
"ds_read_b128 v[74:77], v66 offset:7168\n"
"v_mfma_f32_16x16x32_bf16 a[24:27], v[28:31], a[100:103], a[24:27]\n"
"buffer_load_dwordx4 v47, s[16:19], s31 offen offset:3072 lds\n"
"v_mfma_f32_16x16x32_bf16 a[24:27], v[24:27], a[84:87], a[24:27]\n"
"s_mov_b32 s35, 0xc800\n"
"buffer_load_dwordx4 a[208:211], v12, s[20:23], s35 offen sc1\n"
"v_mfma_f32_16x16x32_bf16 a[44:47], v[24:27], a[88:91], a[44:47]\n"
"buffer_load_dwordx4 a[212:215], v12, s[20:23], s35 offen offset:1024 sc1\n"
"v_mfma_f32_16x16x32_bf16 a[44:47], v[28:31], a[104:107], a[44:47]\n"
"buffer_load_dwordx4 a[216:219], v12, s[20:23], s35 offen offset:2048 sc1\n"
"v_mfma_f32_16x16x32_bf16 a[64:67], v[28:31], a[108:111], a[64:67]\n"
"buffer_load_dwordx4 a[220:223], v12, s[20:23], s35 offen offset:3072 sc1\n"
"v_mfma_f32_16x16x32_bf16 a[64:67], v[24:27], a[92:95], a[64:67]\n"
"s_waitcnt lgkmcnt(0)\n"
"v_mfma_f32_16x16x32_bf16 a[8:11], v[70:73], a[80:83], a[8:11]\n"
"ds_read_b128 v[16:19], v66 offset:3072\n"
"v_accvgpr_read_b32 v220, a0\n"
"v_accvgpr_read_b32 v221, a1\n"
"v_accvgpr_read_b32 v222, a2\n"
"v_accvgpr_read_b32 v223, a3\n"
"v_mfma_f32_16x16x32_bf16 a[8:11], v[74:77], a[96:99], a[8:11]\n"
"ds_read_b128 v[20:23], v66 offset:8192\n"
"v_accvgpr_read_b32 v224, a4\n"
"v_accvgpr_read_b32 v225, a5\n"
"v_accvgpr_read_b32 v226, a6\n"
"v_accvgpr_read_b32 v227, a7\n"
"v_mfma_f32_16x16x32_bf16 a[28:31], v[74:77], a[100:103], a[28:31]\n"
"ds_read_b128 v[24:27], v66 offset:4096\n"
"v_cvt_pk_bf16_f32 v228, v220, v221\n"
"v_cvt_pk_bf16_f32 v229, v222, v223\n"
"v_cvt_pk_bf16_f32 v230, v224, v225\n"
"v_cvt_pk_bf16_f32 v231, v226, v227\n"
"v_mfma_f32_16x16x32_bf16 a[28:31], v[70:73], a[84:87], a[28:31]\n"
"ds_read_b128 v[28:31], v66 offset:9216\n"
"s_mov_b32 s62, 0\n"
"buffer_store_dwordx4 v[228:231], v12, s[72:75], s62 offen sc1\n"
"v_accvgpr_read_b32 v232, a20\n"
"v_accvgpr_read_b32 v233, a21\n"
"v_mfma_f32_16x16x32_bf16 a[48:51], v[70:73], a[88:91], a[48:51]\n"
"buffer_load_dwordx4 a[224:227], v52, s[20:23], s35 offen sc1\n"
"v_accvgpr_read_b32 v234, a22\n"
"v_accvgpr_read_b32 v235, a23\n"
"v_accvgpr_read_b32 v236, a24\n"
"v_accvgpr_read_b32 v237, a25\n"
"v_mfma_f32_16x16x32_bf16 a[48:51], v[74:77], a[104:107], a[48:51]\n"
"s_mov_b32 s31, 0x160000\n"
"s_add_u32 m0, s63, 0x19000\n"
"s_nop 0\n"
"buffer_load_dwordx4 v44, s[16:19], s31 offen lds\n"
"v_accvgpr_read_b32 v238, a26\n"
"v_accvgpr_read_b32 v239, a27\n"
"v_cvt_pk_bf16_f32 v240, v232, v233\n"
"v_cvt_pk_bf16_f32 v241, v234, v235\n"
"v_mfma_f32_16x16x32_bf16 a[68:71], v[74:77], a[108:111], a[68:71]\n"
"buffer_load_dwordx4 v45, s[16:19], s31 offen offset:1024 lds\n"
"v_cvt_pk_bf16_f32 v242, v236, v237\n"
"v_cvt_pk_bf16_f32 v243, v238, v239\n"
"s_mov_b32 s62, 2560\n"
"buffer_store_dwordx4 v[240:243], v12, s[72:75], s62 offen sc1\n"
"v_mfma_f32_16x16x32_bf16 a[68:71], v[70:73], a[92:95], a[68:71]\n"
"buffer_load_dwordx4 v46, s[16:19], s31 offen offset:2048 lds\n"
"v_accvgpr_read_b32 v244, a40\n"
"v_accvgpr_read_b32 v245, a41\n"
"v_accvgpr_read_b32 v246, a42\n"
"v_accvgpr_read_b32 v247, a43\n"
"buffer_load_dwordx4 v47, s[16:19], s31 offen offset:3072 lds\n"
"s_mov_b32 s35, 0xdc00\n"
"buffer_load_dwordx4 a[228:231], v12, s[20:23], s35 offen sc1\n"
"buffer_load_dwordx4 a[232:235], v12, s[20:23], s35 offen offset:1024 sc1\n"
"buffer_load_dwordx4 a[236:239], v12, s[20:23], s35 offen offset:2048 sc1\n"
"buffer_load_dwordx4 a[240:243], v12, s[20:23], s35 offen offset:3072 sc1\n"
"buffer_load_dwordx4 a[244:247], v52, s[20:23], s35 offen sc1\n"
"s_waitcnt lgkmcnt(0)\n"
"s_barrier\n"
"v_mfma_f32_16x16x32_bf16 a[12:15], v[16:19], a[80:83], a[12:15]\n"
"v_accvgpr_read_b32 v248, a44\n"
"v_accvgpr_read_b32 v249, a45\n"
"v_accvgpr_read_b32 v250, a46\n"
"v_accvgpr_read_b32 v251, a47\n"
"v_mfma_f32_16x16x32_bf16 a[12:15], v[20:23], a[96:99], a[12:15]\n"
"v_cvt_pk_bf16_f32 v252, v244, v245\n"
"v_cvt_pk_bf16_f32 v253, v246, v247\n"
"v_cvt_pk_bf16_f32 v254, v248, v249\n"
"v_cvt_pk_bf16_f32 v255, v250, v251\n"
"v_mfma_f32_16x16x32_bf16 a[32:35], v[20:23], a[100:103], a[32:35]\n"
"s_mov_b32 s62, 5120\n"
"buffer_store_dwordx4 v[252:255], v12, s[72:75], s62 offen sc1\n"
"v_accvgpr_read_b32 v220, a60\n"
"v_accvgpr_read_b32 v221, a61\n"
"v_mfma_f32_16x16x32_bf16 a[32:35], v[16:19], a[84:87], a[32:35]\n"
"v_accvgpr_read_b32 v222, a62\n"
"v_accvgpr_read_b32 v223, a63\n"
"v_accvgpr_read_b32 v224, a64\n"
"v_accvgpr_read_b32 v225, a65\n"
"v_mfma_f32_16x16x32_bf16 a[52:55], v[16:19], a[88:91], a[52:55]\n"
"v_accvgpr_read_b32 v226, a66\n"
"v_accvgpr_read_b32 v227, a67\n"
"v_cvt_pk_bf16_f32 v228, v220, v221\n"
"v_cvt_pk_bf16_f32 v229, v222, v223\n"
"v_mfma_f32_16x16x32_bf16 a[52:55], v[20:23], a[104:107], a[52:55]\n"
"v_cvt_pk_bf16_f32 v230, v224, v225\n"
"v_cvt_pk_bf16_f32 v231, v226, v227\n"
"s_mov_b32 s62, 7680\n"
"buffer_store_dwordx4 v[228:231], v12, s[72:75], s62 offen sc1\n"
"v_mfma_f32_16x16x32_bf16 a[72:75], v[20:23], a[108:111], a[72:75]\n"
"v_mfma_f32_16x16x32_bf16 a[72:75], v[16:19], a[92:95], a[72:75]\n"
"v_mfma_f32_16x16x32_bf16 a[16:19], v[24:27], a[80:83], a[16:19]\n"
"v_accvgpr_read_b32 v220, a8\n"
"v_accvgpr_read_b32 v221, a9\n"
"v_accvgpr_read_b32 v222, a10\n"
"v_accvgpr_read_b32 v223, a11\n"
"v_mfma_f32_16x16x32_bf16 a[16:19], v[28:31], a[96:99], a[16:19]\n"
"v_accvgpr_read_b32 v224, a12\n"
"v_accvgpr_read_b32 v225, a13\n"
"v_accvgpr_read_b32 v226, a14\n"
"v_accvgpr_read_b32 v227, a15\n"
"v_mfma_f32_16x16x32_bf16 a[36:39], v[28:31], a[100:103], a[36:39]\n"
"v_cvt_pk_bf16_f32 v228, v220, v221\n"
"v_cvt_pk_bf16_f32 v229, v222, v223\n"
"v_cvt_pk_bf16_f32 v230, v224, v225\n"
"v_cvt_pk_bf16_f32 v231, v226, v227\n"
"v_mfma_f32_16x16x32_bf16 a[36:39], v[24:27], a[84:87], a[36:39]\n"
"s_mov_b32 s62, 0\n"
"buffer_store_dwordx4 v[228:231], v12, s[72:75], s62 offen offset:1024 sc1\n"
"v_accvgpr_read_b32 v232, a28\n"
"v_accvgpr_read_b32 v233, a29\n"
"v_mfma_f32_16x16x32_bf16 a[56:59], v[24:27], a[88:91], a[56:59]\n"
"v_accvgpr_read_b32 v234, a30\n"
"v_accvgpr_read_b32 v235, a31\n"
"v_accvgpr_read_b32 v236, a32\n"
"v_accvgpr_read_b32 v237, a33\n"
"v_mfma_f32_16x16x32_bf16 a[56:59], v[28:31], a[104:107], a[56:59]\n"
"v_accvgpr_read_b32 v238, a34\n"
"v_accvgpr_read_b32 v239, a35\n"
"v_cvt_pk_bf16_f32 v240, v232, v233\n"
"v_cvt_pk_bf16_f32 v241, v234, v235\n"
"v_mfma_f32_16x16x32_bf16 a[76:79], v[28:31], a[108:111], a[76:79]\n"
"v_cvt_pk_bf16_f32 v242, v236, v237\n"
"v_cvt_pk_bf16_f32 v243, v238, v239\n"
"s_mov_b32 s62, 2560\n"
"buffer_store_dwordx4 v[240:243], v12, s[72:75], s62 offen offset:1024 sc1\n"
"v_mfma_f32_16x16x32_bf16 a[76:79], v[24:27], a[92:95], a[76:79]\n"
"v_accvgpr_read_b32 v244, a48\n"
"v_accvgpr_read_b32 v245, a49\n"
"v_accvgpr_read_b32 v246, a50\n"
"v_accvgpr_read_b32 v247, a51\n"
"v_accvgpr_read_b32 v248, a52\n"
"v_accvgpr_read_b32 v249, a53\n"
"v_accvgpr_read_b32 v250, a54\n"
"v_accvgpr_read_b32 v251, a55\n"
"v_cvt_pk_bf16_f32 v252, v244, v245\n"
"v_cvt_pk_bf16_f32 v253, v246, v247\n"
"v_cvt_pk_bf16_f32 v254, v248, v249\n"
"v_cvt_pk_bf16_f32 v255, v250, v251\n"
"s_mov_b32 s62, 5120\n"
"buffer_store_dwordx4 v[252:255], v12, s[72:75], s62 offen offset:1024 sc1\n"
"v_accvgpr_read_b32 v220, a68\n"
"v_accvgpr_read_b32 v221, a69\n"
"v_accvgpr_read_b32 v222, a70\n"
"v_accvgpr_read_b32 v223, a71\n"
"v_accvgpr_read_b32 v224, a72\n"
"v_accvgpr_read_b32 v225, a73\n"
"v_accvgpr_read_b32 v226, a74\n"
"v_accvgpr_read_b32 v227, a75\n"
"v_cvt_pk_bf16_f32 v228, v220, v221\n"
"v_cvt_pk_bf16_f32 v229, v222, v223\n"
"v_cvt_pk_bf16_f32 v230, v224, v225\n"
"v_cvt_pk_bf16_f32 v231, v226, v227\n"
"s_mov_b32 s62, 7680\n"
"buffer_store_dwordx4 v[228:231], v12, s[72:75], s62 offen offset:1024 sc1\n"
"v_accvgpr_read_b32 v220, a16\n"
"v_accvgpr_read_b32 v221, a17\n"
"v_accvgpr_read_b32 v222, a18\n"
"v_accvgpr_read_b32 v223, a19\n"
"v_cvt_pk_bf16_f32 v228, v220, v221\n"
"v_cvt_pk_bf16_f32 v229, v222, v223\n"
"s_mov_b32 s62, 0\n"
"buffer_store_dwordx2 v[228:229], v68, s[72:75], s62 offen offset:2048 sc1\n"
"v_accvgpr_read_b32 v232, a36\n"
"v_accvgpr_read_b32 v233, a37\n"
"v_accvgpr_read_b32 v234, a38\n"
"v_accvgpr_read_b32 v235, a39\n"
"v_cvt_pk_bf16_f32 v240, v232, v233\n"
"v_cvt_pk_bf16_f32 v241, v234, v235\n"
"s_mov_b32 s62, 2560\n"
"buffer_store_dwordx2 v[240:241], v68, s[72:75], s62 offen offset:2048 sc1\n"
"v_accvgpr_read_b32 v244, a56\n"
"v_accvgpr_read_b32 v245, a57\n"
"v_accvgpr_read_b32 v246, a58\n"
"v_accvgpr_read_b32 v247, a59\n"
"v_cvt_pk_bf16_f32 v252, v244, v245\n"
"v_cvt_pk_bf16_f32 v253, v246, v247\n"
"s_mov_b32 s62, 5120\n"
"buffer_store_dwordx2 v[252:253], v68, s[72:75], s62 offen offset:2048 sc1\n"
"v_accvgpr_read_b32 v220, a76\n"
"v_accvgpr_read_b32 v221, a77\n"
"v_accvgpr_read_b32 v222, a78\n"
"v_accvgpr_read_b32 v223, a79\n"
"v_cvt_pk_bf16_f32 v228, v220, v221\n"
"v_cvt_pk_bf16_f32 v229, v222, v223\n"
"s_mov_b32 s62, 7680\n"
"buffer_store_dwordx2 v[228:229], v68, s[72:75], s62 offen offset:2048 sc1\n"
"s_waitcnt vmcnt(30)\n"
"ds_read_b64_tr_b16 v[16:17], v208 offset:40960\n"
"ds_read_b64_tr_b16 v[18:19], v208 offset:41472\n"
"ds_read_b64_tr_b16 v[20:21], v208 offset:61440\n"
"ds_read_b64_tr_b16 v[22:23], v208 offset:61952\n"
"s_waitcnt lgkmcnt(0)\n"
"v_mfma_f32_16x16x32_bf16 v[128:131], a[168:171], v[16:19], v[128:131]\n"
"ds_read_b64_tr_b16 v[24:25], v209 offset:40960\n"
"v_mfma_f32_16x16x32_bf16 v[128:131], a[188:191], v[20:23], v[128:131]\n"
"ds_read_b64_tr_b16 v[26:27], v209 offset:41472\n"
"v_mfma_f32_16x16x32_bf16 v[132:135], a[192:195], v[20:23], v[132:135]\n"
"ds_read_b64_tr_b16 v[28:29], v209 offset:61440\n"
"v_mfma_f32_16x16x32_bf16 v[132:135], a[172:175], v[16:19], v[132:135]\n"
"ds_read_b64_tr_b16 v[30:31], v209 offset:61952\n"
"v_mfma_f32_16x16x32_bf16 v[136:139], a[176:179], v[16:19], v[136:139]\n"
"v_mfma_f32_16x16x32_bf16 v[136:139], a[196:199], v[20:23], v[136:139]\n"
"v_mfma_f32_16x16x32_bf16 v[140:143], a[200:203], v[20:23], v[140:143]\n"
"v_mfma_f32_16x16x32_bf16 v[140:143], a[180:183], v[16:19], v[140:143]\n"
"v_mfma_f32_16x16x32_bf16 v[144:147], a[184:187], v[16:19], v[144:147]\n"
"v_mfma_f32_16x16x32_bf16 v[144:147], a[204:207], v[20:23], v[144:147]\n"
"s_waitcnt lgkmcnt(0)\n"
"v_mfma_f32_16x16x32_bf16 v[148:151], a[168:171], v[24:27], v[148:151]\n"
"ds_read_b64_tr_b16 v[16:17], v210 offset:40960\n"
"v_mfma_f32_16x16x32_bf16 v[148:151], a[188:191], v[28:31], v[148:151]\n"
"ds_read_b64_tr_b16 v[18:19], v210 offset:41472\n"
"v_mfma_f32_16x16x32_bf16 v[152:155], a[192:195], v[28:31], v[152:155]\n"
"ds_read_b64_tr_b16 v[20:21], v210 offset:61440\n"
"v_mfma_f32_16x16x32_bf16 v[152:155], a[172:175], v[24:27], v[152:155]\n"
"ds_read_b64_tr_b16 v[22:23], v210 offset:61952\n"
"v_mfma_f32_16x16x32_bf16 v[156:159], a[176:179], v[24:27], v[156:159]\n"
"v_mfma_f32_16x16x32_bf16 v[156:159], a[196:199], v[28:31], v[156:159]\n"
"v_mfma_f32_16x16x32_bf16 v[160:163], a[200:203], v[28:31], v[160:163]\n"
"v_mfma_f32_16x16x32_bf16 v[160:163], a[180:183], v[24:27], v[160:163]\n"
"v_mfma_f32_16x16x32_bf16 v[164:167], a[184:187], v[24:27], v[164:167]\n"
"v_mfma_f32_16x16x32_bf16 v[164:167], a[204:207], v[28:31], v[164:167]\n"
"s_waitcnt lgkmcnt(0)\n"
"v_mfma_f32_16x16x32_bf16 v[168:171], a[168:171], v[16:19], v[168:171]\n"
"ds_read_b64_tr_b16 v[24:25], v211 offset:40960\n"
"v_mfma_f32_16x16x32_bf16 v[168:171], a[188:191], v[20:23], v[168:171]\n"
"ds_read_b64_tr_b16 v[26:27], v211 offset:41472\n"
"v_mfma_f32_16x16x32_bf16 v[172:175], a[192:195], v[20:23], v[172:175]\n"
"ds_read_b64_tr_b16 v[28:29], v211 offset:61440\n"
"v_mfma_f32_16x16x32_bf16 v[172:175], a[172:175], v[16:19], v[172:175]\n"
"ds_read_b64_tr_b16 v[30:31], v211 offset:61952\n"
"v_mfma_f32_16x16x32_bf16 v[176:179], a[176:179], v[16:19], v[176:179]\n"
"v_mfma_f32_16x16x32_bf16 v[176:179], a[196:199], v[20:23], v[176:179]\n"
"v_mfma_f32_16x16x32_bf16 v[180:183], a[200:203], v[20:23], v[180:183]\n"
"v_mfma_f32_16x16x32_bf16 v[180:183], a[180:183], v[16:19], v[180:183]\n"
"v_mfma_f32_16x16x32_bf16 v[184:187], a[184:187], v[16:19], v[184:187]\n"
"v_mfma_f32_16x16x32_bf16 v[184:187], a[204:207], v[20:23], v[184:187]\n"
"s_waitcnt lgkmcnt(0)\n"
"v_mfma_f32_16x16x32_bf16 v[188:191], a[168:171], v[24:27], v[188:191]\n"
"v_mfma_f32_16x16x32_bf16 v[188:191], a[188:191], v[28:31], v[188:191]\n"
"v_mfma_f32_16x16x32_bf16 v[192:195], a[192:195], v[28:31], v[192:195]\n"
"v_mfma_f32_16x16x32_bf16 v[192:195], a[172:175], v[24:27], v[192:195]\n"
"s_waitcnt vmcnt(0)\n"
"v_lshrrev_b32 v54, 1, v12\n"
"s_mov_b64 exec, 0xff\n"
"buffer_store_dwordx2 v[56:57], v54, s[92:95], s70 offen sc1\n"
"s_mov_b64 exec, -1\n"
"v_mfma_f32_16x16x32_bf16 v[196:199], a[176:179], v[24:27], v[196:199]\n"
"s_mov_b32 s31, 0x180000\n"
"s_add_u32 m0, s63, 0xa000\n"
"s_nop 0\n"
"buffer_load_dwordx4 v44, s[16:19], s31 offen lds\n"
"v_mfma_f32_16x16x32_bf16 v[196:199], a[196:199], v[28:31], v[196:199]\n"
"buffer_load_dwordx4 v45, s[16:19], s31 offen offset:1024 lds\n"
"v_mfma_f32_16x16x32_bf16 v[200:203], a[200:203], v[28:31], v[200:203]\n"
"buffer_load_dwordx4 v46, s[16:19], s31 offen offset:2048 lds\n"
"v_mfma_f32_16x16x32_bf16 v[200:203], a[180:183], v[24:27], v[200:203]\n"
"buffer_load_dwordx4 v47, s[16:19], s31 offen offset:3072 lds\n"
"v_mfma_f32_16x16x32_bf16 v[204:207], a[184:187], v[24:27], v[204:207]\n"
"s_mov_b32 s35, 0xf000\n"
"buffer_load_dwordx4 a[128:131], v12, s[20:23], s35 offen sc1\n"
"v_mfma_f32_16x16x32_bf16 v[204:207], a[204:207], v[28:31], v[204:207]\n"
"buffer_load_dwordx4 a[132:135], v12, s[20:23], s35 offen offset:1024 sc1\n"
"buffer_load_dwordx4 a[136:139], v12, s[20:23], s35 offen offset:2048 sc1\n"
"buffer_load_dwordx4 a[140:143], v12, s[20:23], s35 offen offset:3072 sc1\n"
"buffer_load_dwordx4 a[144:147], v52, s[20:23], s35 offen sc1\n"
"s_mov_b32 s31, 0x1a0000\n"
"s_add_u32 m0, s63, 0xf000\n"
"s_nop 0\n"
"buffer_load_dwordx4 v44, s[16:19], s31 offen lds\n"
"buffer_load_dwordx4 v45, s[16:19], s31 offen offset:1024 lds\n"
"buffer_load_dwordx4 v46, s[16:19], s31 offen offset:2048 lds\n"
"buffer_load_dwordx4 v47, s[16:19], s31 offen offset:3072 lds\n"
"s_mov_b32 s35, 0x10400\n"
"buffer_load_dwordx4 a[148:151], v12, s[20:23], s35 offen sc1\n"
"buffer_load_dwordx4 a[152:155], v12, s[20:23], s35 offen offset:1024 sc1\n"
"buffer_load_dwordx4 a[156:159], v12, s[20:23], s35 offen offset:2048 sc1\n"
"buffer_load_dwordx4 a[160:163], v12, s[20:23], s35 offen offset:3072 sc1\n"
"buffer_load_dwordx4 a[164:167], v52, s[20:23], s35 offen sc1\n"
"s_waitcnt vmcnt(29)\n"
"ds_read_b64_tr_b16 v[16:17], v208 offset:0\n"
"ds_read_b64_tr_b16 v[18:19], v208 offset:512\n"
"ds_read_b64_tr_b16 v[20:21], v208 offset:20480\n"
"ds_read_b64_tr_b16 v[22:23], v208 offset:20992\n"
"s_waitcnt lgkmcnt(0)\n"
"v_mfma_f32_16x16x32_bf16 v[128:131], a[208:211], v[16:19], v[128:131]\n"
"ds_read_b64_tr_b16 v[24:25], v209 offset:0\n"
"v_mfma_f32_16x16x32_bf16 v[128:131], a[228:231], v[20:23], v[128:131]\n"
"ds_read_b64_tr_b16 v[26:27], v209 offset:512\n"
"v_mfma_f32_16x16x32_bf16 v[132:135], a[232:235], v[20:23], v[132:135]\n"
"ds_read_b64_tr_b16 v[28:29], v209 offset:20480\n"
"v_mfma_f32_16x16x32_bf16 v[132:135], a[212:215], v[16:19], v[132:135]\n"
"ds_read_b64_tr_b16 v[30:31], v209 offset:20992\n"
"v_mfma_f32_16x16x32_bf16 v[136:139], a[216:219], v[16:19], v[136:139]\n"
"v_mfma_f32_16x16x32_bf16 v[136:139], a[236:239], v[20:23], v[136:139]\n"
"v_mfma_f32_16x16x32_bf16 v[140:143], a[240:243], v[20:23], v[140:143]\n"
"v_mfma_f32_16x16x32_bf16 v[140:143], a[220:223], v[16:19], v[140:143]\n"
"v_mfma_f32_16x16x32_bf16 v[144:147], a[224:227], v[16:19], v[144:147]\n"
"v_mfma_f32_16x16x32_bf16 v[144:147], a[244:247], v[20:23], v[144:147]\n"
"s_waitcnt lgkmcnt(0)\n"
"v_mfma_f32_16x16x32_bf16 v[148:151], a[208:211], v[24:27], v[148:151]\n"
"ds_read_b64_tr_b16 v[16:17], v210 offset:0\n"
"v_mfma_f32_16x16x32_bf16 v[148:151], a[228:231], v[28:31], v[148:151]\n"
"ds_read_b64_tr_b16 v[18:19], v210 offset:512\n"
"v_mfma_f32_16x16x32_bf16 v[152:155], a[232:235], v[28:31], v[152:155]\n"
"ds_read_b64_tr_b16 v[20:21], v210 offset:20480\n"
"v_mfma_f32_16x16x32_bf16 v[152:155], a[212:215], v[24:27], v[152:155]\n"
"ds_read_b64_tr_b16 v[22:23], v210 offset:20992\n"
"v_mfma_f32_16x16x32_bf16 v[156:159], a[216:219], v[24:27], v[156:159]\n"
"v_mfma_f32_16x16x32_bf16 v[156:159], a[236:239], v[28:31], v[156:159]\n"
"v_mfma_f32_16x16x32_bf16 v[160:163], a[240:243], v[28:31], v[160:163]\n"
"v_mfma_f32_16x16x32_bf16 v[160:163], a[220:223], v[24:27], v[160:163]\n"
"v_mfma_f32_16x16x32_bf16 v[164:167], a[224:227], v[24:27], v[164:167]\n"
"v_mfma_f32_16x16x32_bf16 v[164:167], a[244:247], v[28:31], v[164:167]\n"
"s_waitcnt lgkmcnt(0)\n"
"v_mfma_f32_16x16x32_bf16 v[168:171], a[208:211], v[16:19], v[168:171]\n"
"ds_read_b64_tr_b16 v[24:25], v211 offset:0\n"
"v_mfma_f32_16x16x32_bf16 v[168:171], a[228:231], v[20:23], v[168:171]\n"
"ds_read_b64_tr_b16 v[26:27], v211 offset:512\n"
"v_mfma_f32_16x16x32_bf16 v[172:175], a[232:235], v[20:23], v[172:175]\n"
"ds_read_b64_tr_b16 v[28:29], v211 offset:20480\n"
"v_mfma_f32_16x16x32_bf16 v[172:175], a[212:215], v[16:19], v[172:175]\n"
"ds_read_b64_tr_b16 v[30:31], v211 offset:20992\n"
"v_mfma_f32_16x16x32_bf16 v[176:179], a[216:219], v[16:19], v[176:179]\n"
"v_mfma_f32_16x16x32_bf16 v[176:179], a[236:239], v[20:23], v[176:179]\n"
"v_mfma_f32_16x16x32_bf16 v[180:183], a[240:243], v[20:23], v[180:183]\n"
"v_mfma_f32_16x16x32_bf16 v[180:183], a[220:223], v[16:19], v[180:183]\n"
"v_mfma_f32_16x16x32_bf16 v[184:187], a[224:227], v[16:19], v[184:187]\n"
"v_mfma_f32_16x16x32_bf16 v[184:187], a[244:247], v[20:23], v[184:187]\n"
"s_waitcnt lgkmcnt(0)\n"
"v_mfma_f32_16x16x32_bf16 v[188:191], a[208:211], v[24:27], v[188:191]\n"
"v_mfma_f32_16x16x32_bf16 v[188:191], a[228:231], v[28:31], v[188:191]\n"
"v_mfma_f32_16x16x32_bf16 v[192:195], a[232:235], v[28:31], v[192:195]\n"
"v_mfma_f32_16x16x32_bf16 v[192:195], a[212:215], v[24:27], v[192:195]\n"
"v_mfma_f32_16x16x32_bf16 v[196:199], a[216:219], v[24:27], v[196:199]\n"
"v_mfma_f32_16x16x32_bf16 v[196:199], a[236:239], v[28:31], v[196:199]\n"
"v_mfma_f32_16x16x32_bf16 v[200:203], a[240:243], v[28:31], v[200:203]\n"
"v_mfma_f32_16x16x32_bf16 v[200:203], a[220:223], v[24:27], v[200:203]\n"
"v_mfma_f32_16x16x32_bf16 v[204:207], a[224:227], v[24:27], v[204:207]\n"
"v_mfma_f32_16x16x32_bf16 v[204:207], a[244:247], v[28:31], v[204:207]\n"
"s_waitcnt vmcnt(0)\n"
"buffer_load_dwordx2 v[54:55], v69, s[92:95], 0 offen sc1\n"
"ds_read_b64_tr_b16 v[16:17], v48 offset:40960\n"
"ds_read_b64_tr_b16 v[18:19], v48 offset:41472\n"
"ds_read_b64_tr_b16 v[20:21], v48 offset:61440\n"
"ds_read_b64_tr_b16 v[22:23], v48 offset:61952\n"
"s_waitcnt lgkmcnt(0)\n"
"v_mfma_f32_16x16x32_bf16 v[128:131], a[128:131], v[16:19], v[128:131]\n"
"ds_read_b64_tr_b16 v[24:25], v49 offset:40960\n"
"v_mfma_f32_16x16x32_bf16 v[128:131], a[148:151], v[20:23], v[128:131]\n"
"ds_read_b64_tr_b16 v[26:27], v49 offset:41472\n"
"v_mfma_f32_16x16x32_bf16 v[132:135], a[152:155], v[20:23], v[132:135]\n"
"ds_read_b64_tr_b16 v[28:29], v49 offset:61440\n"
"v_mfma_f32_16x16x32_bf16 v[132:135], a[132:135], v[16:19], v[132:135]\n"
"ds_read_b64_tr_b16 v[30:31], v49 offset:61952\n"
"v_mfma_f32_16x16x32_bf16 v[136:139], a[136:139], v[16:19], v[136:139]\n"
"v_mfma_f32_16x16x32_bf16 v[136:139], a[156:159], v[20:23], v[136:139]\n"
"v_mfma_f32_16x16x32_bf16 v[140:143], a[160:163], v[20:23], v[140:143]\n"
"s_waitcnt vmcnt(0)\n"
"v_cmp_eq_u64 vcc, s[88:89], v[54:55]\n"
"s_mov_b32 s91, 0\n"
"s_nop 2\n"
"s_cmp_eq_u64 vcc, -1\n"
"s_cbranch_scc1 L_OK5%=\n"
"L_SP5%=:\n"
"s_sleep 2\n"
"buffer_load_dwordx2 v[54:55], v69, s[92:95], 0 offen sc1\n"
"s_waitcnt vmcnt(0)\n"
"v_cmp_eq_u64 vcc, s[88:89], v[54:55]\n"
"s_add_u32 s91, s91, 1\n"
"s_nop 3\n"
"s_cmp_eq_u64 vcc, -1\n"
"s_cbranch_scc1 L_OK5%=\n"
"s_cmp_lt_u32 s91, 0x40000\n"
"s_cbranch_scc1 L_SP5%=\n"
"L_OK5%=:\n"
"s_mov_b32 s62, 0\n"
"buffer_load_dwordx4 v[216:219], v12, s[84:87], s62 offen sc1\n"
"buffer_load_dwordx4 v[220:223], v12, s[84:87], s62 offen offset:1024 sc1\n"
"buffer_load_dwordx2 v[224:225], v68, s[84:87], s62 offen offset:2048 sc1\n"
"s_mov_b32 s62, 10240\n"
"buffer_load_dwordx4 v[226:229], v12, s[84:87], s62 offen sc1\n"
"buffer_load_dwordx4 v[230:233], v12, s[84:87], s62 offen offset:1024 sc1\n"
"buffer_load_dwordx2 v[234:235], v68, s[84:87], s62 offen offset:2048 sc1\n"
"s_mov_b32 s62, 20480\n"
"buffer_load_dwordx4 v[236:239], v12, s[84:87], s62 offen sc1\n"
"buffer_load_dwordx4 v[240:243], v12, s[84:87], s62 offen offset:1024 sc1\n"
"buffer_load_dwordx2 v[244:245], v68, s[84:87], s62 offen offset:2048 sc1\n"
"s_mov_b32 s62, 30720\n"
"buffer_load_dwordx4 v[246:249], v12, s[84:87], s62 offen sc1\n"
"buffer_load_dwordx4 v[250:253], v12, s[84:87], s62 offen offset:1024 sc1\n"
"buffer_load_dwordx2 v[254:255], v68, s[84:87], s62 offen offset:2048 sc1\n"
"v_mfma_f32_16x16x32_bf16 v[140:143], a[140:143], v[16:19], v[140:143]\n"
"v_mfma_f32_16x16x32_bf16 v[144:147], a[144:147], v[16:19], v[144:147]\n"
"v_mfma_f32_16x16x32_bf16 v[144:147], a[164:167], v[20:23], v[144:147]\n"
"s_waitcnt lgkmcnt(0)\n"
"v_mfma_f32_16x16x32_bf16 v[148:151], a[128:131], v[24:27], v[148:151]\n"
"ds_read_b64_tr_b16 v[16:17], v50 offset:40960\n"
"v_mfma_f32_16x16x32_bf16 v[148:151], a[148:151], v[28:31], v[148:151]\n"
"ds_read_b64_tr_b16 v[18:19], v50 offset:41472\n"
"v_mfma_f32_16x16x32_bf16 v[152:155], a[152:155], v[28:31], v[152:155]\n"
"ds_read_b64_tr_b16 v[20:21], v50 offset:61440\n"
"v_mfma_f32_16x16x32_bf16 v[152:155], a[132:135], v[24:27], v[152:155]\n"
"ds_read_b64_tr_b16 v[22:23], v50 offset:61952\n"
"v_mfma_f32_16x16x32_bf16 v[156:159], a[136:139], v[24:27], v[156:159]\n"
"v_mfma_f32_16x16x32_bf16 v[156:159], a[156:159], v[28:31], v[156:159]\n"
"v_mfma_f32_16x16x32_bf16 v[160:163], a[160:163], v[28:31], v[160:163]\n"
"v_mfma_f32_16x16x32_bf16 v[160:163], a[140:143], v[24:27], v[160:163]\n"
"v_mfma_f32_16x16x32_bf16 v[164:167], a[144:147], v[24:27], v[164:167]\n"
"v_mfma_f32_16x16x32_bf16 v[164:167], a[164:167], v[28:31], v[164:167]\n"
"s_waitcnt lgkmcnt(0)\n"
"v_mfma_f32_16x16x32_bf16 v[168:171], a[128:131], v[16:19], v[168:171]\n"
"ds_read_b64_tr_b16 v[24:25], v51 offset:40960\n"
"v_mfma_f32_16x16x32_bf16 v[168:171], a[148:151], v[20:23], v[168:171]\n"
"ds_read_b64_tr_b16 v[26:27], v51 offset:41472\n"
"v_mfma_f32_16x16x32_bf16 v[172:175], a[152:155], v[20:23], v[172:175]\n"
"ds_read_b64_tr_b16 v[28:29], v51 offset:61440\n"
"v_mfma_f32_16x16x32_bf16 v[172:175], a[132:135], v[16:19], v[172:175]\n"
"ds_read_b64_tr_b16 v[30:31], v51 offset:61952\n"
"v_mfma_f32_16x16x32_bf16 v[176:179], a[136:139], v[16:19], v[176:179]\n"
"v_mfma_f32_16x16x32_bf16 v[176:179], a[156:159], v[20:23], v[176:179]\n"
"v_mfma_f32_16x16x32_bf16 v[180:183], a[160:163], v[20:23], v[180:183]\n"
"v_mfma_f32_16x16x32_bf16 v[180:183], a[140:143], v[16:19], v[180:183]\n"
"v_mfma_f32_16x16x32_bf16 v[184:187], a[144:147], v[16:19], v[184:187]\n"
"v_mfma_f32_16x16x32_bf16 v[184:187], a[164:167], v[20:23], v[184:187]\n"
"s_waitcnt lgkmcnt(0)\n"
"v_mfma_f32_16x16x32_bf16 v[188:191], a[128:131], v[24:27], v[188:191]\n"
"v_mfma_f32_16x16x32_bf16 v[188:191], a[148:151], v[28:31], v[188:191]\n"
"v_mfma_f32_16x16x32_bf16 v[192:195], a[152:155], v[28:31], v[192:195]\n"
"v_mfma_f32_16x16x32_bf16 v[192:195], a[132:135], v[24:27], v[192:195]\n"
"v_mfma_f32_16x16x32_bf16 v[196:199], a[136:139], v[24:27], v[196:199]\n"
"v_mfma_f32_16x16x32_bf16 v[196:199], a[156:159], v[28:31], v[196:199]\n"
"v_mfma_f32_16x16x32_bf16 v[200:203], a[160:163], v[28:31], v[200:203]\n"
"v_mfma_f32_16x16x32_bf16 v[200:203], a[140:143], v[24:27], v[200:203]\n"
"v_mfma_f32_16x16x32_bf16 v[204:207], a[144:147], v[24:27], v[204:207]\n"
"v_mfma_f32_16x16x32_bf16 v[204:207], a[164:167], v[28:31], v[204:207]\n"
"ds_write_b128 v82, v[148:151] offset:0\n"
"s_waitcnt vmcnt(63)\n"
"v_lshlrev_b32 v90, 16, v90\n"
"v_mov_b32 v91, v90\n"
"ds_write_b128 v82, v[152:155] offset:1024\n"
"v_pk_add_f32 v[128:129], v[128:129], v[90:91]\n"
"v_pk_add_f32 v[130:131], v[130:131], v[90:91]\n"
"v_pk_add_f32 v[132:133], v[132:133], v[90:91]\n"
"ds_write_b128 v82, v[156:159] offset:2048\n"
"v_pk_add_f32 v[134:135], v[134:135], v[90:91]\n"
"v_pk_add_f32 v[136:137], v[136:137], v[90:91]\n"
"v_pk_add_f32 v[138:139], v[138:139], v[90:91]\n"
"ds_write_b128 v82, v[160:163] offset:3072\n"
"v_pk_add_f32 v[140:141], v[140:141], v[90:91]\n"
"v_pk_add_f32 v[142:143], v[142:143], v[90:91]\n"
"v_pk_add_f32 v[144:145], v[144:145], v[90:91]\n"
"ds_write_b128 v82, v[164:167] offset:4096\n"
"v_pk_add_f32 v[146:147], v[146:147], v[90:91]\n"
"v_lshlrev_b32 v16, 16, v92\n"
"v_and_b32 v17, 0xffff0000, v92\n"
"ds_write_b128 v82, v[168:171] offset:5120\n"
"v_lshlrev_b32 v18, 16, v93\n"
"v_and_b32 v19, 0xffff0000, v93\n"
"v_lshlrev_b32 v20, 16, v94\n"
"ds_write_b128 v82, v[172:175] offset:6144\n"
"v_and_b32 v21, 0xffff0000, v94\n"
"v_lshlrev_b32 v22, 16, v95\n"
"v_and_b32 v23, 0xffff0000, v95\n"
"ds_write_b128 v82, v[176:179] offset:7168\n"
"v_lshlrev_b32 v24, 16, v96\n"
"v_and_b32 v25, 0xffff0000, v96\n"
"v_lshlrev_b32 v26, 16, v97\n"
"ds_write_b128 v82, v[180:183] offset:8192\n"
"v_and_b32 v27, 0xffff0000, v97\n"
"v_lshlrev_b32 v28, 16, v98\n"
"v_and_b32 v29, 0xffff0000, v98\n"
"ds_write_b128 v82, v[184:187] offset:9216\n"
"v_lshlrev_b32 v30, 16, v99\n"
"v_and_b32 v31, 0xffff0000, v99\n"
"v_lshlrev_b32 v44, 16, v100\n"
"ds_write_b128 v104, v[188:191] offset:0\n"
"v_and_b32 v45, 0xffff0000, v100\n"
"v_lshlrev_b32 v46, 16, v101\n"
"v_and_b32 v47, 0xffff0000, v101\n"
"ds_write_b128 v104, v[192:195] offset:1024\n"
"s_lshl_b32 s60, %1, 2\n"
"s_add_u32 s60, s60, s90\n"
"s_cmp_eq_u32 s60, 0\n"
"s_cselect_b32 s62, 0, 0x7fff0000\n"
"ds_write_b128 v104, v[196:199] offset:2048\n"
"v_lshrrev_b32 v52, 4, v13\n"
"v_lshl_add_u32 v52, v52, 3, v89\n"
"v_mov_b32 v48, 0xbfb8aa3b\n"
"ds_write_b128 v104, v[200:203] offset:3072\n"
"v_mov_b32 v49, v48\n"
"v_mov_b32 v50, 1.0\n"
"v_mov_b32 v51, 1.0\n"
"ds_write_b128 v104, v[204:207] offset:4096\n"
"s_waitcnt lgkmcnt(0)\n"
"s_barrier\n"
"ds_read_b128 v[148:151], v85 offset:0\n"
"ds_read_b128 v[152:155], v85 offset:1024\n"
"ds_read_b128 v[156:159], v85 offset:2048\n"
"ds_read_b128 v[160:163], v85 offset:3072\n"
"ds_read_b128 v[164:167], v85 offset:4096\n"
"ds_read_b128 v[168:171], v86 offset:0\n"
"ds_read_b128 v[172:175], v86 offset:1024\n"
"ds_read_b128 v[176:179], v86 offset:2048\n"
"ds_read_b128 v[180:183], v86 offset:3072\n"
"ds_read_b128 v[184:187], v86 offset:4096\n"
"ds_read_b128 v[188:191], v106 offset:0\n"
"ds_read_b128 v[192:195], v106 offset:1024\n"
"ds_read_b128 v[196:199], v106 offset:2048\n"
"ds_read_b128 v[200:203], v106 offset:3072\n"
"ds_read_b128 v[204:207], v106 offset:4096\n"
"s_waitcnt lgkmcnt(10)\n"
"v_pk_add_f32 v[128:129], v[128:129], v[148:149]\n"
"v_pk_add_f32 v[130:131], v[130:131], v[150:151]\n"
"v_pk_add_f32 v[132:133], v[132:133], v[152:153]\n"
"v_pk_add_f32 v[134:135], v[134:135], v[154:155]\n"
"v_pk_add_f32 v[136:137], v[136:137], v[156:157]\n"
"v_pk_add_f32 v[138:139], v[138:139], v[158:159]\n"
"v_pk_add_f32 v[140:141], v[140:141], v[160:161]\n"
"v_pk_add_f32 v[142:143], v[142:143], v[162:163]\n"
"v_pk_add_f32 v[144:145], v[144:145], v[164:165]\n"
"v_pk_add_f32 v[146:147], v[146:147], v[166:167]\n"
"s_waitcnt lgkmcnt(5)\n"
"v_pk_add_f32 v[128:129], v[128:129], v[168:169]\n"
"v_pk_add_f32 v[130:131], v[130:131], v[170:171]\n"
"v_pk_add_f32 v[132:133], v[132:133], v[172:173]\n"
"v_pk_add_f32 v[134:135], v[134:135], v[174:175]\n"
"v_pk_add_f32 v[136:137], v[136:137], v[176:177]\n"
"v_pk_add_f32 v[138:139], v[138:139], v[178:179]\n"
"v_pk_add_f32 v[140:141], v[140:141], v[180:181]\n"
"v_pk_add_f32 v[142:143], v[142:143], v[182:183]\n"
"v_pk_add_f32 v[144:145], v[144:145], v[184:185]\n"
"v_pk_add_f32 v[146:147], v[146:147], v[186:187]\n"
"s_waitcnt lgkmcnt(0)\n"
"v_pk_add_f32 v[128:129], v[128:129], v[188:189]\n"
"v_pk_add_f32 v[130:131], v[130:131], v[190:191]\n"
"v_pk_add_f32 v[132:133], v[132:133], v[192:193]\n"
"v_pk_add_f32 v[134:135], v[134:135], v[194:195]\n"
"v_pk_add_f32 v[136:137], v[136:137], v[196:197]\n"
"v_pk_add_f32 v[138:139], v[138:139], v[198:199]\n"
"v_pk_add_f32 v[140:141], v[140:141], v[200:201]\n"
"v_pk_add_f32 v[142:143], v[142:143], v[202:203]\n"
"v_pk_add_f32 v[144:145], v[144:145], v[204:205]\n"
"v_pk_add_f32 v[146:147], v[146:147], v[206:207]\n"
"v_pk_mul_f32 v[148:149], v[128:129], v[48:49]\n"
"v_pk_mul_f32 v[150:151], v[130:131], v[48:49]\n"
"v_pk_mul_f32 v[152:153], v[132:133], v[48:49]\n"
"v_pk_mul_f32 v[154:155], v[134:135], v[48:49]\n"
"v_pk_mul_f32 v[156:157], v[136:137], v[48:49]\n"
"v_pk_mul_f32 v[158:159], v[138:139], v[48:49]\n"
"v_pk_mul_f32 v[160:161], v[140:141], v[48:49]\n"
"v_pk_mul_f32 v[162:163], v[142:143], v[48:49]\n"
"v_pk_mul_f32 v[164:165], v[144:145], v[48:49]\n"
"v_pk_mul_f32 v[166:167], v[146:147], v[48:49]\n"
"v_exp_f32 v148, v148\n"
"v_exp_f32 v149, v149\n"
"v_exp_f32 v150, v150\n"
"v_exp_f32 v151, v151\n"
"v_exp_f32 v152, v152\n"
"v_exp_f32 v153, v153\n"
"v_exp_f32 v154, v154\n"
"v_exp_f32 v155, v155\n"
"v_exp_f32 v156, v156\n"
"v_exp_f32 v157, v157\n"
"v_exp_f32 v158, v158\n"
"v_exp_f32 v159, v159\n"
"v_exp_f32 v160, v160\n"
"v_exp_f32 v161, v161\n"
"v_exp_f32 v162, v162\n"
"v_exp_f32 v163, v163\n"
"v_exp_f32 v164, v164\n"
"v_exp_f32 v165, v165\n"
"v_exp_f32 v166, v166\n"
"v_exp_f32 v167, v167\n"
"v_pk_add_f32 v[148:149], v[148:149], v[50:51]\n"
"v_pk_add_f32 v[150:151], v[150:151], v[50:51]\n"
"v_pk_add_f32 v[152:153], v[152:153], v[50:51]\n"
"v_pk_add_f32 v[154:155], v[154:155], v[50:51]\n"
"v_pk_add_f32 v[156:157], v[156:157], v[50:51]\n"
"v_pk_add_f32 v[158:159], v[158:159], v[50:51]\n"
"v_pk_add_f32 v[160:161], v[160:161], v[50:51]\n"
"v_pk_add_f32 v[162:163], v[162:163], v[50:51]\n"
"v_pk_add_f32 v[164:165], v[164:165], v[50:51]\n"
"v_pk_add_f32 v[166:167], v[166:167], v[50:51]\n"
"v_rcp_f32 v148, v148\n"
"v_rcp_f32 v149, v149\n"
"v_rcp_f32 v150, v150\n"
"v_rcp_f32 v151, v151\n"
"v_rcp_f32 v152, v152\n"
"v_rcp_f32 v153, v153\n"
"v_rcp_f32 v154, v154\n"
"v_rcp_f32 v155, v155\n"
"v_rcp_f32 v156, v156\n"
"v_rcp_f32 v157, v157\n"
"v_rcp_f32 v158, v158\n"
"v_rcp_f32 v159, v159\n"
"v_rcp_f32 v160, v160\n"
"v_rcp_f32 v161, v161\n"
"v_rcp_f32 v162, v162\n"
"v_rcp_f32 v163, v163\n"
"v_rcp_f32 v164, v164\n"
"v_rcp_f32 v165, v165\n"
"v_rcp_f32 v166, v166\n"
"v_rcp_f32 v167, v167\n"
"v_pk_mul_f32 v[148:149], v[148:149], v[128:129]\n"
"v_pk_mul_f32 v[150:151], v[150:151], v[130:131]\n"
"v_pk_mul_f32 v[152:153], v[152:153], v[132:133]\n"
"v_pk_mul_f32 v[154:155], v[154:155], v[134:135]\n"
"v_pk_mul_f32 v[156:157], v[156:157], v[136:137]\n"
"v_pk_mul_f32 v[158:159], v[158:159], v[138:139]\n"
"v_pk_mul_f32 v[160:161], v[160:161], v[140:141]\n"
"v_pk_mul_f32 v[162:163], v[162:163], v[142:143]\n"
"v_pk_mul_f32 v[164:165], v[164:165], v[144:145]\n"
"v_pk_mul_f32 v[166:167], v[166:167], v[146:147]\n"
"s_waitcnt vmcnt(0)\n"
"v_lshlrev_b32 v108, 16, v216\n"
"v_and_b32 v109, 0xffff0000, v216\n"
"v_lshlrev_b32 v110, 16, v217\n"
"v_and_b32 v111, 0xffff0000, v217\n"
"v_lshlrev_b32 v112, 16, v218\n"
"v_and_b32 v113, 0xffff0000, v218\n"
"v_lshlrev_b32 v114, 16, v219\n"
"v_and_b32 v115, 0xffff0000, v219\n"
"v_lshlrev_b32 v116, 16, v220\n"
"v_and_b32 v117, 0xffff0000, v220\n"
"v_lshlrev_b32 v118, 16, v221\n"
"v_and_b32 v119, 0xffff0000, v221\n"
"v_lshlrev_b32 v120, 16, v222\n"
"v_and_b32 v121, 0xffff0000, v222\n"
"v_lshlrev_b32 v122, 16, v223\n"
"v_and_b32 v123, 0xffff0000, v223\n"
"v_lshlrev_b32 v124, 16, v224\n"
"v_and_b32 v125, 0xffff0000, v224\n"
"v_lshlrev_b32 v126, 16, v225\n"
"v_and_b32 v127, 0xffff0000, v225\n"
"v_lshlrev_b32 v70, 16, v226\n"
"v_and_b32 v71, 0xffff0000, v226\n"
"v_pk_add_f32 v[108:109], v[108:109], v[70:71]\n"
"v_lshlrev_b32 v72, 16, v227\n"
"v_and_b32 v73, 0xffff0000, v227\n"
"v_pk_add_f32 v[110:111], v[110:111], v[72:73]\n"
"v_lshlrev_b32 v70, 16, v228\n"
"v_and_b32 v71, 0xffff0000, v228\n"
"v_pk_add_f32 v[112:113], v[112:113], v[70:71]\n"
"v_lshlrev_b32 v72, 16, v229\n"
"v_and_b32 v73, 0xffff0000, v229\n"
"v_pk_add_f32 v[114:115], v[114:115], v[72:73]\n"
"v_lshlrev_b32 v70, 16, v230\n"
"v_and_b32 v71, 0xffff0000, v230\n"
"v_pk_add_f32 v[116:117], v[116:117], v[70:71]\n"
"v_lshlrev_b32 v72, 16, v231\n"
"v_and_b32 v73, 0xffff0000, v231\n"
"v_pk_add_f32 v[118:119], v[118:119], v[72:73]\n"
"v_lshlrev_b32 v70, 16, v232\n"
"v_and_b32 v71, 0xffff0000, v232\n"
"v_pk_add_f32 v[120:121], v[120:121], v[70:71]\n"
"v_lshlrev_b32 v72, 16, v233\n"
"v_and_b32 v73, 0xffff0000, v233\n"
"v_pk_add_f32 v[122:123], v[122:123], v[72:73]\n"
"v_lshlrev_b32 v70, 16, v234\n"
"v_and_b32 v71, 0xffff0000, v234\n"
"v_pk_add_f32 v[124:125], v[124:125], v[70:71]\n"
"v_lshlrev_b32 v72, 16, v235\n"
"v_and_b32 v73, 0xffff0000, v235\n"
"v_pk_add_f32 v[126:127], v[126:127], v[72:73]\n"
"v_lshlrev_b32 v70, 16, v236\n"
"v_and_b32 v71, 0xffff0000, v236\n"
"v_pk_add_f32 v[108:109], v[108:109], v[70:71]\n"
"v_lshlrev_b32 v72, 16, v237\n"
"v_and_b32 v73, 0xffff0000, v237\n"
"v_pk_add_f32 v[110:111], v[110:111], v[72:73]\n"
"v_lshlrev_b32 v70, 16, v238\n"
"v_and_b32 v71, 0xffff0000, v238\n"
"v_pk_add_f32 v[112:113], v[112:113], v[70:71]\n"
"v_lshlrev_b32 v72, 16, v239\n"
"v_and_b32 v73, 0xffff0000, v239\n"
"v_pk_add_f32 v[114:115], v[114:115], v[72:73]\n"
"v_lshlrev_b32 v70, 16, v240\n"
"v_and_b32 v71, 0xffff0000, v240\n"
"v_pk_add_f32 v[116:117], v[116:117], v[70:71]\n"
"v_lshlrev_b32 v72, 16, v241\n"
"v_and_b32 v73, 0xffff0000, v241\n"
"v_pk_add_f32 v[118:119], v[118:119], v[72:73]\n"
"v_lshlrev_b32 v70, 16, v242\n"
"v_and_b32 v71, 0xffff0000, v242\n"
"v_pk_add_f32 v[120:121], v[120:121], v[70:71]\n"
"v_lshlrev_b32 v72, 16, v243\n"
"v_and_b32 v73, 0xffff0000, v243\n"
"v_pk_add_f32 v[122:123], v[122:123], v[72:73]\n"
"v_lshlrev_b32 v70, 16, v244\n"
"v_and_b32 v71, 0xffff0000, v244\n"
"v_pk_add_f32 v[124:125], v[124:125], v[70:71]\n"
"v_lshlrev_b32 v72, 16, v245\n"
"v_and_b32 v73, 0xffff0000, v245\n"
"v_pk_add_f32 v[126:127], v[126:127], v[72:73]\n"
"v_lshlrev_b32 v70, 16, v246\n"
"v_and_b32 v71, 0xffff0000, v246\n"
"v_pk_add_f32 v[108:109], v[108:109], v[70:71]\n"
"v_lshlrev_b32 v72, 16, v247\n"
"v_and_b32 v73, 0xffff0000, v247\n"
"v_pk_add_f32 v[110:111], v[110:111], v[72:73]\n"
"v_lshlrev_b32 v70, 16, v248\n"
"v_and_b32 v71, 0xffff0000, v248\n"
"v_pk_add_f32 v[112:113], v[112:113], v[70:71]\n"
"v_lshlrev_b32 v72, 16, v249\n"
"v_and_b32 v73, 0xffff0000, v249\n"
"v_pk_add_f32 v[114:115], v[114:115], v[72:73]\n"
"v_lshlrev_b32 v70, 16, v250\n"
"v_and_b32 v71, 0xffff0000, v250\n"
"v_pk_add_f32 v[116:117], v[116:117], v[70:71]\n"
"v_lshlrev_b32 v72, 16, v251\n"
"v_and_b32 v73, 0xffff0000, v251\n"
"v_pk_add_f32 v[118:119], v[118:119], v[72:73]\n"
"v_lshlrev_b32 v70, 16, v252\n"
"v_and_b32 v71, 0xffff0000, v252\n"
"v_pk_add_f32 v[120:121], v[120:121], v[70:71]\n"
"v_lshlrev_b32 v72, 16, v253\n"
"v_and_b32 v73, 0xffff0000, v253\n"
"v_pk_add_f32 v[122:123], v[122:123], v[72:73]\n"
"v_lshlrev_b32 v70, 16, v254\n"
"v_and_b32 v71, 0xffff0000, v254\n"
"v_pk_add_f32 v[124:125], v[124:125], v[70:71]\n"
"v_lshlrev_b32 v72, 16, v255\n"
"v_and_b32 v73, 0xffff0000, v255\n"
"v_pk_add_f32 v[126:127], v[126:127], v[72:73]\n"
"v_pk_fma_f32 v[148:149], v[148:149], v[108:109], v[16:17]\n"
"v_pk_fma_f32 v[150:151], v[150:151], v[110:111], v[18:19]\n"
"v_cvt_pk_bf16_f32 v168, v148, v149\n"
"v_cvt_pk_bf16_f32 v169, v150, v151\n"
"v_pk_fma_f32 v[152:153], v[152:153], v[112:113], v[20:21]\n"
"v_pk_fma_f32 v[154:155], v[154:155], v[114:115], v[22:23]\n"
"v_cvt_pk_bf16_f32 v170, v152, v153\n"
"v_cvt_pk_bf16_f32 v171, v154, v155\n"
"v_pk_fma_f32 v[156:157], v[156:157], v[116:117], v[24:25]\n"
"v_pk_fma_f32 v[158:159], v[158:159], v[118:119], v[26:27]\n"
"v_cvt_pk_bf16_f32 v172, v156, v157\n"
"v_cvt_pk_bf16_f32 v173, v158, v159\n"
"v_pk_fma_f32 v[160:161], v[160:161], v[120:121], v[28:29]\n"
"v_pk_fma_f32 v[162:163], v[162:163], v[122:123], v[30:31]\n"
"v_cvt_pk_bf16_f32 v174, v160, v161\n"
"v_cvt_pk_bf16_f32 v175, v162, v163\n"
"v_pk_fma_f32 v[164:165], v[164:165], v[124:125], v[44:45]\n"
"v_pk_fma_f32 v[166:167], v[166:167], v[126:127], v[46:47]\n"
"v_cvt_pk_bf16_f32 v176, v164, v165\n"
"v_cvt_pk_bf16_f32 v177, v166, v167\n"
"s_bitcmp1_b32 s99, 0\n"
"s_cbranch_scc1 L_ODD%=\n"
"L_SPX2%=:\n"
"s_nop 1\n"
"v_permlane32_swap_b32 v168, v170\n"
"v_permlane32_swap_b32 v169, v171\n"
"s_nop 1\n"
"v_permlane16_swap_b32 v168, v170\n"
"v_permlane16_swap_b32 v169, v171\n"
"buffer_store_dwordx4 v[168:171], v52, s[24:27], 0 offen sc1\n"
"v_permlane32_swap_b32 v172, v174\n"
"v_permlane32_swap_b32 v173, v175\n"
"s_nop 1\n"
"v_permlane16_swap_b32 v172, v174\n"
"v_permlane16_swap_b32 v173, v175\n"
"buffer_store_dwordx4 v[172:175], v52, s[24:27], 0 offen offset:64 sc1\n"
"buffer_store_dwordx2 v[176:177], v89, s[24:27], 0 offen offset:128 sc1\n"
"s_branch L_SKP2%=\n"
"L_ODD%=:\n"
"L_SKPX%=:\n"
"v_permlane32_swap_b32 v170, v172\n"
"v_permlane32_swap_b32 v171, v173\n"
"s_nop 0\n"
"v_permlane16_swap_b32 v170, v172\n"
"v_permlane16_swap_b32 v171, v173\n"
"buffer_store_dwordx4 v[170:173], v52, s[24:27], 0 offen offset:32 sc1\n"
"v_permlane32_swap_b32 v174, v176\n"
"v_permlane32_swap_b32 v175, v177\n"
"s_nop 0\n"
"v_permlane16_swap_b32 v174, v176\n"
"v_permlane16_swap_b32 v175, v177\n"
"buffer_store_dwordx4 v[174:177], v52, s[24:27], 0 offen offset:96 sc1\n"
"buffer_store_dwordx2 v[168:169], v89, s[24:27], 0 offen sc1\n"
"L_SKP2%=:\n"
"v_lshrrev_b32 v54, 1, v12\n"
"s_mov_b64 exec, 0xff\n"
"buffer_store_dwordx2 v[56:57], v54, s[92:95], s62 offen sc1\n"
"s_mov_b64 exec, -1\n"
"s_endpgm\n"
"L_END%=:\n"
"L_FB%=:\n"
"s_waitcnt vmcnt(0) lgkmcnt(0)\n"
"s_barrier\n"
"s_mov_b32 s30, 1\n"
"s_mov_b32 s28, 0\n"
"s_mov_b32 s74, 0\n"
"s_mov_b32 s86, 0\n"
"s_mov_b32 s66, 0\n"
"v_subrev_u32 v5, 0x8000, v5\n"
"v_subrev_u32 v6, 0x8000, v6\n"
"v_subrev_u32 v7, 0x8000, v7\n"
"v_subrev_u32 v8, 0x8000, v8\n"
"v_subrev_u32 v9, 0x4000, v9\n"
"v_subrev_u32 v10, 0x4000, v10\n"
"s_add_u32 m0, s29, 0x0\n"
"s_mov_b32 s31, 0x0\n"
"buffer_load_dwordx4 v5, s[8:11], s31 offen lds\n"
"buffer_load_dwordx4 v6, s[8:11], s31 offen offset:2048 lds\n"
"s_add_u32 m0, s29, 0x1000\n"
"s_nop 0\n"
"buffer_load_dwordx4 v7, s[8:11], s31 offen lds\n"
"buffer_load_dwordx4 v8, s[8:11], s31 offen offset:2048 lds\n"
"s_add_u32 m0, s29, 0xc000\n"
"s_mov_b32 s31, 0x0\n"
"buffer_load_dwordx4 v9, s[12:15], s31 offen lds\n"
"buffer_load_dwordx4 v9, s[12:15], s31 offen offset:2048 lds\n"
"s_add_u32 m0, s29, 0xd000\n"
"s_nop 0\n"
"buffer_load_dwordx4 v10, s[12:15], s31 offen lds\n"
"buffer_load_dwordx4 v10, s[12:15], s31 offen offset:2048 lds\n"
"s_add_u32 m0, s29, 0x4000\n"
"s_mov_b32 s31, 0x4000\n"
"buffer_load_dwordx4 v5, s[8:11], s31 offen lds\n"
"buffer_load_dwordx4 v6, s[8:11], s31 offen offset:2048 lds\n"
"s_add_u32 m0, s29, 0x5000\n"
"s_nop 0\n"
"buffer_load_dwordx4 v7, s[8:11], s31 offen lds\n"
"buffer_load_dwordx4 v8, s[8:11], s31 offen offset:2048 lds\n"
"v_add_u32 v5, 0x8000, v5\n"
"v_add_u32 v6, 0x8000, v6\n"
"v_add_u32 v7, 0x8000, v7\n"
"v_add_u32 v8, 0x8000, v8\n"
"v_add_u32 v9, 0x4000, v9\n"
"v_add_u32 v10, 0x4000, v10\n"
"s_mov_b32 s78, s29\n"
"s_add_u32 s79, s29, 0x4000\n"
"s_add_u32 s80, s29, 0x8000\n"
"v_add_u32 v1, s29, v4\n"
"v_lshrrev_b32 v20, 3, v13\n"
"v_bfe_u32 v21, v13, 1, 2\n"
"v_bfe_u32 v22, v13, 4, 1\n"
"v_xor_b32 v21, v21, v22\n"
"v_mul_u32_u24 v20, 3456, v20\n"
"v_and_b32 v22, 1, v13\n"
"v_lshl_add_u32 v20, v22, 4, v20\n"
"v_lshl_add_u32 v44, v21, 5, v20\n"
"v_xor_b32 v21, 2, v21\n"
"v_lshl_add_u32 v22, v21, 5, v20\n"
"v_add_u32 v45, 26624, v22\n"
"v_add_u32 v46, 53248, v44\n"
"v_add_u32 v47, 79872, v22\n"
"s_lshl_b32 s71, s90, 12\n"
"s_add_u32 s60, s71, 118784\n"
"v_lshrrev_b32 v20, 4, v13\n"
"v_lshlrev_b32 v20, 10, v20\n"
"v_bfe_u32 v21, v13, 2, 2\n"
"v_lshl_add_u32 v20, v21, 7, v20\n"
"v_and_b32 v21, 3, v13\n"
"v_lshl_add_u32 v20, v21, 3, v20\n"
"v_add_u32 v20, s60, v20\n"
"v_bfe_u32 v21, v13, 3, 2\n"
"s_add_u32 s60, s90, 0\n"
"s_and_b32 s60, s60, 3\n"
"v_xor_b32 v22, s60, v21\n"
"v_lshl_add_u32 v48, v22, 5, v20\n"
"s_add_u32 s60, s90, 1\n"
"s_and_b32 s60, s60, 3\n"
"v_xor_b32 v22, s60, v21\n"
"v_lshl_add_u32 v49, v22, 5, v20\n"
"s_add_u32 s60, s90, 2\n"
"s_and_b32 s60, s60, 3\n"
"v_xor_b32 v22, s60, v21\n"
"v_lshl_add_u32 v50, v22, 5, v20\n"
"s_add_u32 s60, s90, 3\n"
"s_and_b32 s60, s60, 3\n"
"v_xor_b32 v22, s60, v21\n"
"v_lshl_add_u32 v51, v22, 5, v20\n"
"s_lshl_b32 s60, s97, 7\n"
"s_mul_i32 s61, s90, 1769472\n"
"s_add_u32 s60, s60, s61\n"
"s_cmp_lt_u32 s97, 27\n"
"s_cselect_b32 s61, 1766144, 0\n"
"s_cselect_b32 s62, 81920, 0\n"
"s_add_u32 s16, s46, s60\n"
"s_addc_u32 s17, s47, 0\n"
"s_mov_b32 s18, s61\n"
"s_mov_b32 s19, 0x20000\n"
"s_mul_i32 s60, s96, 327680\n"
"s_mul_i32 s61, s90, 81920\n"
"s_add_u32 s60, s60, s61\n"
"s_add_u32 s60, s60, 0x2c0000\n"
"s_add_u32 s20, s56, s60\n"
"s_addc_u32 s21, s57, 0\n"
"s_mov_b32 s22, s62\n"
"s_mov_b32 s23, 0x20000\n"
"s_mov_b32 s82, 0\n"
"s_mov_b32 s83, 0\n"
"s_add_u32 m0, s71, 0x1d000\n"
"s_nop 0\n"
"buffer_load_dwordx4 v44, s[16:19], s82 offen lds\n"
"buffer_load_dwordx4 v45, s[16:19], s82 offen offset:1024 lds\n"
"buffer_load_dwordx4 v46, s[16:19], s82 offen offset:2048 lds\n"
"buffer_load_dwordx4 v47, s[16:19], s82 offen offset:3072 lds\n"
"buffer_load_dwordx4 v[224:227], v12, s[20:23], s83 offen sc1\n"
"buffer_load_dwordx4 v[228:231], v12, s[20:23], s83 offen offset:1024 sc1\n"
"buffer_load_dwordx4 v[232:235], v12, s[20:23], s83 offen offset:2048 sc1\n"
"buffer_load_dwordx4 v[236:239], v12, s[20:23], s83 offen offset:3072 sc1\n"
"buffer_load_dwordx4 v[240:243], v52, s[20:23], s83 offen sc1\n"
"s_lshl_b32 s60, s97, 7\n"
"s_lshl_b32 s61, s90, 5\n"
"s_add_u32 s60, s60, s61\n"
"s_add_u32 s58, s48, s60\n"
"s_addc_u32 s59, s49, 0\n"
"s_cmp_lt_u32 s97, 27\n"
"s_cselect_b32 s58, s58, s48\n"
"s_cselect_b32 s59, s59, s49\n"
"v_lshrrev_b32 v22, 4, v13\n"
"v_lshlrev_b32 v22, 3, v22\n"
"global_load_dwordx2 v[22:23], v22, s[58:59]\n"
"v_lshrrev_b32 v17, 4, v13\n"
"v_and_b32 v16, 15, v13\n"
"v_lshlrev_b32 v16, 8, v16\n"
"v_lshl_add_u32 v16, v17, 4, v16\n"
"v_mov_b32 v36, 0xff800000\n"
"v_mov_b32 v37, 0xff800000\n"
"v_mov_b32 v38, 0xff800000\n"
"v_mov_b32 v39, 0xff800000\n"
"s_mov_b32 s31, 0\n"
"L_FBL%=:\n"
"buffer_load_dwordx4 a[192:195], v16, s[8:11], s31 offen offset:0\n"
"buffer_load_dwordx4 a[196:199], v16, s[8:11], s31 offen offset:64\n"
"buffer_load_dwordx4 a[200:203], v16, s[8:11], s31 offen offset:128\n"
"buffer_load_dwordx4 a[204:207], v16, s[8:11], s31 offen offset:192\n"
"s_waitcnt vmcnt(0)\n"
"v_mfma_f32_16x16x32_bf16 v[144:147], a[192:195], v[64:67], 0\n"
"v_mfma_f32_16x16x32_bf16 v[144:147], a[196:199], v[68:71], v[144:147]\n"
"v_mfma_f32_16x16x32_bf16 v[144:147], a[200:203], v[72:75], v[144:147]\n"
"v_mfma_f32_16x16x32_bf16 v[144:147], a[204:207], v[76:79], v[144:147]\n"
"v_mfma_f32_16x16x32_bf16 v[148:151], a[192:195], v[80:83], 0\n"
"v_mfma_f32_16x16x32_bf16 v[148:151], a[196:199], v[84:87], v[148:151]\n"
"v_mfma_f32_16x16x32_bf16 v[148:151], a[200:203], v[88:91], v[148:151]\n"
"v_mfma_f32_16x16x32_bf16 v[148:151], a[204:207], v[92:95], v[148:151]\n"
"v_mfma_f32_16x16x32_bf16 v[152:155], a[192:195], v[96:99], 0\n"
"v_mfma_f32_16x16x32_bf16 v[152:155], a[196:199], v[100:103], v[152:155]\n"
"v_mfma_f32_16x16x32_bf16 v[152:155], a[200:203], v[104:107], v[152:155]\n"
"v_mfma_f32_16x16x32_bf16 v[152:155], a[204:207], v[108:111], v[152:155]\n"
"v_mfma_f32_16x16x32_bf16 v[156:159], a[192:195], v[112:115], 0\n"
"v_mfma_f32_16x16x32_bf16 v[156:159], a[196:199], v[116:119], v[156:159]\n"
"v_mfma_f32_16x16x32_bf16 v[156:159], a[200:203], v[120:123], v[156:159]\n"
"v_mfma_f32_16x16x32_bf16 v[156:159], a[204:207], v[124:127], v[156:159]\n"
"s_nop 15\n"
"v_max3_f32 v36, v36, v144, v145\n"
"v_max3_f32 v36, v36, v146, v147\n"
"v_max3_f32 v37, v37, v148, v149\n"
"v_max3_f32 v37, v37, v150, v151\n"
"v_max3_f32 v38, v38, v152, v153\n"
"v_max3_f32 v38, v38, v154, v155\n"
"v_max3_f32 v39, v39, v156, v157\n"
"v_max3_f32 v39, v39, v158, v159\n"
"s_add_u32 s31, s31, 0x1000\n"
"s_cmp_lt_u32 s31, 0x80000\n"
"s_cbranch_scc1 L_FBL%=\n"
"v_mov_b32 v40, v36\n"
"v_mov_b32 v41, v37\n"
"v_mov_b32 v42, v38\n"
"v_mov_b32 v43, v39\n"
"v_permlane32_swap_b32 v36, v40\n"
"v_permlane32_swap_b32 v37, v41\n"
"v_permlane32_swap_b32 v38, v42\n"
"v_permlane32_swap_b32 v39, v43\n"
"v_max_f32 v36, v36, v40\n"
"v_max_f32 v37, v37, v41\n"
"v_max_f32 v38, v38, v42\n"
"v_max_f32 v39, v39, v43\n"
"v_mov_b32 v40, v36\n"
"v_mov_b32 v41, v37\n"
"v_mov_b32 v42, v38\n"
"v_mov_b32 v43, v39\n"
"v_permlane16_swap_b32 v36, v40\n"
"v_permlane16_swap_b32 v37, v41\n"
"v_permlane16_swap_b32 v38, v42\n"
"v_permlane16_swap_b32 v39, v43\n"
"v_max_f32 v36, v36, v40\n"
"v_max_f32 v37, v37, v41\n"
"v_max_f32 v38, v38, v42\n"
"v_max_f32 v39, v39, v43\n"
"v_sub_f32 v36, 0x41000000, v36\n"
"v_sub_f32 v37, 0x41000000, v37\n"
"v_sub_f32 v38, 0x41000000, v38\n"
"v_sub_f32 v39, 0x41000000, v39\n"
"v_mov_b32 v128, v36\n"
"v_mov_b32 v129, v36\n"
"v_mov_b32 v130, v36\n"
"v_mov_b32 v131, v36\n"
"v_mov_b32 v132, v37\n"
"v_mov_b32 v133, v37\n"
"v_mov_b32 v134, v37\n"
"v_mov_b32 v135, v37\n"
"v_mov_b32 v136, v38\n"
"v_mov_b32 v137, v38\n"
"v_mov_b32 v138, v38\n"
"v_mov_b32 v139, v38\n"
"v_mov_b32 v140, v39\n"
"v_mov_b32 v141, v39\n"
"v_mov_b32 v142, v39\n"
"v_mov_b32 v143, v39\n"
"v_accvgpr_write_b32 a0, 0\n"
"v_accvgpr_write_b32 a1, 0\n"
"v_accvgpr_write_b32 a2, 0\n"
"v_accvgpr_write_b32 a3, 0\n"
"v_accvgpr_write_b32 a4, 0\n"
"v_accvgpr_write_b32 a5, 0\n"
"v_accvgpr_write_b32 a6, 0\n"
"v_accvgpr_write_b32 a7, 0\n"
"v_accvgpr_write_b32 a8, 0\n"
"v_accvgpr_write_b32 a9, 0\n"
"v_accvgpr_write_b32 a10, 0\n"
"v_accvgpr_write_b32 a11, 0\n"
"v_accvgpr_write_b32 a12, 0\n"
"v_accvgpr_write_b32 a13, 0\n"
"v_accvgpr_write_b32 a14, 0\n"
"v_accvgpr_write_b32 a15, 0\n"
"v_accvgpr_write_b32 a16, 0\n"
"v_accvgpr_write_b32 a17, 0\n"
"v_accvgpr_write_b32 a18, 0\n"
"v_accvgpr_write_b32 a19, 0\n"
"v_accvgpr_write_b32 a20, 0\n"
"v_accvgpr_write_b32 a21, 0\n"
"v_accvgpr_write_b32 a22, 0\n"
"v_accvgpr_write_b32 a23, 0\n"
"v_accvgpr_write_b32 a24, 0\n"
"v_accvgpr_write_b32 a25, 0\n"
"v_accvgpr_write_b32 a26, 0\n"
"v_accvgpr_write_b32 a27, 0\n"
"v_accvgpr_write_b32 a28, 0\n"
"v_accvgpr_write_b32 a29, 0\n"
"v_accvgpr_write_b32 a30, 0\n"
"v_accvgpr_write_b32 a31, 0\n"
"v_accvgpr_write_b32 a32, 0\n"
"v_accvgpr_write_b32 a33, 0\n"
"v_accvgpr_write_b32 a34, 0\n"
"v_accvgpr_write_b32 a35, 0\n"
"v_accvgpr_write_b32 a36, 0\n"
"v_accvgpr_write_b32 a37, 0\n"
"v_accvgpr_write_b32 a38, 0\n"
"v_accvgpr_write_b32 a39, 0\n"
"v_accvgpr_write_b32 a40, 0\n"
"v_accvgpr_write_b32 a41, 0\n"
"v_accvgpr_write_b32 a42, 0\n"
"v_accvgpr_write_b32 a43, 0\n"
"v_accvgpr_write_b32 a44, 0\n"
"v_accvgpr_write_b32 a45, 0\n"
"v_accvgpr_write_b32 a46, 0\n"
"v_accvgpr_write_b32 a47, 0\n"
"v_accvgpr_write_b32 a48, 0\n"
"v_accvgpr_write_b32 a49, 0\n"
"v_accvgpr_write_b32 a50, 0\n"
"v_accvgpr_write_b32 a51, 0\n"
"v_accvgpr_write_b32 a52, 0\n"
"v_accvgpr_write_b32 a53, 0\n"
"v_accvgpr_write_b32 a54, 0\n"
"v_accvgpr_write_b32 a55, 0\n"
"v_accvgpr_write_b32 a56, 0\n"
"v_accvgpr_write_b32 a57, 0\n"
"v_accvgpr_write_b32 a58, 0\n"
"v_accvgpr_write_b32 a59, 0\n"
"v_accvgpr_write_b32 a60, 0\n"
"v_accvgpr_write_b32 a61, 0\n"
"v_accvgpr_write_b32 a62, 0\n"
"v_accvgpr_write_b32 a63, 0\n"
"v_accvgpr_write_b32 a64, 0\n"
"v_accvgpr_write_b32 a65, 0\n"
"v_accvgpr_write_b32 a66, 0\n"
"v_accvgpr_write_b32 a67, 0\n"
"v_accvgpr_write_b32 a68, 0\n"
"v_accvgpr_write_b32 a69, 0\n"
"v_accvgpr_write_b32 a70, 0\n"
"v_accvgpr_write_b32 a71, 0\n"
"v_accvgpr_write_b32 a72, 0\n"
"v_accvgpr_write_b32 a73, 0\n"
"v_accvgpr_write_b32 a74, 0\n"
"v_accvgpr_write_b32 a75, 0\n"
"v_accvgpr_write_b32 a76, 0\n"
"v_accvgpr_write_b32 a77, 0\n"
"v_accvgpr_write_b32 a78, 0\n"
"v_accvgpr_write_b32 a79, 0\n"
"v_accvgpr_write_b32 a80, 0\n"
"v_accvgpr_write_b32 a81, 0\n"
"v_accvgpr_write_b32 a82, 0\n"
"v_accvgpr_write_b32 a83, 0\n"
"v_accvgpr_write_b32 a84, 0\n"
"v_accvgpr_write_b32 a85, 0\n"
"v_accvgpr_write_b32 a86, 0\n"
"v_accvgpr_write_b32 a87, 0\n"
"v_accvgpr_write_b32 a88, 0\n"
"v_accvgpr_write_b32 a89, 0\n"
"v_accvgpr_write_b32 a90, 0\n"
"v_accvgpr_write_b32 a91, 0\n"
"v_accvgpr_write_b32 a92, 0\n"
"v_accvgpr_write_b32 a93, 0\n"
"v_accvgpr_write_b32 a94, 0\n"
"v_accvgpr_write_b32 a95, 0\n"
"v_accvgpr_write_b32 a96, 0\n"
"v_accvgpr_write_b32 a97, 0\n"
"v_accvgpr_write_b32 a98, 0\n"
"v_accvgpr_write_b32 a99, 0\n"
"v_accvgpr_write_b32 a100, 0\n"
"v_accvgpr_write_b32 a101, 0\n"
"v_accvgpr_write_b32 a102, 0\n"
"v_accvgpr_write_b32 a103, 0\n"
"v_accvgpr_write_b32 a104, 0\n"
"v_accvgpr_write_b32 a105, 0\n"
"v_accvgpr_write_b32 a106, 0\n"
"v_accvgpr_write_b32 a107, 0\n"
"v_accvgpr_write_b32 a108, 0\n"
"v_accvgpr_write_b32 a109, 0\n"
"v_accvgpr_write_b32 a110, 0\n"
"v_accvgpr_write_b32 a111, 0\n"
"v_accvgpr_write_b32 a112, 0\n"
"v_accvgpr_write_b32 a113, 0\n"
"v_accvgpr_write_b32 a114, 0\n"
"v_accvgpr_write_b32 a115, 0\n"
"v_accvgpr_write_b32 a116, 0\n"
"v_accvgpr_write_b32 a117, 0\n"
"v_accvgpr_write_b32 a118, 0\n"
"v_accvgpr_write_b32 a119, 0\n"
"v_accvgpr_write_b32 a120, 0\n"
"v_accvgpr_write_b32 a121, 0\n"
"v_accvgpr_write_b32 a122, 0\n"
"v_accvgpr_write_b32 a123, 0\n"
"v_accvgpr_write_b32 a124, 0\n"
"v_accvgpr_write_b32 a125, 0\n"
"v_accvgpr_write_b32 a126, 0\n"
"v_accvgpr_write_b32 a127, 0\n"
"v_mov_b32 v32, 0\n"
"v_mov_b32 v33, 0\n"
"v_mov_b32 v34, 0\n"
"v_mov_b32 v35, 0\n"
"s_barrier\n"
"ds_read_b128 a[192:195], v1 offset:0\n"
"ds_read_b128 a[196:199], v1 offset:512\n"
"ds_read_b128 a[200:203], v1 offset:1024\n"
"ds_read_b128 a[204:207], v1 offset:1536\n"
"s_waitcnt lgkmcnt(0)\n"
"v_mfma_f32_16x16x32_bf16 v[144:147], a[192:195], v[64:67], v[128:131]\n"
"v_mfma_f32_16x16x32_bf16 v[144:147], a[196:199], v[68:71], v[144:147]\n"
"v_mfma_f32_16x16x32_bf16 v[144:147], a[200:203], v[72:75], v[144:147]\n"
"v_mfma_f32_16x16x32_bf16 v[144:147], a[204:207], v[76:79], v[144:147]\n"
"v_mfma_f32_16x16x32_bf16 v[148:151], a[192:195], v[80:83], v[132:135]\n"
"v_mfma_f32_16x16x32_bf16 v[148:151], a[196:199], v[84:87], v[148:151]\n"
"v_mfma_f32_16x16x32_bf16 v[148:151], a[200:203], v[88:91], v[148:151]\n"
"v_mfma_f32_16x16x32_bf16 v[148:151], a[204:207], v[92:95], v[148:151]\n"
"v_mfma_f32_16x16x32_bf16 v[152:155], a[192:195], v[96:99], v[136:139]\n"
"v_mfma_f32_16x16x32_bf16 v[152:155], a[196:199], v[100:103], v[152:155]\n"
"v_mfma_f32_16x16x32_bf16 v[152:155], a[200:203], v[104:107], v[152:155]\n"
"v_mfma_f32_16x16x32_bf16 v[152:155], a[204:207], v[108:111], v[152:155]\n"
"v_mfma_f32_16x16x32_bf16 v[156:159], a[192:195], v[112:115], v[140:143]\n"
"v_mfma_f32_16x16x32_bf16 v[156:159], a[196:199], v[116:119], v[156:159]\n"
"v_mfma_f32_16x16x32_bf16 v[156:159], a[200:203], v[120:123], v[156:159]\n"
"v_mfma_f32_16x16x32_bf16 v[156:159], a[204:207], v[124:127], v[156:159]\n"
"s_nop 15\n"
"s_branch L_JOIN%=\n"
  :: "s"(__builtin_amdgcn_kernarg_segment_ptr()), "s"(blockIdx.x), "v"(threadIdx.x), "s"((unsigned)(size_t)smem)
  : "memory", "vcc", "v1", "v2", "v3", "v4", "v5", "v6", "v7", "v8", "v9", "v10", "v11", "v12", "v13", "v14", "v15", "v16", "v17", "v18", "v19", "v20", "v21", "v22", "v23", "v24", "v25", "v26", "v27", "v28", "v29", "v30", "v31", "v32", "v33", "v34", "v35", "v36", "v37", "v38", "v39", "v40", "v41", "v42", "v43", "v44", "v45", "v46", "v47", "v48", "v49", "v50", "v51", "v52", "v53", "v54", "v55", "v56", "v57", "v58", "v59", "v60", "v61", "v62", "v63", "v64", "v65", "v66", "v67", "v68", "v69", "v70", "v71", "v72", "v73", "v74", "v75", "v76", "v77", "v78", "v79", "v80", "v81", "v82", "v83", "v84", "v85", "v86", "v87", "v88", "v89", "v90", "v91", "v92", "v93", "v94", "v95", "v96", "v97", "v98", "v99", "v100", "v101", "v102", "v103", "v104", "v105", "v106", "v107", "v108", "v109", "v110", "v111", "v112", "v113", "v114", "v115", "v116", "v117", "v118", "v119", "v120", "v121", "v122", "v123", "v124", "v125", "v126", "v127", "v128", "v129", "v130", "v131", "v132", "v133", "v134", "v135", "v136", "v137", "v138", "v139", "v140", "v141", "v142", "v143", "v144", "v145", "v146", "v147", "v148", "v149", "v150", "v151", "v152", "v153", "v154", "v155", "v156", "v157", "v158", "v159", "v160", "v161", "v162", "v163", "v164", "v165", "v166", "v167", "v168", "v169", "v170", "v171", "v172", "v173", "v174", "v175", "v176", "v177", "v178", "v179", "v180", "v181", "v182", "v183", "v184", "v185", "v186", "v187", "v188", "v189", "v190", "v191", "v192", "v193", "v194", "v195", "v196", "v197", "v198", "v199", "v200", "v201", "v202", "v203", "v204", "v205", "v206", "v207", "v208", "v209", "v210", "v211", "v212", "v213", "v214", "v215", "v216", "v217", "v218", "v219", "v220", "v221", "v222", "v223", "v224", "v225", "v226", "v227", "v228", "v229", "v230", "v231", "v232", "v233", "v234", "v235", "v236", "v237", "v238", "v239", "v240", "v241", "v242", "v243", "v244", "v245", "v246", "v247", "v248", "v249", "v250", "v251", "v252", "v253", "v254", "v255", "a0", "a1", "a2", "a3", "a4", "a5", "a6", "a7", "a8", "a9", "a10", "a11", "a12", "a13", "a14", "a15", "a16", "a17", "a18", "a19", "a20", "a21", "a22", "a23", "a24", "a25", "a26", "a27", "a28", "a29", "a30", "a31", "a32", "a33", "a34", "a35", "a36", "a37", "a38", "a39", "a40", "a41", "a42", "a43", "a44", "a45", "a46", "a47", "a48", "a49", "a50", "a51", "a52", "a53", "a54", "a55", "a56", "a57", "a58", "a59", "a60", "a61", "a62", "a63", "a64", "a65", "a66", "a67", "a68", "a69", "a70", "a71", "a72", "a73", "a74", "a75", "a76", "a77", "a78", "a79", "a80", "a81", "a82", "a83", "a84", "a85", "a86", "a87", "a88", "a89", "a90", "a91", "a92", "a93", "a94", "a95", "a96", "a97", "a98", "a99", "a100", "a101", "a102", "a103", "a104", "a105", "a106", "a107", "a108", "a109", "a110", "a111", "a112", "a113", "a114", "a115", "a116", "a117", "a118", "a119", "a120", "a121", "a122", "a123", "a124", "a125", "a126", "a127", "a128", "a129", "a130", "a131", "a132", "a133", "a134", "a135", "a136", "a137", "a138", "a139", "a140", "a141", "a142", "a143", "a144", "a145", "a146", "a147", "a148", "a149", "a150", "a151", "a152", "a153", "a154", "a155", "a156", "a157", "a158", "a159", "a160", "a161", "a162", "a163", "a164", "a165", "a166", "a167", "a168", "a169", "a170", "a171", "a172", "a173", "a174", "a175", "a176", "a177", "a178", "a179", "a180", "a181", "a182", "a183", "a184", "a185", "a186", "a187", "a188", "a189", "a190", "a191", "a192", "a193", "a194", "a195", "a196", "a197", "a198", "a199", "a200", "a201", "a202", "a203", "a204", "a205", "a206", "a207", "a208", "a209", "a210", "a211", "a212", "a213", "a214", "a215", "a216", "a217", "a218", "a219", "a220", "a221", "a222", "a223", "a224", "a225", "a226", "a227", "a228", "a229", "a230", "a231", "a232", "a233", "a234", "a235", "a236", "a237", "a238", "a239", "a240", "a241", "a242", "a243", "a244", "a245", "a246", "a247", "a248", "a249", "a250", "a251", "a252", "a253", "a254", "a255", "s8", "s9", "s10", "s11", "s12", "s13", "s14", "s15", "s16", "s17", "s18", "s19", "s20", "s21", "s22", "s23", "s24", "s25", "s26", "s27", "s28", "s29", "s30", "s31", "s33", "s34", "s35", "s36", "s37", "s38", "s39", "s40", "s41", "s42", "s43", "s44", "s45", "s46", "s47", "s48", "s49", "s50", "s51", "s52", "s53", "s54", "s55", "s56", "s57", "s58", "s59", "s60", "s61", "s62", "s63", "s64", "s65", "s66", "s67", "s68", "s69", "s70", "s71", "s72", "s73", "s74", "s75", "s76", "s77", "s78", "s79", "s80", "s81", "s82", "s83", "s84", "s85", "s86", "s87", "s88", "s89", "s90", "s91", "s92", "s93", "s94", "s95", "s96", "s97", "s98", "s99");
}

extern "C" void kernel_launch(void* const* d_in, const int* in_sizes, int n_in,
                              void* d_out, int out_size, void* d_ws, size_t ws_size,
                              hipStream_t stream) {
  hipLaunchKernelGGL(fused_block_kernel, dim3(256), dim3(256), 0, stream,
                     (const void*)d_in[0], (const void*)d_in[1], (const void*)d_in[2], (const void*)d_in[3], (const void*)d_in[4],
                     (const void*)d_in[6], (const void*)d_in[7], (const void*)d_in[8], (const void*)d_in[9], d_out, d_ws);
}
```
